```python
import torch

def _softmax(x, axis=-1):
    return torch.softmax(x, dim=axis)
def _gelu(x):
    return torch.nn.functional.gelu(x, approximate="tanh")
def _silu(x):
    return torch.nn.functional.silu(x)
def _sigmoid(x):
    return torch.sigmoid(x)
def _layernorm(x, axis=-1, eps=1e-5):
    m = x.mean(dim=axis, keepdim=True)
    v = x.var(dim=axis, keepdim=True, unbiased=False)
    return (x - m) / torch.sqrt(v + eps)
def _scatter_add(tgt, idx, src, axis=0):
    return tgt.clone().index_add_(axis, idx, src)
def _segment_sum(src, seg, n):
    out = torch.zeros((n,) + src.shape[1:], dtype=src.dtype)
    return out.index_add_(0, seg, src)
def _pad(x, pw):
    flat = tuple(v for pair in reversed(pw) for v in pair)
    return torch.nn.functional.pad(x, flat)

kernel_name = 'parallel_sum__mha_block__swiglu_ffn__prompt'

def setup_inputs():

    _w = (lambda a: (lambda t: t.to(torch.bfloat16) if t.is_floating_point() else t)(torch.as_tensor(a)))
    def _A_setup():
        B, S, D, H = 1, 2048, 512, 32
        return {
            "x": _w((lambda *s: torch.randn(*s, dtype=torch.bfloat16) * 0.05)(B, S, D)),
            "wqkv": _w((lambda *s: torch.randn(*s, dtype=torch.bfloat16) * 0.2)(D, 3 * D)),
            "wo": _w((lambda *s: torch.randn(*s, dtype=torch.bfloat16) * 0.1)(D, D)),
        }
    _A_in = _A_setup()

    def _B_setup():
        B, S, D, F = 1, 2048, 512, 1792
        return {
            "x": _w((lambda *s: torch.randn(*s, dtype=torch.bfloat16) * 0.05)(B, S, D)),
            "w_gate": _w((lambda *s: torch.randn(*s, dtype=torch.bfloat16) * 0.1)(D, F)),
            "w_up": _w((lambda *s: torch.randn(*s, dtype=torch.bfloat16) * 0.1)(D, F)),
            "w_down": _w((lambda *s: torch.randn(*s, dtype=torch.bfloat16) * 0.1)(F, D)),
        }
    _B_in = _B_setup()

    return {
        "A_x": _A_in["x"],
        "A_wqkv": _A_in["wqkv"],
        "A_wo": _A_in["wo"],
        "B_x": _B_in["x"],
        "B_w_gate": _B_in["w_gate"],
        "B_w_up": _B_in["w_up"],
        "B_w_down": _B_in["w_down"],
    }

def reference(A_x, A_wqkv, A_wo, B_x, B_w_gate, B_w_up, B_w_down):
    def _A_ref(x, wqkv, wo):
        B, S, D = x.shape
        H = 32
        Dh = D // H
        qkv = (lambda x, *d: x.permute(*d))((x @ wqkv).reshape(B, S, 3, H, Dh), 2, 0, 3, 1, 4)
        q, k, v = qkv[0], qkv[1], qkv[2]
        scores = torch.einsum("bhqd,bhkd->bhqk", q, k) * (Dh ** -0.5)
        p = _softmax(scores, axis=-1)
        o = torch.einsum("bhqk,bhkd->bhqd", p, v)
        return (lambda x, *d: x.permute(*d))(o, 0, 2, 1, 3).reshape(B, S, D) @ wo

    def _B_ref(x, w_gate, w_up, w_down):
        g = _silu(x @ w_gate)
        u = x @ w_up
        return (g * u) @ w_down

    x = A_x
    xn = _layernorm(x, axis=-1)
    return x + _A_ref(xn, A_wqkv, A_wo) + _B_ref(xn, B_w_gate, B_w_up, B_w_down)
```

```cpp
#include <hip/hip_runtime.h>
#include <hip/hip_bf16.h>
#include <stdint.h>

typedef __attribute__((ext_vector_type(8))) short bf16x8;
typedef __attribute__((ext_vector_type(4))) short s16x4;
typedef __attribute__((ext_vector_type(16))) float f32x16;
typedef __attribute__((ext_vector_type(4))) float f32x4;
typedef __attribute__((ext_vector_type(2))) float f32x2;
typedef __attribute__((ext_vector_type(4))) unsigned u32x4;
typedef __attribute__((ext_vector_type(2))) __bf16 bf2;
typedef __amdgpu_buffer_rsrc_t rsrc_t;
#define LAS __attribute__((address_space(3)))
#define GAS __attribute__((address_space(1)))
typedef LAS char* lptr;

#define MFMA(a, b, c) __builtin_amdgcn_mfma_f32_32x32x16_bf16((a), (b), (c), 0, 0, 0)
#define RFL(x) __builtin_amdgcn_readfirstlane(x)

constexpr int S_ = 2048, D_ = 512, F_ = 1792;
constexpr size_t WS_FLAG = 0;
constexpr size_t WS_Q    = 8192;
constexpr size_t WS_KV   = WS_Q + 2097152;
constexpr size_t WS_XN   = WS_KV + 4194304;
constexpr size_t WS_H    = WS_XN + 2097152;
constexpr size_t WS_O    = WS_H + 7340032;
constexpr size_t WS_FF   = WS_O + 2097152;
constexpr float QSCALE = 0.25f * 1.4426950408889634f;
constexpr float MBIAS = 60.0f;

__device__ __forceinline__ rsrc_t mk_rsrc(const void* p, unsigned bytes) {
  return __builtin_amdgcn_make_buffer_rsrc((void*)p, (short)0, (int)bytes, 0x00020000);
}
__device__ __forceinline__ void dma16(rsrc_t r, unsigned voff, unsigned soff, unsigned lds) {
  unsigned keep;
  asm volatile("s_mov_b32 %0, m0\n\ts_mov_b32 m0, %1\n\ts_nop 0\n\tbuffer_load_dwordx4 %2, %3, %4 offen lds\n\ts_mov_b32 m0, %0"
               : "=&s"(keep) : "s"(lds), "v"(voff), "s"(r), "s"(soff) : "memory");
}
#define WAIT_BAR(N) asm volatile("s_waitcnt vmcnt(" #N ") lgkmcnt(0)\n\ts_barrier" ::: "memory")
#define BAR() asm volatile("s_waitcnt lgkmcnt(0)\n\ts_barrier" ::: "memory")

__device__ __forceinline__ unsigned pk2(float lo, float hi) {
  f32x2 v = {lo, hi};
  return __builtin_bit_cast(unsigned, __builtin_convertvector(v, bf2));
}
__device__ __forceinline__ float bf_lo(unsigned u) { return __uint_as_float(u << 16); }
__device__ __forceinline__ float bf_hi(unsigned u) { return __uint_as_float(u & 0xffff0000u); }
__device__ __forceinline__ float rbf(float x) { return bf_lo(pk2(x, x)); }
__device__ __forceinline__ float xhalf_sum(float v) {
  auto r = __builtin_amdgcn_permlane32_swap(__float_as_uint(v), __float_as_uint(v), false, false);
  return __uint_as_float(r[0]) + __uint_as_float(r[1]);
}
__device__ __forceinline__ float xhalf_max(float v) {
  auto r = __builtin_amdgcn_permlane32_swap(__float_as_uint(v), __float_as_uint(v), false, false);
  return __builtin_fmaxf(__uint_as_float(r[0]), __uint_as_float(r[1]));
}
__device__ __forceinline__ bf16x8 lds_frag(lptr p) { return *(const LAS bf16x8*)p; }
typedef short v4i16_t __attribute__((ext_vector_type(4)));
__device__ __forceinline__ bf16x8 lds_frag_tr(lptr p0, lptr p1) {
  s16x4 a = __builtin_bit_cast(s16x4, __builtin_amdgcn_ds_read_tr16_b64_v4i16((LAS v4i16_t*)p0));
  s16x4 b = __builtin_bit_cast(s16x4, __builtin_amdgcn_ds_read_tr16_b64_v4i16((LAS v4i16_t*)p1));
  return (bf16x8){a[0], a[1], a[2], a[3], b[0], b[1], b[2], b[3]};
}
__device__ __forceinline__ float dot2(unsigned a, unsigned b, float c) {
  return __builtin_amdgcn_fdot2_f32_bf16(__builtin_bit_cast(bf2, a), __builtin_bit_cast(bf2, b), c, false);
}
__device__ __forceinline__ u32x4 pack8(const f32x16& a, int o) {
  return (u32x4){pk2(a[o], a[o + 1]), pk2(a[o + 2], a[o + 3]), pk2(a[o + 4], a[o + 5]), pk2(a[o + 6], a[o + 7])};
}

constexpr int K1_SLOT = 32768, K1_NSLOT = 4, K1_LDS = K1_SLOT * K1_NSLOT + 4 * 512;

__global__ __launch_bounds__(512, 2) void k_qkv(const char* __restrict__ gx, const char* __restrict__ gw, char* __restrict__ ws) {
  extern __shared__ __attribute__((aligned(16))) char smem[];
  const lptr lds = (lptr)smem;
  const unsigned lds0 = (unsigned)(uintptr_t)lds;
  const int tid = threadIdx.x, lane = tid & 63, wid = RFL(tid >> 6);
  const int b = blockIdx.x, xcd = b & 7, bi = b >> 3;
  const int rb = (xcd >> 1) * 8 + (bi & 7);
  const int cb = (xcd & 1) * 4 + (bi >> 3);
  if (b == 0 && tid < 16) *(unsigned*)(ws + WS_FLAG + tid * 256) = 0u;

  if (wid >= 4) {
    const int lw = wid - 4;
    const rsrc_t rx = mk_rsrc(gx, S_ * D_ * 2), rw = mk_rsrc(gw, D_ * 3 * D_ * 2);
    const unsigned vx0 = (lane >> 3) * 1024 + (((lane & 7) ^ ((0 + (lane >> 4)) & 7)) * 16);
    const unsigned vx1 = (lane >> 3) * 1024 + (((lane & 7) ^ ((4 + (lane >> 4)) & 7)) * 16);
    const unsigned vw = ((lane >> 2) & 7) * 3072 + (lane >> 5) * 64 + (lane & 3) * 16;
    const unsigned sx = (unsigned)(rb * 64 + lw * 16) * 1024;
    const unsigned sw = (unsigned)(lw * 16) * 3072 + cb * 128;
    auto issue = [&](int s) {
      const unsigned slot = lds0 + (s & 3) * K1_SLOT;
      dma16(rx, vx0, sx + s * 128, slot + (2 * lw) * 1024);
      dma16(rx, vx1, sx + 8192 + s * 128, slot + (2 * lw + 1) * 1024);
#pragma unroll
      for (int m = 0; m < 3; ++m) {
        dma16(rw, vw, sw + s * 64 * 3072 + m * 1024, slot + 8192 + m * 8192 + (2 * lw) * 1024);
        dma16(rw, vw, sw + (s * 64 + 8) * 3072 + m * 1024, slot + 8192 + m * 8192 + (2 * lw + 1) * 1024);
      }
    };
    issue(0); issue(1); issue(2); issue(3);
    WAIT_BAR(24);
    WAIT_BAR(16); issue(4);
    WAIT_BAR(16); issue(5);
    WAIT_BAR(16); issue(6);
    WAIT_BAR(16); issue(7);
    WAIT_BAR(16);
    WAIT_BAR(8);
    WAIT_BAR(0);
    return;
  }
  const int rt = wid & 1, ch = wid >> 1, r = lane & 31, h = lane >> 5;
  const int tile = rb * 2 + rt;
  const int kgx = cb * 4 + ch * 2;
  const u32x4 xk0 = *(const u32x4*)(gx + (size_t)(tile * 32 + r) * 1024 + kgx * 32 + h * 16);
  const u32x4 xk1 = *(const u32x4*)(gx + (size_t)(tile * 32 + r) * 1024 + kgx * 32 + 32 + h * 16);
  lptr pa[4];
#pragma unroll
  for (int g = 0; g < 4; ++g) pa[g] = lds + (rt * 4 + (r >> 3)) * 1024 + (r & 7) * 128 + (((2 * g + h) ^ ((r >> 1) & 7)) * 16);
  const lptr pw = lds + 8192 + h * 1024 + ch * 512 + ((lane & 15) >> 2) * 64 + ((lane >> 4) & 1) * 32 + (lane & 3) * 8;
  f32x16 aq = {}, ak = {}, av = {};
  float sum = 0.f, sq = 0.f, cq = 0.f, ck = 0.f, cv = 0.f;
  const unsigned ONE2 = 0x3f803f80u;
#pragma unroll
  for (int s = 0; s < 8; ++s) {
    BAR();
    const int so = (s & 3) * K1_SLOT;
#pragma unroll
    for (int g = 0; g < 4; ++g) {
      const bf16x8 xa = lds_frag(pa[g] + so);
      const bf16x8 wq = lds_frag_tr(pw + so + g * 2048, pw + so + g * 2048 + 256);
      const bf16x8 wk = lds_frag_tr(pw + so + 8192 + g * 2048, pw + so + 8192 + g * 2048 + 256);
      const bf16x8 wv = lds_frag_tr(pw + so + 16384 + g * 2048, pw + so + 16384 + g * 2048 + 256);
      aq = MFMA(wq, xa, aq);
      ak = MFMA(wk, xa, ak);
      av = MFMA(xa, wv, av);
      const u32x4 xu = __builtin_bit_cast(u32x4, xa), qu = __builtin_bit_cast(u32x4, wq), ku = __builtin_bit_cast(u32x4, wk),
                  vu = __builtin_bit_cast(u32x4, wv);
#pragma unroll
      for (int i = 0; i < 4; ++i) {
        sum = dot2(xu[i], ONE2, sum); sq = dot2(xu[i], xu[i], sq);
        cq = dot2(qu[i], ONE2, cq); ck = dot2(ku[i], ONE2, ck); cv = dot2(vu[i], ONE2, cv);
      }
    }
  }
  sum = xhalf_sum(sum); sq = xhalf_sum(sq); cq = xhalf_sum(cq); ck = xhalf_sum(ck); cv = xhalf_sum(cv);
  const float mean = sum * (1.0f / 512.0f);
  const float var = __builtin_fmaxf(sq * (1.0f / 512.0f) - mean * mean, 0.0f);
  const float rstd = 1.0f / __builtin_sqrtf(var + 1e-5f);
  LAS float* sc = (LAS float*)(lds + K1_SLOT * K1_NSLOT + wid * 512);
  if (h == 0) { sc[r] = cq; sc[32 + r] = ck; } else { sc[64 + r] = rstd; sc[96 + r] = mean * rstd; }
  asm volatile("s_waitcnt lgkmcnt(0)" ::: "memory");
  const int pair = cb * 2 + ch;
  {
    const float rq = rstd * QSCALE;
#pragma unroll
    for (int g = 0; g < 4; ++g) {
      const f32x4 c0 = *(const LAS f32x4*)(sc + 8 * g + 4 * h), c1 = *(const LAS f32x4*)(sc + 32 + 8 * g + 4 * h);
      const f32x4 c2 = *(const LAS f32x4*)(sc + 64 + 8 * g + 4 * h), c3 = *(const LAS f32x4*)(sc + 96 + 8 * g + 4 * h);
#pragma unroll
      for (int j = 0; j < 4; ++j) {
        aq[4 * g + j] = (aq[4 * g + j] - mean * c0[j]) * rq;
        ak[4 * g + j] = (ak[4 * g + j] - mean * c1[j]) * rstd;
        av[4 * g + j] = av[4 * g + j] * c2[j] - c3[j] * cv;
      }
    }
  }
  char* q = ws + WS_Q + (size_t)((2 * pair) * 64 + tile) * 1024 + lane * 16;
  *(u32x4*)q = pack8(aq, 0);
  *(u32x4*)(q + 65536) = pack8(aq, 8);
  char* k = ws + WS_KV + (size_t)((2 * pair) * 64 + ((tile + 62) & 63)) * 2048 + lane * 16;
  *(u32x4*)k = pack8(ak, 0);
  *(u32x4*)(k + 131072) = pack8(ak, 8);
  char* v = ws + WS_KV + (size_t)(2 * pair + (r >> 4)) * 131072 + h * 256 + (r & 15) * 16;
  *(u32x4*)(v + ((tile + 63) & 63) * 2048 + 1024) = pack8(av, 0);
  *(u32x4*)(v + tile * 2048 + 1536) = pack8(av, 8);
  {
    const float mr = mean * rstd;
    u32x4 n0, n1;
#pragma unroll
    for (int i = 0; i < 4; ++i) {
      n0[i] = pk2(bf_lo(xk0[i]) * rstd - mr, bf_hi(xk0[i]) * rstd - mr);
      n1[i] = pk2(bf_lo(xk1[i]) * rstd - mr, bf_hi(xk1[i]) * rstd - mr);
    }
    char* xn = ws + WS_XN + (size_t)(tile * 32 + kgx) * 1024 + lane * 16;
    *(u32x4*)xn = n0;
    *(u32x4*)(xn + 1024) = n1;
  }
}

template <int NSTAGE, int NSLOT, int KGROUPS>
__device__ __forceinline__ void gemm64(lptr lds, unsigned lds0, rsrc_t ra, rsrc_t rw, int rb, int cb, int wid, int lane, float (&res)[8]) {
  const int rt = wid & 1, kq = wid >> 1, r = lane & 31, h = lane >> 5;
  const unsigned va = lane * 16;
  const unsigned vw = ((lane >> 2) & 7) * 1024 + (lane >> 5) * 64 + (lane & 3) * 16;
  const unsigned sa = (unsigned)((rb * 2 + rt) * KGROUPS + kq) * 1024;
  const unsigned sw = (unsigned)(wid * 8) * 1024 + cb * 128;
  auto issue = [&](int s, int slot) {
    dma16(ra, va, sa + s * 4096, lds0 + slot * 16384 + wid * 1024);
    dma16(rw, vw, sw + s * 65536, lds0 + slot * 16384 + 8192 + wid * 1024);
  };
  const lptr pa = lds + wid * 1024 + lane * 16;
  const lptr pw = lds + 8192 + kq * 2048 + (4 * h + ((lane & 15) >> 2)) * 64 + ((lane >> 4) & 1) * 32 + (lane & 3) * 8;
  f32x16 a0 = {}, a1 = {};
  constexpr int PRE = (NSLOT >= NSTAGE) ? NSTAGE : NSLOT - 1;
#pragma unroll
  for (int s = 0; s < PRE; ++s) issue(s, s);
  auto compute = [&](int slot) {
    const int so = slot * 16384;
    const bf16x8 xa = lds_frag(pa + so);
    const bf16x8 w0 = lds_frag_tr(pw + so, pw + so + 1024);
    const bf16x8 w1 = lds_frag_tr(pw + so + 512, pw + so + 1536);
    a0 = MFMA(w0, xa, a0);
    a1 = MFMA(w1, xa, a1);
  };
  if constexpr (NSLOT >= NSTAGE) {
    static_assert(NSTAGE == 8, "");
    WAIT_BAR(14); compute(0); WAIT_BAR(12); compute(1); WAIT_BAR(10); compute(2); WAIT_BAR(8); compute(3);
    WAIT_BAR(6); compute(4); WAIT_BAR(4); compute(5); WAIT_BAR(2); compute(6); WAIT_BAR(0); compute(7);
  } else {
    static_assert(NSLOT == 4 && NSTAGE % 4 == 0, "");
    for (int s = 0; s < NSTAGE - 4; s += 4) {
      WAIT_BAR(4); issue(s + 3, 3); compute(0);
      WAIT_BAR(4); issue(s + 4, 0); compute(1);
      WAIT_BAR(4); issue(s + 5, 1); compute(2);
      WAIT_BAR(4); issue(s + 6, 2); compute(3);
    }
    WAIT_BAR(4); issue(NSTAGE - 1, 3); compute(0);
    WAIT_BAR(4); compute(1);
    WAIT_BAR(2); compute(2);
    WAIT_BAR(0); compute(3);
  }
  BAR();
  LAS float* red = (LAS float*)lds + kq * (64 * 68) + (rt * 32 + r) * 68 + 4 * h;
#pragma unroll
  for (int g = 0; g < 4; ++g) {
    *(LAS f32x4*)(red + 8 * g) = (f32x4){a0[4 * g], a0[4 * g + 1], a0[4 * g + 2], a0[4 * g + 3]};
    *(LAS f32x4*)(red + 32 + 8 * g) = (f32x4){a1[4 * g], a1[4 * g + 1], a1[4 * g + 2], a1[4 * g + 3]};
  }
  BAR();
  const int t = wid * 64 + lane;
  const LAS float* rd = (const LAS float*)lds + (t >> 3) * 68 + (t & 7) * 8;
  f32x4 s0 = *(const LAS f32x4*)rd, s1 = *(const LAS f32x4*)(rd + 4);
#pragma unroll
  for (int p = 1; p < 4; ++p) { s0 += *(const LAS f32x4*)(rd + p * 64 * 68); s1 += *(const LAS f32x4*)(rd + p * 64 * 68 + 4); }
#pragma unroll
  for (int j = 0; j < 4; ++j) { res[j] = s0[j]; res[4 + j] = s1[j]; }
}

constexpr int REC = 2064, CHUNK = 8 * REC;

__device__ __forceinline__ void attn_wg(lptr lds, unsigned lds0, char* __restrict__ ws, int b, int wid, int lane) {
  const int xcd = b & 7, bi = b >> 3;
  const int head = xcd * 4 + (bi >> 3), qt = (bi & 7) * 8 + wid;
  const int r = lane & 31, h = lane >> 5;
  const char* kvh = ws + WS_KV + (size_t)head * 131072;
  const bf16x8 ONES = {0x3f80, 0x3f80, 0x3f80, 0x3f80, 0x3f80, 0x3f80, 0x3f80, 0x3f80};
  bf16x8 Q = *(const bf16x8*)(ws + WS_Q + (size_t)(head * 64 + qt) * 1024 + lane * 16);
  const bf16x8 K0 = *(const bf16x8*)(kvh + 62 * 2048 + lane * 16);
  bf16x8 K = *(const bf16x8*)(kvh + 63 * 2048 + lane * 16);
  bf16x8 V0 = *(const bf16x8*)(kvh + 63 * 2048 + 1024 + h * 256 + (r & 15) * 16);
  const rsrc_t rkv = mk_rsrc(kvh, 131072);
  const unsigned voff = lane * 16;
  dma16(rkv, voff, wid * 2048, lds0 + wid * REC);
  dma16(rkv, voff, wid * 2048 + 1024, lds0 + wid * REC + 1024);
  if (wid == 0 && lane < 32) *(LAS bf16x8*)(lds + lane * REC + 2048) = ONES;
  if (r >= 16) V0 = ONES;
  asm volatile("" : "+v"(V0), "+v"(Q), "+v"(K));
  dma16(rkv, voff, 16384 + wid * 2048, lds0 + CHUNK + wid * REC);
  dma16(rkv, voff, 16384 + wid * 2048 + 1024, lds0 + CHUNK + wid * REC + 1024);
  f32x16 SA, SB, C, O = {};
  {
    const f32x16 z = {};
    SA = MFMA(K0, Q, z);
    SB = MFMA(K, Q, z);
  }
  float m = SA[0];
#pragma unroll
  for (int i = 1; i < 16; ++i) m = __builtin_fmaxf(m, SA[i]);
#pragma unroll
  for (int i = 0; i < 16; ++i) m = __builtin_fmaxf(m, SB[i]);
  m = xhalf_max(m) + MBIAS;
#pragma unroll
  for (int i = 0; i < 16; ++i) { C[i] = -m; SA[i] -= m; }
  bf16x8 V1 = {}, P1 = {};
  unsigned aK = lds0 + lane * 16;
  unsigned aV0 = lds0 + (r < 16 ? 1024 + h * 256 + r * 16 : 2048);
  unsigned aV1 = lds0 + (r < 16 ? 1536 + h * 256 + r * 16 : 2048);
  unsigned cnt = 0, soff = 32768 + wid * 2048, mbase = lds0 + wid * REC, t0, t1, keep;
#define EXP(x) "v_exp_f32 v" #x ", v" #x "\n\t"
#define CVT(d, a, b) "v_cvt_pk_bf16_f32 v" #d ", v" #a ", v" #b "\n\t"
#define STEP(c0, c1, c2, c3, c4, c5, c6, c7, c8, c9, c10, c11, c12, c13, c14, c15, SN, off) \
  "s_waitcnt lgkmcnt(1)\n\t" \
  "v_mfma_f32_32x32x16_bf16 " SN ", v[68:71], v[64:67], v[0:15]\n\t" \
  "ds_read_b128 v[68:71], v88 offset:" #off "\n\t" \
  EXP(c0) EXP(c1) EXP(c2) EXP(c3) \
  "v_mfma_f32_32x32x16_bf16 v[48:63], v[76:79], v[84:87], v[48:63]\n\t" \
  "ds_read_b128 v[76:79], v90 offset:" #off "\n\t" \
  EXP(c4) EXP(c5) CVT(80, c0, c1) CVT(81, c2, c3) EXP(c6) EXP(c7) EXP(c8) EXP(c9) CVT(82, c4, c5) CVT(83, c6, c7) \
  EXP(c10) EXP(c11) \
  "s_waitcnt lgkmcnt(2)\n\t" \
  "v_mfma_f32_32x32x16_bf16 v[48:63], v[72:75], v[80:83], v[48:63]\n\t" \
  "ds_read_b128 v[72:75], v89 offset:" #off "\n\t" \
  EXP(c12) EXP(c13) CVT(84, c8, c9) CVT(85, c10, c11) EXP(c14) EXP(c15) CVT(86, c12, c13) CVT(87, c14, c15)
#define STEP_A(off) STEP(16, 17, 18, 19, 20, 21, 22, 23, 24, 25, 26, 27, 28, 29, 30, 31, "v[32:47]", off)
#define STEP_B(off) STEP(32, 33, 34, 35, 36, 37, 38, 39, 40, 41, 42, 43, 44, 45, 46, 47, "v[16:31]", off)
  asm volatile(
      "s_mov_b32 %[keep], m0\n\t"
      "s_waitcnt lgkmcnt(0)\n\t"
      "s_nop 4\n\t"
      "L_attn_%=:\n\t"
      "s_waitcnt vmcnt(2)\n\t"
      "s_barrier\n\t"
      "s_add_u32 %[t0], %[cnt], 2\n\t"
      "s_and_b32 %[t0], %[t0], 3\n\t"
      "s_mul_i32 %[t0], %[t0], 16512\n\t"
      "s_add_u32 m0, %[t0], %[mbase]\n\t"
      "s_and_b32 %[t1], %[soff], 0x1ffff\n\t"
      "s_add_u32 %[soff], %[soff], 16384\n\t"
      "buffer_load_dwordx4 v91, %[rkv], %[t1] offen lds\n\t"
      "buffer_load_dwordx4 v91, %[rkv], %[t1] offen offset:1024 lds\n\t"
      STEP_A(0) STEP_B(2064) STEP_A(4128) STEP_B(6192) STEP_A(8256) STEP_B(10320) STEP_A(12384) STEP_B(14448)
      "s_add_u32 %[cnt], %[cnt], 1\n\t"
      "s_and_b32 %[t0], %[cnt], 3\n\t"
      "s_cmp_eq_u32 %[t0], 0\n\t"
      "s_movk_i32 %[t1], 16512\n\t"
      "s_cselect_b32 %[t0], 0xffff3e80, %[t1]\n\t"
      "v_add_u32 v88, %[t0], v88\n\t"
      "v_add_u32 v89, %[t0], v89\n\t"
      "v_add_u32 v90, %[t0], v90\n\t"
      "s_cmp_lt_u32 %[cnt], 8\n\t"
      "s_cbranch_scc1 L_attn_%=\n\t"
      "s_waitcnt vmcnt(0) lgkmcnt(0)\n\t"
      "v_mfma_f32_32x32x16_bf16 v[48:63], v[76:79], v[84:87], v[48:63]\n\t"
      "s_mov_b32 m0, %[keep]\n\t"
      "s_nop 15\n\t"
      : "+{v[48:63]}"(O), "+{v[16:31]}"(SA), "={v[32:47]}"(SB), "+{v[68:71]}"(K), "+{v[72:75]}"(V0), "+{v[76:79]}"(V1),
        "+{v[84:87]}"(P1), "+{v88}"(aK), "+{v89}"(aV0), "+{v90}"(aV1), [cnt] "+s"(cnt), [soff] "+s"(soff), [t0] "=&s"(t0),
        [t1] "=&s"(t1), [keep] "=&s"(keep)
      : "{v[0:15]}"(C), "{v[64:67]}"(Q), "{v91}"(voff), [rkv] "s"(rkv), [mbase] "s"(mbase)
      : "v80", "v81", "v82", "v83", "scc", "memory");
#undef EXP
#undef CVT
#undef STEP
  float l = O[8];
  float omax = 0.f;
#pragma unroll
  for (int i = 0; i < 8; ++i) omax = __builtin_fmaxf(omax, __builtin_fabsf(O[i]));
  bool bad = !(l < 0x1p120f) || !(xhalf_max(omax) > 0x1p-90f);
#pragma unroll
  for (int i = 0; i < 8; ++i) bad = bad || !(__builtin_fabsf(O[i]) < 0x1p126f);
  if (__builtin_expect(__any(bad), 0)) {
    const f32x16 z = {};
    float mt = -__builtin_inff();
    for (int t = 0; t < 64; ++t) {
      const f32x16 s = MFMA(*(const bf16x8*)(kvh + t * 2048 + lane * 16), Q, z);
#pragma unroll
      for (int i = 0; i < 16; ++i) mt = __builtin_fmaxf(mt, s[i]);
    }
    mt = xhalf_max(mt);
    O = z;
    for (int t = 0; t < 64; ++t) {
      f32x16 s = MFMA(*(const bf16x8*)(kvh + ((t + 62) & 63) * 2048 + lane * 16), Q, z);
      bf16x8 v0 = *(const bf16x8*)(kvh + ((t + 63) & 63) * 2048 + 1024 + h * 256 + (r & 15) * 16);
      bf16x8 v1 = *(const bf16x8*)(kvh + t * 2048 + 1536 + h * 256 + (r & 15) * 16);
      if (r >= 16) { v0 = ONES; v1 = ONES; }
#pragma unroll
      for (int i = 0; i < 16; ++i) s[i] = __builtin_amdgcn_exp2f(s[i] - mt);
      O = MFMA(v0, __builtin_bit_cast(bf16x8, pack8(s, 0)), O);
      O = MFMA(v1, __builtin_bit_cast(bf16x8, pack8(s, 8)), O);
    }
    l = O[8];
  }
  const float il = 1.0f / l;
#pragma unroll
  for (int i = 0; i < 8; ++i) O[i] *= il;
  *(u32x4*)(ws + WS_O + (size_t)(qt * 32 + head) * 1024 + lane * 16) = pack8(O, 0);
}

__device__ __forceinline__ void ffn_wg(lptr lds, unsigned lds0, const char* __restrict__ gwg, const char* __restrict__ gwu,
                                       const char* __restrict__ gwd, char* __restrict__ ws, int b, int wid, int lane) {
  const int xcd = b & 7, bi = b >> 3;
  const rsrc_t rh = mk_rsrc(ws + WS_H, 7340032);
  if (bi < 28) {
    const int rb = (xcd >> 1) * 4 + (bi & 3);
    const int fb = (xcd & 1) * 7 + (bi >> 2);
    const int rh2 = wid & 1, ft = wid >> 1, r = lane & 31, h = lane >> 5;
    const rsrc_t rx = mk_rsrc(ws + WS_XN, 2097152), rg = mk_rsrc(gwg, D_ * F_ * 2), ru = mk_rsrc(gwu, D_ * F_ * 2);
    const unsigned vx = lane * 16;
    const unsigned vw = ((lane >> 2) & 7) * (F_ * 2) + (lane >> 5) * 64 + (lane & 3) * 16;
    const unsigned sx = (unsigned)((rb * 4 + (wid >> 1)) * 32 + (wid & 1)) * 1024;
    const unsigned sw = (unsigned)((wid >> 1) * 8) * (F_ * 2) + fb * 256 + (wid & 1) * 128;
    auto issue = [&](int s, int slot) {
      const unsigned d = lds0 + slot * 24576 + wid * 1024;
      dma16(rx, vx, sx + s * 2048, d);
      dma16(rg, vw, sw + s * 32 * (F_ * 2), d + 8192);
      dma16(ru, vw, sw + s * 32 * (F_ * 2), d + 16384);
    };
    const lptr px = lds + rh2 * 4096 + lane * 16;
    const lptr pw = lds + 8192 + h * 2048 + (ft >> 1) * 1024 + (ft & 1) * 512 + ((lane & 15) >> 2) * 64 + ((lane >> 4) & 1) * 32 + (lane & 3) * 8;
    f32x16 g0 = {}, g1 = {}, u0 = {}, u1 = {};
    auto compute = [&](int slot) {
      const int so = slot * 24576;
#pragma unroll
      for (int kg = 0; kg < 2; ++kg) {
        const bf16x8 x0 = lds_frag(px + so + kg * 1024), x1 = lds_frag(px + so + 2048 + kg * 1024);
        const bf16x8 wg = lds_frag_tr(pw + so + kg * 4096, pw + so + kg * 4096 + 256);
        const bf16x8 wu = lds_frag_tr(pw + so + 8192 + kg * 4096, pw + so + 8192 + kg * 4096 + 256);
        g0 = MFMA(wg, x0, g0); g1 = MFMA(wg, x1, g1);
        u0 = MFMA(wu, x0, u0); u1 = MFMA(wu, x1, u1);
      }
    };
    issue(0, 0); issue(1, 1);
    for (int s = 0; s < 12; s += 3) {
      WAIT_BAR(3); issue(s + 2, 2); compute(0);
      WAIT_BAR(3); issue(s + 3, 0); compute(1);
      WAIT_BAR(3); issue(s + 4, 1); compute(2);
    }
    WAIT_BAR(3); issue(14, 2); compute(0);
    WAIT_BAR(3); issue(15, 0); compute(1);
    WAIT_BAR(3); compute(2);
    WAIT_BAR(0); compute(0);
#pragma unroll
    for (int i = 0; i < 16; ++i) {
      g0[i] = g0[i] * u0[i] * __builtin_amdgcn_rcpf(1.0f + __builtin_amdgcn_exp2f(g0[i] * -1.4426950408889634f));
      g1[i] = g1[i] * u1[i] * __builtin_amdgcn_rcpf(1.0f + __builtin_amdgcn_exp2f(g1[i] * -1.4426950408889634f));
    }
    const unsigned ho = (unsigned)((rb * 4 + rh2 * 2) * 112 + fb * 8 + ft * 2) * 1024 + lane * 16;
    __builtin_amdgcn_raw_buffer_store_b128(pack8(g0, 0), rh, ho, 0, 16);
    __builtin_amdgcn_raw_buffer_store_b128(pack8(g0, 8), rh, ho + 1024, 0, 16);
    __builtin_amdgcn_raw_buffer_store_b128(pack8(g1, 0), rh, ho + 112 * 1024, 0, 16);
    __builtin_amdgcn_raw_buffer_store_b128(pack8(g1, 8), rh, ho + 113 * 1024, 0, 16);
    asm volatile("s_waitcnt vmcnt(0)" ::: "memory");
    BAR();
    if (wid == 0 && lane == 0)
      __hip_atomic_fetch_add((GAS unsigned*)(ws + WS_FLAG + rb * 256), 1u, __ATOMIC_RELAXED, __HIP_MEMORY_SCOPE_AGENT);
  }
  const int rb = (xcd >> 1) * 8 + (bi & 7);
  const int cb = (xcd & 1) * 4 + (bi >> 3);
  if (wid == 0) {
    GAS unsigned* flag = (GAS unsigned*)(ws + WS_FLAG + (rb >> 1) * 256);
    for (unsigned spins = 0; spins < (1u << 22); ++spins) {
      if (RFL(__hip_atomic_load(flag, __ATOMIC_RELAXED, __HIP_MEMORY_SCOPE_AGENT)) >= 14u) break;
      __builtin_amdgcn_s_sleep(8);
    }
    __builtin_amdgcn_fence(__ATOMIC_ACQUIRE, "agent");
    asm volatile("s_waitcnt vmcnt(0)" ::: "memory");
  }
  BAR();
  float res[8];
  gemm64<28, 4, 112>(lds, lds0, rh, mk_rsrc(gwd, F_ * D_ * 2), rb, cb, wid, lane, res);
  const int t = wid * 64 + lane;
  *(u32x4*)(ws + WS_FF + (size_t)(rb * 64 + (t >> 3)) * 1024 + cb * 128 + (t & 7) * 16) =
      (u32x4){pk2(res[0], res[1]), pk2(res[2], res[3]), pk2(res[4], res[5]), pk2(res[6], res[7])};
}

constexpr int K2_LDS = 81920;
__global__ __launch_bounds__(512, 4) void k_mid(const char* __restrict__ gwg, const char* __restrict__ gwu, const char* __restrict__ gwd,
                                                char* __restrict__ ws) {
  extern __shared__ __attribute__((aligned(16))) char smem[];
  const lptr lds = (lptr)smem;
  const unsigned lds0 = (unsigned)(uintptr_t)lds;
  const int lane = threadIdx.x & 63, wid = RFL(threadIdx.x >> 6), b = blockIdx.x;
  if (b < 256) {
    __builtin_amdgcn_s_setprio(1);
    attn_wg(lds, lds0, ws, b, wid, lane);
  } else {
    ffn_wg(lds, lds0, gwg, gwu, gwd, ws, b - 256, wid, lane);
  }
}

constexpr int K3_LDS = 131072;
__global__ __launch_bounds__(512, 2) void k_out(const char* __restrict__ gx, const char* __restrict__ gwo, const char* __restrict__ ws,
                                                char* __restrict__ out) {
  extern __shared__ __attribute__((aligned(16))) char smem[];
  const lptr lds = (lptr)smem;
  const unsigned lds0 = (unsigned)(uintptr_t)lds;
  const int lane = threadIdx.x & 63, wid = RFL(threadIdx.x >> 6), b = blockIdx.x;
  const int xcd = b & 7, bi = b >> 3;
  const int rb = (xcd >> 1) * 8 + (bi & 7), cb = (xcd & 1) * 4 + (bi >> 3);
  float res[8];
  gemm64<8, 8, 32>(lds, lds0, mk_rsrc(ws + WS_O, 2097152), mk_rsrc(gwo, D_ * D_ * 2), rb, cb, wid, lane, res);
  const int t = threadIdx.x;
  const size_t off = (size_t)(rb * 64 + (t >> 3)) * 1024 + cb * 128 + (t & 7) * 16;
  const u32x4 xv = *(const u32x4*)(gx + off), fv = *(const u32x4*)(ws + WS_FF + off);
  u32x4 o;
#pragma unroll
  for (int i = 0; i < 4; ++i)
    o[i] = pk2(bf_lo(xv[i]) + res[2 * i] + bf_lo(fv[i]), bf_hi(xv[i]) + res[2 * i + 1] + bf_hi(fv[i]));
  *(u32x4*)(out + off) = o;
}

extern "C" void kernel_launch(void* const* d_in, const int* in_sizes, int n_in, void* d_out, int out_size, void* d_ws,
                              size_t ws_size, hipStream_t stream) {
  const char* x = (const char*)d_in[0];
  const char* wqkv = (const char*)d_in[1];
  const char* wo = (const char*)d_in[2];
  const char* wg = (const char*)d_in[4];
  const char* wu = (const char*)d_in[5];
  const char* wd = (const char*)d_in[6];
  char* ws = (char*)d_ws;
  (void)hipFuncSetAttribute((const void*)k_qkv, hipFuncAttributeMaxDynamicSharedMemorySize, K1_LDS);
  (void)hipFuncSetAttribute((const void*)k_mid, hipFuncAttributeMaxDynamicSharedMemorySize, K2_LDS);
  (void)hipFuncSetAttribute((const void*)k_out, hipFuncAttributeMaxDynamicSharedMemorySize, K3_LDS);
  k_qkv<<<256, 512, K1_LDS, stream>>>(x, wqkv, ws);
  k_mid<<<512, 512, K2_LDS, stream>>>(wg, wu, wd, ws);
  k_out<<<256, 512, K3_LDS, stream>>>(x, wo, ws, (char*)d_out);
}
```

```cpp
#include <hip/hip_runtime.h>
#include <hip/hip_bf16.h>
#include <stdint.h>

typedef __attribute__((ext_vector_type(8))) short bf16x8;
typedef __attribute__((ext_vector_type(4))) short s16x4;
typedef __attribute__((ext_vector_type(16))) float f32x16;
typedef __attribute__((ext_vector_type(4))) float f32x4;
typedef __attribute__((ext_vector_type(2))) float f32x2;
typedef __attribute__((ext_vector_type(4))) unsigned u32x4;
typedef __attribute__((ext_vector_type(2))) __bf16 bf2;
typedef unsigned long long u64;
typedef __amdgpu_buffer_rsrc_t rsrc_t;
#define LAS __attribute__((address_space(3)))
#define GAS __attribute__((address_space(1)))
typedef LAS char* lptr;

#define MFMA(a, b, c) __builtin_amdgcn_mfma_f32_32x32x16_bf16((a), (b), (c), 0, 0, 0)
#define MM(c, a, b) do { __builtin_amdgcn_sched_barrier(0); c = MFMA(a, b, c); asm volatile("" : "+v"(c) :: "memory"); \
                         __builtin_amdgcn_sched_barrier(0); } while (0)
#define RFL(x) __builtin_amdgcn_readfirstlane(x)

constexpr int S_ = 2048, D_ = 512, F_ = 1792;
constexpr size_t WS_EPOCH = 0;
constexpr size_t WS_FH = 2048;
constexpr size_t WS_FQ = 16384;
constexpr size_t WS_FO = 32768;
constexpr size_t WS_Q    = 65536;
constexpr size_t WS_KV   = WS_Q + 2097152;
constexpr size_t WS_O    = WS_KV + 4194304;
constexpr size_t WS_H    = WS_O + 2097152;
constexpr float QSCALE = 0.25f * 1.4426950408889634f;
constexpr float MBIAS = 80.0f;
constexpr int LDS_BYTES = 163840;
#define POLL_SLEEP 4
#define SPIN_CAP (1u << 16)

#define ST_DECL
#define ST(i)
#define STC(i)
#define TR(i)
#define TRD(i)
#define TR_OUT(wsp)
#define ST_OUT(wsp)
#define ST_ARG
#define ST_PASS

__device__ __forceinline__ rsrc_t mk_rsrc(const void* p, unsigned bytes) {
  return __builtin_amdgcn_make_buffer_rsrc((void*)p, (short)0, (int)bytes, 0x00020000);
}
__device__ __forceinline__ void dma16(rsrc_t r, unsigned voff, unsigned soff, unsigned lds) {
  unsigned keep;
  asm volatile("s_mov_b32 %0, m0\n\ts_mov_b32 m0, %1\n\ts_nop 0\n\tbuffer_load_dwordx4 %2, %3, %4 offen lds\n\ts_mov_b32 m0, %0"
               : "=&s"(keep) : "s"(lds), "v"(voff), "s"(r), "s"(soff) : "memory");
}
__device__ __forceinline__ void dma16_dev(rsrc_t r, unsigned voff, unsigned soff, unsigned lds) {
  unsigned keep;
  asm volatile("s_mov_b32 %0, m0\n\ts_mov_b32 m0, %1\n\ts_nop 0\n\tbuffer_load_dwordx4 %2, %3, %4 offen sc1 lds\n\ts_mov_b32 m0, %0"
               : "=&s"(keep) : "s"(lds), "v"(voff), "s"(r), "s"(soff) : "memory");
}
__device__ __forceinline__ void dma16x3(rsrc_t r, unsigned voff, unsigned soff, unsigned lds) {
  unsigned keep;
  asm volatile("s_mov_b32 %0, m0\n\ts_mov_b32 m0, %1\n\ts_nop 0\n\tbuffer_load_dwordx4 %2, %3, %4 offen lds\n\t"
               "buffer_load_dwordx4 %2, %3, %4 offen offset:1024 lds\n\tbuffer_load_dwordx4 %2, %3, %4 offen offset:2048 lds\n\ts_mov_b32 m0, %0"
               : "=&s"(keep) : "s"(lds), "v"(voff), "s"(r), "s"(soff) : "memory");
}
__device__ __forceinline__ float fma1(float a, float b, float c) {
  float d; asm("v_fma_f32 %0, %1, %2, %3" : "=v"(d) : "v"(a), "v"(b), "v"(c)); return d;
}
#define BLOAD(dst, voff, r, soff) asm volatile("buffer_load_dwordx4 %0, %1, %2, %3 offen" : "=v"(dst) : "v"(voff), "s"(r), "s"(soff) : "memory")
#define BLOAD_DEV(dst, voff, r, soff) asm volatile("buffer_load_dwordx4 %0, %1, %2, %3 offen sc1" : "=v"(dst) : "v"(voff), "s"(r), "s"(soff) : "memory")
#define BSTORE_DEV(val, r, voff) __builtin_amdgcn_raw_buffer_store_b128((val), (r), (voff), 0, 16)
#define WAIT_BAR(N) asm volatile("s_waitcnt vmcnt(" #N ") lgkmcnt(0)\n\ts_barrier" ::: "memory")
#define BAR() asm volatile("s_waitcnt lgkmcnt(0)\n\ts_barrier" ::: "memory")
template <int N> __device__ __forceinline__ void wait_bar() { asm volatile("s_waitcnt vmcnt(%0) lgkmcnt(0)\n\ts_barrier" :: "n"(N) : "memory"); }
template <int N> __device__ __forceinline__ void wait_vm2(u32x4& a, u32x4& b) {
  asm volatile("s_waitcnt vmcnt(%2)" : "+v"(a), "+v"(b) : "n"(N) : "memory");
}
template <int N> __device__ __forceinline__ void wait_vm1(u32x4& a) { asm volatile("s_waitcnt vmcnt(%1)" : "+v"(a) : "n"(N) : "memory"); }

__device__ __forceinline__ unsigned pk2(float lo, float hi) {
  f32x2 v = {lo, hi};
  return __builtin_bit_cast(unsigned, __builtin_convertvector(v, bf2));
}
__device__ __forceinline__ float bf_lo(unsigned u) { return __uint_as_float(u << 16); }
__device__ __forceinline__ float bf_hi(unsigned u) { return __uint_as_float(u & 0xffff0000u); }
__device__ __forceinline__ float xhalf_max(float v) {
  auto r = __builtin_amdgcn_permlane32_swap(__float_as_uint(v), __float_as_uint(v), false, false);
  return __builtin_fmaxf(__uint_as_float(r[0]), __uint_as_float(r[1]));
}
__device__ __forceinline__ bf16x8 lds_frag(lptr p) { return *(const LAS bf16x8*)p; }
typedef short v4i16_t __attribute__((ext_vector_type(4)));
__device__ __forceinline__ bf16x8 lds_frag_tr(lptr p0, lptr p1) {
  s16x4 a = __builtin_bit_cast(s16x4, __builtin_amdgcn_ds_read_tr16_b64_v4i16((LAS v4i16_t*)p0));
  s16x4 b = __builtin_bit_cast(s16x4, __builtin_amdgcn_ds_read_tr16_b64_v4i16((LAS v4i16_t*)p1));
  return (bf16x8){a[0], a[1], a[2], a[3], b[0], b[1], b[2], b[3]};
}
__device__ __forceinline__ float dot2(unsigned a, unsigned b, float c) {
  return __builtin_amdgcn_fdot2_f32_bf16(__builtin_bit_cast(bf2, a), __builtin_bit_cast(bf2, b), c, false);
}
__device__ __forceinline__ u32x4 pack8(const f32x16& a, int o) {
  return (u32x4){pk2(a[o], a[o + 1]), pk2(a[o + 2], a[o + 3]), pk2(a[o + 4], a[o + 5]), pk2(a[o + 6], a[o + 7])};
}
template <int CTRL> __device__ __forceinline__ float dpp_add(float x) {
  return x + __builtin_bit_cast(float, __builtin_amdgcn_mov_dpp(__builtin_bit_cast(int, x), CTRL, 0xf, 0xf, true));
}
__device__ __forceinline__ float sum8(float x) { return dpp_add<0x141>(dpp_add<0x4E>(dpp_add<0xB1>(x))); }

__device__ __forceinline__ u64 read_tag(const char* ws) {
  u64 e;
  asm volatile("s_load_dwordx2 %0, %1, 0x0\n\ts_waitcnt lgkmcnt(0)" : "=s"(e) : "s"(ws + WS_EPOCH) : "memory");
  e = (e ^ (e >> 31)) * 0x9E3779B97F4A7C15ull + 0x632BE59BD9B4E019ull;
  return e ^ (e >> 29);
}
__device__ __forceinline__ void set_flag(char* p, u64 tag) { __hip_atomic_store((GAS u64*)p, tag, __ATOMIC_RELAXED, __HIP_MEMORY_SCOPE_AGENT); }
__device__ __forceinline__ void wait_flags(const char* p, int n, u64 tag, int lane) {
  GAS u64* f = (GAS u64*)p + (lane < n ? lane : 0);
#pragma unroll 1
  for (unsigned spins = 0; spins < SPIN_CAP; ++spins) {
    if (__all(__hip_atomic_load(f, __ATOMIC_RELAXED, __HIP_MEMORY_SCOPE_AGENT) == tag)) break;
    __builtin_amdgcn_s_sleep(POLL_SLEEP);
  }
  asm volatile("" ::: "memory");
}

__device__ __forceinline__ void dma16x2(rsrc_t r, unsigned voff, unsigned soff0, unsigned soff1, unsigned lds) {
  unsigned keep;
  asm volatile("s_mov_b32 %0, m0\n\ts_mov_b32 m0, %1\n\ts_nop 0\n\tbuffer_load_dwordx4 %2, %3, %4 offen lds\n\t"
               "buffer_load_dwordx4 %2, %3, %5 offen offset:1024 lds\n\ts_mov_b32 m0, %0"
               : "=&s"(keep) : "s"(lds), "v"(voff), "s"(r), "s"(soff0), "s"(soff1 - 1024u) : "memory");
}
template <int N> __device__ __forceinline__ void wait_vm() { asm volatile("s_waitcnt vmcnt(%0)" :: "n"(N) : "memory"); }
#define V4(c, g_) (f32x4){c[4 * (g_)], c[4 * (g_) + 1], c[4 * (g_) + 2], c[4 * (g_) + 3]}

constexpr int WR = 65536, QSLOT = 24576;
constexpr int REC = 2080, CHUNK = 8 * REC;

struct F3 { bf16x8 x, a, b; };
struct F4 { bf16x8 x0, x1, w0, w1; };

template <bool QK, int G, class IW, class IG>
__device__ __forceinline__ void qkv_steps(const lptr (&pa)[4], lptr pw, F3& c, F3& n, f32x16& a0, f32x16& a1, IW issue_w, IG issue_gu) {
  if constexpr (G < 32) {
    constexpr int s = G >> 2, N = G + 1, ns = N >> 2;
    if constexpr ((G & 3) == 3) {
      if constexpr (s < 3) wait_bar<7>(); else if constexpr (s < 5) wait_bar<6>(); else if constexpr (s == 5) wait_bar<3>();
      else if constexpr (s == 6) wait_bar<QK ? 0 : 12>(); else BAR();
    }
    const lptr w = pw + (ns & 3) * QSLOT + (N & 3) * 6144;
    if constexpr (QK) {
      MM(a0, c.a, c.x);
      if constexpr (N < 32) { n.x = lds_frag(pa[N & 3] + ns * 4096); n.a = lds_frag_tr(w, w + 256); n.b = lds_frag_tr(w + 1024, w + 1024 + 256); }
      MM(a1, c.b, c.x);
    } else {
      if constexpr (N < 32) { n.x = lds_frag(pa[N & 3] + ns * 4096); n.a = lds_frag_tr(w + 2048, w + 2048 + 256); }
      MM(a0, c.x, c.a);
    }
    if constexpr ((G & 3) == 3) {
      if constexpr (s < 4) issue_w(s + 4);
      if constexpr ((QK && s == 7) || (!QK && s == 5)) issue_gu();
    }
    qkv_steps<QK, G + 1>(pa, pw, n, c, a0, a1, issue_w, issue_gu);
  }
}

#define EXP(x) "v_exp_f32 v" #x ", v" #x "\n\t"
#define CVT(d, a, b) "v_cvt_pk_bf16_f32 v" #d ", v" #a ", v" #b "\n\t"
#define STEP(c0, c1, c2, c3, c4, c5, c6, c7, c8, c9, c10, c11, c12, c13, c14, c15, SN, off) \
  "s_waitcnt lgkmcnt(1)\n\t" \
  "v_mfma_f32_32x32x16_bf16 " SN ", v[68:71], v[64:67], v[0:15]\n\t" \
  "ds_read_b128 v[68:71], v88 offset:" #off "\n\t" \
  EXP(c0) EXP(c1) EXP(c2) EXP(c3) \
  "v_mfma_f32_32x32x16_bf16 v[48:63], v[76:79], v[84:87], v[48:63]\n\t" \
  "ds_read_b128 v[76:79], v90 offset:" #off "\n\t" \
  EXP(c4) EXP(c5) CVT(80, c0, c1) CVT(81, c2, c3) EXP(c6) EXP(c7) EXP(c8) EXP(c9) CVT(82, c4, c5) CVT(83, c6, c7) \
  EXP(c10) EXP(c11) \
  "s_waitcnt lgkmcnt(2)\n\t" \
  "v_mfma_f32_32x32x16_bf16 v[48:63], v[72:75], v[80:83], v[48:63]\n\t" \
  "ds_read_b128 v[72:75], v89 offset:" #off "\n\t" \
  EXP(c12) EXP(c13) CVT(84, c8, c9) CVT(85, c10, c11) EXP(c14) EXP(c15) CVT(86, c12, c13) CVT(87, c14, c15)
#define STEP_A(off) STEP(16, 17, 18, 19, 20, 21, 22, 23, 24, 25, 26, 27, 28, 29, 30, 31, "v[32:47]", off)
#define STEP_B(off) STEP(32, 33, 34, 35, 36, 37, 38, 39, 40, 41, 42, 43, 44, 45, 46, 47, "v[16:31]", off)
#define CHUNK_STEPS STEP_A(0) STEP_B(2080) STEP_A(4160) STEP_B(6240) STEP_A(8320) STEP_B(10400) STEP_A(12480) STEP_B(14560)
#define CHUNK_NEXT \
      "s_add_u32 %[cnt], %[cnt], 1\n\t" \
      "s_and_b32 %[t0], %[cnt], 3\n\t" \
      "s_cmp_eq_u32 %[t0], 0\n\t" \
      "s_movk_i32 %[t1], 16640\n\t" \
      "s_cselect_b32 %[t0], 0xffff3d00, %[t1]\n\t" \
      "v_add_u32 v88, %[t0], v88\n\t" \
      "v_add_u32 v89, %[t0], v89\n\t" \
      "v_add_u32 v90, %[t0], v90\n\t"
#define ATT_STATE "+{v[48:63]}"(O), "+{v[16:31]}"(SA), "+{v[32:47]}"(SB), "+{v[68:71]}"(K), "+{v[72:75]}"(V0), "+{v[76:79]}"(V1), \
                  "+{v[84:87]}"(P1), "+{v88}"(aK), "+{v89}"(aV0), "+{v90}"(aV1), [cnt] "+s"(cnt), [t0] "=&s"(t0), [t1] "=&s"(t1)

__device__ __forceinline__ void sum4(lptr lds, int t, f32x4& s0, f32x4& s1) {
  const LAS float* rd = (const LAS float*)lds + (t >> 3) * 68 + (t & 7) * 8;
#pragma unroll
  for (int p = 0; p < 4; ++p) { s0 += *(const LAS f32x4*)(rd + p * 4352); s1 += *(const LAS f32x4*)(rd + p * 4352 + 4); }
}

__global__ __launch_bounds__(512, 2) void k_all(const char* __restrict__ gx, const char* __restrict__ gwqkv, const char* __restrict__ gwo,
                                                const char* __restrict__ gwg, const char* __restrict__ gwu, const char* __restrict__ gwd,
                                                char* __restrict__ ws, char* __restrict__ out) {
  extern __shared__ __attribute__((aligned(16))) char smem[];
  const lptr lds = (lptr)smem;
  const unsigned lds0 = (unsigned)(uintptr_t)lds;
  const int lane = threadIdx.x & 63, wid = RFL(threadIdx.x >> 6), b = blockIdx.x;
  const int xcd = b & 7, bi = b >> 3;
  const int rb = (xcd >> 1) * 8 + (bi & 7);
  const int cb = (xcd & 1) * 4 + (bi >> 3);
  const int rot = bi & 7;
  const int r = lane & 31, h = lane >> 5;
  const unsigned trn = ((lane & 15) >> 2) * 64 + ((lane >> 4) & 1) * 32 + (lane & 3) * 8;
  const unsigned voff = lane * 16;
  ST_DECL; ST(0);

  const rsrc_t rx = mk_rsrc(gx, S_ * D_ * 2), rw = mk_rsrc(gwqkv, D_ * 3 * D_ * 2);
  u32x4 xr[8];
  {
    const unsigned vx = (lane >> 3) * 1024 + (lane & 7) * 16, sx = (unsigned)(rb * 64 + wid * 8) * 1024;
#pragma unroll
    for (int i = 0; i < 8; ++i) BLOAD(xr[i], vx, rx, sx + ((i + rot) & 7) * 128);
  }
  BAR();
  const unsigned vw3 = ((lane >> 2) & 7) * 3072 + (lane >> 5) * 64 + (lane & 3) * 16;
  auto issue_w = [&](int s) {
    dma16x3(rw, vw3, (unsigned)(wid * 8) * 3072 + cb * 128 + ((s + rot) & 7) * (64 * 3072), lds0 + WR + (s & 3) * QSLOT + wid * 3072);
  };
  issue_w(0); issue_w(1); issue_w(2); issue_w(3);
  const u64 tag = read_tag(ws);
  unsigned pf;
  {
    const unsigned ONE2 = 0x3f803f80u;
    float s0 = 0.f, s1 = 0.f, q0 = 0.f, q1 = 0.f;
#define STAT(i) do { wait_vm1<19 - (i)>(xr[i]); \
      s0 = dot2(xr[i][0], ONE2, s0); q0 = dot2(xr[i][0], xr[i][0], q0); s1 = dot2(xr[i][1], ONE2, s1); q1 = dot2(xr[i][1], xr[i][1], q1); \
      s0 = dot2(xr[i][2], ONE2, s0); q0 = dot2(xr[i][2], xr[i][2], q0); s1 = dot2(xr[i][3], ONE2, s1); q1 = dot2(xr[i][3], xr[i][3], q1); \
      asm volatile("" : "+v"(s0), "+v"(s1), "+v"(q0), "+v"(q1)); } while (0)
    STAT(0); STAT(1); STAT(2); STAT(3); STAT(4); STAT(5); STAT(6); STAT(7);
    ST(1);
    {
      const int t = (b * 8 + wid) * 7 + (r < 7 ? r : 0);
      const char* p = (h ? gwu : gwg) + (size_t)t * 128;
      asm volatile("global_load_dword %0, %1, off" : "=v"(pf) : "v"(p) : "memory");
    }
    const float mean = sum8(s0 + s1) * (1.0f / 512.0f);
    const float rs = __builtin_amdgcn_rsqf(__builtin_fmaxf(sum8(q0 + q1) * (1.0f / 512.0f) - mean * mean, 0.0f) + 1e-5f);
    const float mr = -mean * rs;
    const lptr pxw = lds + (wid >> 2) * 32768 + (lane & 7) * 512 + ((((wid & 3) * 8 + (lane >> 3)) ^ (lane & 7)) * 16);
#pragma unroll
    for (int j = 0; j < 8; ++j) {
      u32x4 n;
#pragma unroll
      for (int i = 0; i < 4; ++i) n[i] = pk2(__builtin_fmaf(bf_lo(xr[j][i]), rs, mr), __builtin_fmaf(bf_hi(xr[j][i]), rs, mr));
      *(LAS u32x4*)(pxw + j * 4096) = n;
    }
  }
  const int fq = wid & 3, gm = wid >> 2;
  const bool has_ffn = cb < 7;
  const rsrc_t rgu = mk_rsrc(gm ? gwu : gwg, D_ * F_ * 2);
  const unsigned vwf = ((lane >> 2) & 7) * (F_ * 2) + (lane >> 5) * 64 + (lane & 3) * 16;
  const unsigned gring = WR + ((wid + 4) & 7) * 12288;
  const unsigned sgu = cb * 512 + fq * 128;
  auto issue_g = [&](int j, int slot) {
    const unsigned so = sgu + (unsigned)((j + 4 * rot) & 31) * (16 * F_ * 2);
    dma16x2(rgu, vwf, so, so + 8 * F_ * 2, lds0 + gring + slot * 2048);
  };
  auto issue_gu = [&] {
#pragma unroll
    for (int j = 0; j < 6; ++j) issue_g(j, j);
  };
  lptr pa[4];
#pragma unroll
  for (int g = 0; g < 4; ++g) pa[g] = lds + (2 * g + h) * 512 + ((r ^ (2 * g + h)) * 16);
  {
    const int rt = wid & 1, ch = (wid >> 1) & 1;
    lptr par[4];
#pragma unroll
    for (int g = 0; g < 4; ++g) par[g] = pa[g] + rt * 32768;
    const lptr pw = lds + WR + h * 3072 + ch * 512 + trn;
    const int pair = cb * 2 + ch, tile = rb * 2 + rt;
    const rsrc_t rq = mk_rsrc(ws + WS_Q, 2097152 + 4194304);
    f32x16 a0 = {}, a1 = {};
    F3 f0, f1;
    WAIT_BAR(10);
    ST(2);
    if (wid < 4) {
      f0.x = lds_frag(par[0]); f0.a = lds_frag_tr(pw, pw + 256); f0.b = lds_frag_tr(pw + 1024, pw + 1024 + 256);
      qkv_steps<true, 0>(par, pw, f0, f1, a0, a1, issue_w, issue_gu);
      const unsigned ko = 2097152 + (unsigned)((2 * pair) * 64 + ((tile + 62) & 63)) * 2048 + voff;
      BSTORE_DEV(pack8(a1, 0), rq, ko);
      BSTORE_DEV(pack8(a1, 8), rq, ko + 131072);
#pragma unroll
      for (int i = 0; i < 16; ++i) a0[i] *= QSCALE;
      const unsigned qo = (unsigned)((2 * pair) * 64 + tile) * 1024 + voff;
      BSTORE_DEV(pack8(a0, 0), rq, qo);
      BSTORE_DEV(pack8(a0, 8), rq, qo + 65536);
    } else {
      f0.x = lds_frag(par[0]); f0.a = lds_frag_tr(pw + 2048, pw + 2048 + 256);
      qkv_steps<false, 0>(par, pw, f0, f1, a0, a1, issue_w, issue_gu);
      const unsigned vo = 2097152 + (unsigned)(2 * pair + (r >> 4)) * 131072 + h * 256 + (r & 15) * 16;
      BSTORE_DEV(pack8(a0, 0), rq, vo + ((tile + 63) & 63) * 2048 + 1024);
      BSTORE_DEV(pack8(a0, 8), rq, vo + tile * 2048 + 1536);
    }
    asm volatile("" :: "v"(pf));
    ST(3);
  }
  char* const fq_flag = ws + WS_FQ + (((cb * 2 + ((wid >> 1) & 1)) * 2 + gm) * 64 + rb * 2 + (wid & 1)) * 8;

  f32x16 c00 = {}, c01 = {}, c10 = {}, c11 = {};
  if (has_ffn) {
    const lptr pg = lds + gring + h * 1024 + trn;
    F4 f0, f1;
    int slot = 0;
#define GU_READ(n, j) do { const lptr w_ = pg + slot * 2048; const lptr x_ = pa[(j) & 3] + xo; \
      n.x0 = lds_frag(x_); n.w0 = lds_frag_tr(w_, w_ + 256); n.x1 = lds_frag(x_ + 32768); n.w1 = lds_frag_tr(w_ + 512, w_ + 512 + 256); } while (0)
#define GU_STEP(c, n, j, jj, VM, RD, FILL) do { \
      const int ps_ = slot; slot = (slot == 5) ? 0 : slot + 1; \
      if (((j) & 3) == 3) xo += 4096; \
      if (RD) wait_vm<VM>(); \
      MM(c00, c.w0, c.x0); \
      if (RD) GU_READ(n, (j) + 1); \
      MM(c01, c.w1, c.x0); MM(c10, c.w0, c.x1); \
      if (FILL) issue_g((jj) + 6, ps_); \
      MM(c11, c.w1, c.x1); } while (0)
    unsigned xo = 0;
    wait_vm<10>();
    GU_READ(f0, 0);
#pragma unroll 1
    for (int j = 0; j < 24; j += 4) {
      GU_STEP(f0, f1, 0, j, 8, true, true); GU_STEP(f1, f0, 1, j + 1, 8, true, true);
      GU_STEP(f0, f1, 2, j + 2, 8, true, true); GU_STEP(f1, f0, 3, j + 3, 8, true, true);
      if (j == 4 && lane == 0) set_flag(fq_flag, tag);
    }
    GU_STEP(f0, f1, 0, 24, 8, true, true); GU_STEP(f1, f0, 1, 25, 8, true, true);
    GU_STEP(f0, f1, 2, 26, 8, true, false); GU_STEP(f1, f0, 3, 27, 6, true, false);
    GU_STEP(f0, f1, 0, 28, 4, true, false); GU_STEP(f1, f0, 1, 29, 2, true, false);
    GU_STEP(f0, f1, 2, 30, 0, true, false); GU_STEP(f1, f0, 3, 31, 0, false, false);
    ST(4);
    LAS f32x4* ex = (LAS f32x4*)(lds + gring + 4096) + lane;
    const f32x16 &e0 = gm ? c00 : c10, &e1 = gm ? c01 : c11;
#pragma unroll
    for (int i = 0; i < 4; ++i) { ex[i * 64] = V4(e0, i); ex[256 + i * 64] = V4(e1, i); }
  } else {
    wait_vm<0>();
    if (lane == 0) set_flag(fq_flag, tag);
    unsigned d[2];
#pragma unroll
    for (int i = 0; i < 2; ++i) {
      const int t = ((rb * 8 + wid) * 2 + i) * 36 + (lane < 36 ? lane : 0);
      const char* p = (t < 14336) ? gwd + (size_t)t * 128 : gwo + (size_t)(t - 14336) * 128;
      asm volatile("global_load_dword %0, %1, off" : "=v"(d[i]) : "v"(p) : "memory");
    }
    asm volatile("s_waitcnt vmcnt(0)" : "+v"(d[0]), "+v"(d[1]) :: "memory");
  }

  const int head = xcd * 4 + (bi >> 3), qt = (bi & 7) * 8 + wid;
  if (wid < 2) wait_flags(ws + WS_FQ + ((head >> 1) * 2 + wid) * 512, 64, tag, lane);
  BAR();
  ST(5);
  const rsrc_t rkv = mk_rsrc(ws + WS_KV + (size_t)head * 131072, 131072);
  const unsigned vvo = h * 256 + (r & 15) * 16;
  const bf16x8 ONES = {0x3f80, 0x3f80, 0x3f80, 0x3f80, 0x3f80, 0x3f80, 0x3f80, 0x3f80};
  const bf16x8 PAD = ((r & 27) == 16) ? ONES : (bf16x8){};
  u32x4 Qu, K0u, Ku, V0u;
  BLOAD_DEV(Qu, voff, mk_rsrc(ws + WS_Q, 2097152), (unsigned)(head * 64 + qt) * 1024);
  BLOAD_DEV(K0u, voff, rkv, 62u * 2048);
  BLOAD_DEV(Ku, voff, rkv, 63u * 2048);
  BLOAD_DEV(V0u, vvo, rkv, 63u * 2048 + 1024);
  auto issue_kv = [&](int c) {
    dma16_dev(rkv, voff, c * 16384 + wid * 2048, lds0 + (c & 3) * CHUNK + wid * REC);
    dma16_dev(rkv, voff, c * 16384 + wid * 2048 + 1024, lds0 + (c & 3) * CHUNK + wid * REC + 1024);
  };
  issue_kv(0); issue_kv(1);
  if (wid == 7) *(LAS bf16x8*)(lds + r * REC + 2048 + h * 16) = h ? (bf16x8){} : ONES;
  const rsrc_t rh = mk_rsrc(ws + WS_H, 7340032);
  {
    const LAS f32x4* ex = (const LAS f32x4*)(lds + WR + wid * 12288 + 4096) + lane;
    f32x16 &k0 = gm ? c10 : c00, &k1 = gm ? c11 : c01;
    f32x16 p0, p1;
#pragma unroll
    for (int i = 0; i < 4; ++i) {
      const f32x4 u = ex[i * 64], v = ex[256 + i * 64];
#pragma unroll
      for (int j = 0; j < 4; ++j) { p0[4 * i + j] = u[j]; p1[4 * i + j] = v[j]; }
    }
#pragma unroll
    for (int i = 0; i < 16; ++i) {
      const float g0 = gm ? p0[i] : k0[i], g1 = gm ? p1[i] : k1[i];
      p0[i] = p0[i] * k0[i] * __builtin_amdgcn_rcpf(1.0f + __builtin_amdgcn_exp2f(g0 * -1.4426950408889634f));
      p1[i] = p1[i] * k1[i] * __builtin_amdgcn_rcpf(1.0f + __builtin_amdgcn_exp2f(g1 * -1.4426950408889634f));
    }
    const unsigned ho = has_ffn ? (unsigned)((rb * 2 + gm) * 112 + cb * 16 + fq * 4) * 1024 + voff : 0x7ff00000u;
    BSTORE_DEV(pack8(p0, 0), rh, ho);
    BSTORE_DEV(pack8(p0, 8), rh, ho + 1024);
    BSTORE_DEV(pack8(p1, 0), rh, ho + 2048);
    BSTORE_DEV(pack8(p1, 8), rh, ho + 3072);
  }
  ST(6);
  asm volatile("s_waitcnt vmcnt(8)" : "+v"(Qu), "+v"(K0u), "+v"(Ku), "+v"(V0u) :: "memory");
  const bf16x8 Q = __builtin_bit_cast(bf16x8, Qu);
  bf16x8 K = __builtin_bit_cast(bf16x8, Ku), V0 = (r >= 16) ? PAD : __builtin_bit_cast(bf16x8, V0u);
  f32x16 SA, SB, C, O = {};
  {
    const f32x16 z = {};
    SA = MFMA(__builtin_bit_cast(bf16x8, K0u), Q, z);
    SB = MFMA(K, Q, z);
  }
  float m = SA[0];
#pragma unroll
  for (int i = 1; i < 16; ++i) m = __builtin_fmaxf(m, SA[i]);
#pragma unroll
  for (int i = 0; i < 16; ++i) m = __builtin_fmaxf(m, SB[i]);
  m = xhalf_max(m) + MBIAS;
#pragma unroll
  for (int i = 0; i < 16; ++i) { C[i] = -m; SA[i] -= m; }
  bf16x8 V1 = {}, P1 = {};
  unsigned aK = lds0 + voff;
  const unsigned apad = ((r & 27) == 16) ? 2048 : 2064;
  unsigned aV0 = lds0 + (r < 16 ? 1024 + vvo : apad);
  unsigned aV1 = lds0 + (r < 16 ? 1536 + vvo : apad);
  unsigned cnt = 0, t0, t1;
  ST(7); STC(14);
#define ATT_RUN(LAST) asm volatile( \
      "s_nop 4\n\t" \
      "L_attn_%=:\n\t" \
      CHUNK_STEPS CHUNK_NEXT \
      "s_cmp_lt_u32 %[cnt], " #LAST "\n\t" \
      "s_cbranch_scc1 L_attn_%=\n\t" \
      "s_waitcnt lgkmcnt(0)\n\t" \
      "s_nop 7\n\t" \
      : ATT_STATE : "{v[0:15]}"(C), "{v[64:67]}"(Q) : "v80", "v81", "v82", "v83", "scc", "memory")
  const int rt = wid & 1, kq = wid >> 1;
  const int drot = (((bi >> 3) * 7 + 1) / 2 + (bi & 7) * 2) % 14;
  const rsrc_t rwd = mk_rsrc(gwd, F_ * D_ * 2), rwo = mk_rsrc(gwo, D_ * D_ * 2);
  const unsigned vw1 = ((lane >> 2) & 7) * 1024 + (lane >> 5) * 64 + (lane & 3) * 16;
  auto dsrc = [&](int st) { const int t = st + drot; return t >= 14 ? t - 14 : t; };
  auto issue_d = [&](int st) {
    const unsigned so = (unsigned)((st < 14 ? dsrc(st) : st - 14) * 128 + wid * 16) * 1024 + cb * 128;
    dma16x2(st < 14 ? rwd : rwo, vw1, so, so + 8192, lds0 + ((st + 5) % 10) * 16384 + wid * 2048);
  };
  WAIT_BAR(6);  issue_kv(2); issue_kv(3);
  ATT_RUN(1);
  WAIT_BAR(4);
  if (wid == 0 && lane == 0) set_flag(ws + WS_FH + (rb * 8 + cb) * 8, tag);
  issue_kv(4);
  issue_d(0); issue_d(1); issue_d(2); issue_d(3); issue_d(4);
  ATT_RUN(2);
  WAIT_BAR(12); issue_kv(5);
  ATT_RUN(3);
  BAR();        issue_kv(6);
  u64 fl;
  asm volatile("global_load_dwordx2 %0, %1, off sc1" : "=v"(fl) : "v"(ws + WS_FH + (rb * 8 + (lane < 7 ? lane : 0)) * 8) : "memory");
  ATT_RUN(4);
  asm volatile("s_waitcnt vmcnt(0) lgkmcnt(0)\n\ts_barrier" : "+v"(fl) :: "memory");
  issue_kv(7);
  if (!__all(fl == tag)) wait_flags(ws + WS_FH + rb * 64, 7, tag, lane);
  u32x4 ha[14], hb[14];
#pragma unroll
  for (int st = 0; st < 14; ++st) {
    const unsigned so = (unsigned)((rb * 2 + rt) * 112 + dsrc(st) * 8 + kq * 2) * 1024;
    BLOAD_DEV(ha[st], voff, rh, so); BLOAD_DEV(hb[st], voff, rh, so + 1024);
  }
  const int t = threadIdx.x;
  const unsigned off = (unsigned)(rb * 64 + (t >> 3)) * 1024 + cb * 128 + (t & 7) * 16;
  u32x4 xv;
  BLOAD(xv, off, rx, 0u);
  ATT_RUN(7);
  WAIT_BAR(29);
  ATT_RUN(8);
  asm volatile("s_waitcnt vmcnt(0)" : "+v"(ha[0]), "+v"(ha[1]), "+v"(ha[2]), "+v"(ha[3]), "+v"(ha[4]), "+v"(ha[5]), "+v"(ha[6]), "+v"(ha[7]),
               "+v"(ha[8]), "+v"(ha[9]), "+v"(ha[10]), "+v"(ha[11]), "+v"(ha[12]), "+v"(ha[13]), "+v"(xv) :: "memory");
  asm volatile("" : "+v"(hb[0]), "+v"(hb[1]), "+v"(hb[2]), "+v"(hb[3]), "+v"(hb[4]), "+v"(hb[5]), "+v"(hb[6]), "+v"(hb[7]),
               "+v"(hb[8]), "+v"(hb[9]), "+v"(hb[10]), "+v"(hb[11]), "+v"(hb[12]), "+v"(hb[13]) :: "memory");
  O = MFMA(V1, P1, O);
  ST(8); STC(15);
  {
    float l = O[8];
    float omax = 0.f;
#pragma unroll
    for (int i = 0; i < 8; ++i) omax = __builtin_fmaxf(omax, __builtin_fabsf(O[i]));
    bool bad = !(l < 0x1p120f) || !(xhalf_max(omax) > 0x1p-100f);
#pragma unroll
    for (int i = 0; i < 8; ++i) bad = bad || !(__builtin_fabsf(O[i]) < 0x1p126f);
    if (__builtin_expect(__any(bad), 0)) {
      const f32x16 z = {};
      float mt = -__builtin_inff();
#pragma unroll 1
      for (int tt = 0; tt < 64; ++tt) {
        const f32x16 s = MFMA(__builtin_bit_cast(bf16x8, __builtin_amdgcn_raw_buffer_load_b128(rkv, voff, tt * 2048, 16)), Q, z);
#pragma unroll
        for (int i = 0; i < 16; ++i) mt = __builtin_fmaxf(mt, s[i]);
      }
      mt = xhalf_max(mt);
      O = z;
#pragma unroll 1
      for (int tt = 0; tt < 64; ++tt) {
        f32x16 s = MFMA(__builtin_bit_cast(bf16x8, __builtin_amdgcn_raw_buffer_load_b128(rkv, voff, ((tt + 62) & 63) * 2048, 16)), Q, z);
        bf16x8 v0 = __builtin_bit_cast(bf16x8, __builtin_amdgcn_raw_buffer_load_b128(rkv, vvo, ((tt + 63) & 63) * 2048 + 1024, 16));
        bf16x8 v1 = __builtin_bit_cast(bf16x8, __builtin_amdgcn_raw_buffer_load_b128(rkv, vvo, tt * 2048 + 1536, 16));
        if (r >= 16) { v0 = PAD; v1 = PAD; }
#pragma unroll
        for (int i = 0; i < 16; ++i) s[i] = __builtin_amdgcn_exp2f(s[i] - mt);
        O = MFMA(v0, __builtin_bit_cast(bf16x8, pack8(s, 0)), O);
        O = MFMA(v1, __builtin_bit_cast(bf16x8, pack8(s, 8)), O);
      }
      l = O[8];
    }
    const float il = 1.0f / l;
#pragma unroll
    for (int i = 0; i < 8; ++i) O[i] *= il;
  }
  const rsrc_t ro = mk_rsrc(ws + WS_O, 2097152);
  BSTORE_DEV(pack8(O, 0), ro, (unsigned)(qt * 32 + head) * 1024 + voff);

  f32x16 d0 = {}, d1 = {};
  const lptr pd = lds + kq * 4096 + h * 256 + trn;
  F4 e0, e1;
#define D_READ(n, st) do { const lptr w_ = pd + (((st) + 5) % 10) * 16384; \
    n.w0 = lds_frag_tr(w_, w_ + 1024); n.w1 = lds_frag_tr(w_ + 512, w_ + 1536); \
    n.x0 = lds_frag_tr(w_ + 2048, w_ + 3072); n.x1 = lds_frag_tr(w_ + 2560, w_ + 3584); } while (0)
#define D_STEP(c, n, st, A, B, RD) do { \
    MM(d0, c.w0, __builtin_bit_cast(bf16x8, A)); if (RD) D_READ(n, (st) + 1); MM(d1, c.w1, __builtin_bit_cast(bf16x8, A)); \
    MM(d0, c.x0, __builtin_bit_cast(bf16x8, B)); MM(d1, c.x1, __builtin_bit_cast(bf16x8, B)); } while (0)
  if (b == 193 && wid == 0) {
#pragma unroll 1
    for (int i = 0; i < 4; ++i) wait_flags(ws + WS_FH + i * 512, 64, tag, lane);
    if (lane == 0) set_flag(ws + WS_EPOCH, tag);
  }
  BAR();
  ST(9);
  issue_d(5); issue_d(6); issue_d(7); issue_d(8); issue_d(9);
  D_READ(e0, 0);
  D_STEP(e0, e1, 0, ha[0], hb[0], true); D_STEP(e1, e0, 1, ha[1], hb[1], true);
  wait_vm<10>();
  if (lane == 0) set_flag(ws + WS_FO + (qt * 32 + head) * 8, tag);
  D_STEP(e0, e1, 2, ha[2], hb[2], true); D_STEP(e1, e0, 3, ha[3], hb[3], true);
  WAIT_BAR(1);
  issue_d(10); issue_d(11); issue_d(12); issue_d(13); issue_d(14);
  D_STEP(e0, e1, 4, ha[4], hb[4], true); D_STEP(e1, e0, 5, ha[5], hb[5], true); D_STEP(e0, e1, 6, ha[6], hb[6], true);
  D_STEP(e1, e0, 7, ha[7], hb[7], true); D_STEP(e0, e1, 8, ha[8], hb[8], true);
  WAIT_BAR(0);
  issue_d(15); issue_d(16); issue_d(17);
  asm volatile("global_load_dwordx2 %0, %1, off sc1" : "=v"(fl) : "v"(ws + WS_FO + ((rb * 2 + rt) * 32 + r) * 8) : "memory");
  D_STEP(e1, e0, 9, ha[9], hb[9], true); D_STEP(e0, e1, 10, ha[10], hb[10], true);
  asm volatile("s_waitcnt vmcnt(0)" : "+v"(fl) :: "memory");
  u32x4 oa[4], ob[4];
  const bool early = __all(fl == tag);
#define O_LOADS(base) do { _Pragma("unroll") for (int j = 0; j < 4; ++j) { const unsigned so = (base) + j * 8192; \
      BLOAD_DEV(oa[j], voff, ro, so); BLOAD_DEV(ob[j], voff, ro, so + 1024); } } while (0)
  const unsigned obase = (unsigned)((rb * 2 + rt) * 32 + kq * 2) * 1024;
  O_LOADS(early ? obase : 0x7ff00000u);
  D_STEP(e1, e0, 11, ha[11], hb[11], true); D_STEP(e0, e1, 12, ha[12], hb[12], true);
  BAR();
  D_STEP(e1, e0, 13, ha[13], hb[13], true);
  ST(10);
#define O_WAIT() asm volatile("s_waitcnt vmcnt(0)" : "+v"(oa[0]), "+v"(oa[1]), "+v"(oa[2]), "+v"(oa[3]), "+v"(ob[0]), "+v"(ob[1]), "+v"(ob[2]), "+v"(ob[3]) :: "memory")
  O_WAIT();
  if (!early) {
    wait_flags(ws + WS_FO + (rb * 2 + rt) * 256, 32, tag, lane);
    O_LOADS(obase);
    O_WAIT();
  }
  ST(11);
  D_STEP(e0, e1, 14, oa[0], ob[0], true); D_STEP(e1, e0, 15, oa[1], ob[1], true); D_STEP(e0, e1, 16, oa[2], ob[2], true);
  D_STEP(e1, e0, 17, oa[3], ob[3], false);
  const lptr red0 = lds + 49152;
  LAS float* red = (LAS float*)(red0 + kq * 17408) + (rt * 32 + r) * 68 + 4 * h;
#pragma unroll
  for (int i = 0; i < 4; ++i) { *(LAS f32x4*)(red + 8 * i) = V4(d0, i); *(LAS f32x4*)(red + 32 + 8 * i) = V4(d1, i); }
  BAR();
  f32x4 s0 = {bf_lo(xv[0]), bf_hi(xv[0]), bf_lo(xv[1]), bf_hi(xv[1])}, s1 = {bf_lo(xv[2]), bf_hi(xv[2]), bf_lo(xv[3]), bf_hi(xv[3])};
  sum4(red0, t, s0, s1);
  *(u32x4*)(out + off) = (u32x4){pk2(s0[0], s0[1]), pk2(s0[2], s0[3]), pk2(s1[0], s1[1]), pk2(s1[2], s1[3])};
  ST(12); ST_OUT(ws); TR_OUT(ws);
}

extern "C" void kernel_launch(void* const* d_in, const int* in_sizes, int n_in, void* d_out, int out_size, void* d_ws,
                              size_t ws_size, hipStream_t stream) {
  (void)hipFuncSetAttribute((const void*)k_all, hipFuncAttributeMaxDynamicSharedMemorySize, LDS_BYTES);
  k_all<<<256, 512, LDS_BYTES, stream>>>((const char*)d_in[0], (const char*)d_in[1], (const char*)d_in[2], (const char*)d_in[4],
                                         (const char*)d_in[5], (const char*)d_in[6], (char*)d_ws, (char*)d_out);
}
```

```cpp
#include <hip/hip_runtime.h>
#include <hip/hip_bf16.h>
#include <stdint.h>

typedef __attribute__((ext_vector_type(8))) short bf16x8;
typedef __attribute__((ext_vector_type(4))) short s16x4;
typedef __attribute__((ext_vector_type(16))) float f32x16;
typedef __attribute__((ext_vector_type(4))) float f32x4;
typedef __attribute__((ext_vector_type(2))) float f32x2;
typedef __attribute__((ext_vector_type(4))) unsigned u32x4;
typedef __attribute__((ext_vector_type(2))) __bf16 bf2;
typedef unsigned long long u64;
typedef __amdgpu_buffer_rsrc_t rsrc_t;
#define LAS __attribute__((address_space(3)))
#define GAS __attribute__((address_space(1)))
typedef LAS char* lptr;

#define MFMA(a, b, c) __builtin_amdgcn_mfma_f32_32x32x16_bf16((a), (b), (c), 0, 0, 0)
#define MM(c, a, b) do { __builtin_amdgcn_sched_barrier(0); c = MFMA(a, b, c); asm volatile("" : "+v"(c) :: "memory"); \
                         __builtin_amdgcn_sched_barrier(0); } while (0)
#define RFL(x) __builtin_amdgcn_readfirstlane(x)

#define PRIO_A 1
#define PRIO_G 0
constexpr int S_ = 2048, D_ = 512, F_ = 1792;
constexpr size_t WS_EPOCH = 0;
constexpr size_t WS_FX = 2048, WS_FQ = 4096, WS_FH = 6144, WS_FO = 8192;
constexpr size_t WS_GO   = 16384;
constexpr size_t WS_Q    = 65536;
constexpr size_t WS_KV   = WS_Q + 2097152;
constexpr size_t WS_XN   = WS_KV + 4194304;
constexpr size_t WS_O    = WS_XN + 2097152;
constexpr size_t WS_H    = WS_O + 2097152;
constexpr float QSCALE = 0.25f * 1.4426950408889634f;
constexpr float MBIAS = 80.0f;
constexpr int LDS_BYTES = 81920;
#define POLL_SLEEP 2
#define G_QUIET 450
#define G_TOUCH 1200
#define FX_HS 5
#define GO_HS 5
#define SPIN_CAP (1u << 16)

#define ST_DECL
#define ST(i)
#define STC(i)
#define TR(i)
#define TRD(i)
#define TR_OUT(wsp)
#define ST_OUT(wsp)
#define ST_ARG
#define ST_PASS

__device__ __forceinline__ rsrc_t mk_rsrc(const void* p, unsigned bytes) {
  return __builtin_amdgcn_make_buffer_rsrc((void*)p, (short)0, (int)bytes, 0x00020000);
}
__device__ __forceinline__ void dma16(rsrc_t r, unsigned voff, unsigned soff, unsigned lds) {
  unsigned keep;
  asm volatile("s_mov_b32 %0, m0\n\ts_mov_b32 m0, %1\n\ts_nop 0\n\tbuffer_load_dwordx4 %2, %3, %4 offen lds\n\ts_mov_b32 m0, %0"
               : "=&s"(keep) : "s"(lds), "v"(voff), "s"(r), "s"(soff) : "memory");
}
__device__ __forceinline__ void dma16_dev(rsrc_t r, unsigned voff, unsigned soff, unsigned lds) {
  unsigned keep;
  asm volatile("s_mov_b32 %0, m0\n\ts_mov_b32 m0, %1\n\ts_nop 0\n\tbuffer_load_dwordx4 %2, %3, %4 offen sc1 lds\n\ts_mov_b32 m0, %0"
               : "=&s"(keep) : "s"(lds), "v"(voff), "s"(r), "s"(soff) : "memory");
}
__device__ __forceinline__ void dma16x3(rsrc_t r, unsigned voff, unsigned soff, unsigned lds) {
  unsigned keep;
  asm volatile("s_mov_b32 %0, m0\n\ts_mov_b32 m0, %1\n\ts_nop 0\n\tbuffer_load_dwordx4 %2, %3, %4 offen lds\n\t"
               "buffer_load_dwordx4 %2, %3, %4 offen offset:1024 lds\n\tbuffer_load_dwordx4 %2, %3, %4 offen offset:2048 lds\n\ts_mov_b32 m0, %0"
               : "=&s"(keep) : "s"(lds), "v"(voff), "s"(r), "s"(soff) : "memory");
}
__device__ __forceinline__ float fma1(float a, float b, float c) {
  float d; asm("v_fma_f32 %0, %1, %2, %3" : "=v"(d) : "v"(a), "v"(b), "v"(c)); return d;
}
#define BLOAD(dst, voff, r, soff) asm volatile("buffer_load_dwordx4 %0, %1, %2, %3 offen" : "=v"(dst) : "v"(voff), "s"(r), "s"(soff) : "memory")
#define BLOAD_DEV(dst, voff, r, soff) asm volatile("buffer_load_dwordx4 %0, %1, %2, %3 offen sc1" : "=v"(dst) : "v"(voff), "s"(r), "s"(soff) : "memory")
#define BSTORE_DEV(val, r, voff) __builtin_amdgcn_raw_buffer_store_b128((val), (r), (voff), 0, 16)
#define WAIT_BAR(N) asm volatile("s_waitcnt vmcnt(" #N ") lgkmcnt(0)\n\ts_barrier" ::: "memory")
#define BAR() asm volatile("s_waitcnt lgkmcnt(0)\n\ts_barrier" ::: "memory")
template <int N> __device__ __forceinline__ void wait_bar() { asm volatile("s_waitcnt vmcnt(%0) lgkmcnt(0)\n\ts_barrier" :: "n"(N) : "memory"); }
template <int N> __device__ __forceinline__ void wait_vm2(u32x4& a, u32x4& b) {
  asm volatile("s_waitcnt vmcnt(%2)" : "+v"(a), "+v"(b) : "n"(N) : "memory");
}
template <int N> __device__ __forceinline__ void wait_vm1(u32x4& a) { asm volatile("s_waitcnt vmcnt(%1)" : "+v"(a) : "n"(N) : "memory"); }

__device__ __forceinline__ unsigned pk2(float lo, float hi) {
  f32x2 v = {lo, hi};
  return __builtin_bit_cast(unsigned, __builtin_convertvector(v, bf2));
}
__device__ __forceinline__ float bf_lo(unsigned u) { return __uint_as_float(u << 16); }
__device__ __forceinline__ float bf_hi(unsigned u) { return __uint_as_float(u & 0xffff0000u); }
__device__ __forceinline__ float xhalf_max(float v) {
  auto r = __builtin_amdgcn_permlane32_swap(__float_as_uint(v), __float_as_uint(v), false, false);
  return __builtin_fmaxf(__uint_as_float(r[0]), __uint_as_float(r[1]));
}
__device__ __forceinline__ bf16x8 lds_frag(lptr p) { return *(const LAS bf16x8*)p; }
typedef short v4i16_t __attribute__((ext_vector_type(4)));
__device__ __forceinline__ bf16x8 lds_frag_tr(lptr p0, lptr p1) {
  s16x4 a = __builtin_bit_cast(s16x4, __builtin_amdgcn_ds_read_tr16_b64_v4i16((LAS v4i16_t*)p0));
  s16x4 b = __builtin_bit_cast(s16x4, __builtin_amdgcn_ds_read_tr16_b64_v4i16((LAS v4i16_t*)p1));
  return (bf16x8){a[0], a[1], a[2], a[3], b[0], b[1], b[2], b[3]};
}
__device__ __forceinline__ float dot2(unsigned a, unsigned b, float c) {
  return __builtin_amdgcn_fdot2_f32_bf16(__builtin_bit_cast(bf2, a), __builtin_bit_cast(bf2, b), c, false);
}
__device__ __forceinline__ u32x4 pack8(const f32x16& a, int o) {
  return (u32x4){pk2(a[o], a[o + 1]), pk2(a[o + 2], a[o + 3]), pk2(a[o + 4], a[o + 5]), pk2(a[o + 6], a[o + 7])};
}
template <int CTRL> __device__ __forceinline__ float dpp_add(float x) {
  return x + __builtin_bit_cast(float, __builtin_amdgcn_mov_dpp(__builtin_bit_cast(int, x), CTRL, 0xf, 0xf, true));
}
__device__ __forceinline__ float sum8(float x) { return dpp_add<0x141>(dpp_add<0x4E>(dpp_add<0xB1>(x))); }

__device__ __forceinline__ u64 read_tag(const char* ws) {
  u64 e;
  asm volatile("s_load_dwordx2 %0, %1, 0x0\n\ts_waitcnt lgkmcnt(0)" : "=s"(e) : "s"(ws + WS_EPOCH) : "memory");
  e = (e ^ (e >> 31)) * 0x9E3779B97F4A7C15ull + 0x632BE59BD9B4E019ull;
  return e ^ (e >> 29);
}
__device__ __forceinline__ void set_flag(char* p, u64 tag) { __hip_atomic_store((GAS u64*)p, tag, __ATOMIC_RELAXED, __HIP_MEMORY_SCOPE_AGENT); }
__device__ __forceinline__ void wait_flags(const char* p, int n, u64 tag, int lane) {
  GAS u64* f = (GAS u64*)p + (lane < n ? lane : 0);
#pragma unroll 1
  for (unsigned spins = 0; spins < SPIN_CAP; ++spins) {
    if (__all(__hip_atomic_load(f, __ATOMIC_RELAXED, __HIP_MEMORY_SCOPE_AGENT) == tag)) break;
    __builtin_amdgcn_s_sleep(POLL_SLEEP);
  }
  asm volatile("" ::: "memory");
}

constexpr int QW_SLOT = 12288, QX_OFF = 4 * QW_SLOT;
struct QFrag { bf16x8 x, q, k, v; };

template <int I>
__device__ __forceinline__ void qkv_hs(const lptr (&pa)[4], lptr pw, QFrag& f0, QFrag& f1, f32x16& aq, f32x16& ak, f32x16& av,
                                       rsrc_t rxn, unsigned xno, int ch, char* fx, char* go, u64 tag, bool leader ST_ARG) {
  if constexpr (I < 16) {
    {
      const lptr w = pw + (I & 3) * QW_SLOT + 6144;
      MM(aq, f0.q, f0.x);
      f1.x = lds_frag(pa[(I & 1) * 2 + 1] + ((I >> 1) % 3) * 8192); f1.q = lds_frag_tr(w, w + 256);
      MM(ak, f0.k, f0.x);
      f1.k = lds_frag_tr(w + 1024, w + 1024 + 256); f1.v = lds_frag_tr(w + 2048, w + 2048 + 256);
      MM(av, f0.x, f0.v);
    }
    if constexpr (I < 2) {
      if (ch == I) { BSTORE_DEV(__builtin_bit_cast(u32x4, f0.x), rxn, xno); BSTORE_DEV(__builtin_bit_cast(u32x4, f1.x), rxn, xno + 1024); }
    }
    if constexpr (I < 15) {
      if constexpr (I < 3) wait_bar<(2 - I) * 3 + (I >= 1 ? 2 : 0)>(); else if constexpr (I == FX_HS) wait_bar<0>(); else BAR();
      if constexpr (I == FX_HS) { if (leader) set_flag(fx, tag); }
      if constexpr (I == GO_HS) { if (leader) set_flag(go, tag); }
      const lptr w = pw + ((I + 1) & 3) * QW_SLOT;
      MM(aq, f1.q, f1.x);
      f0.x = lds_frag(pa[((I + 1) & 1) * 2] + (((I + 1) >> 1) % 3) * 8192); f0.q = lds_frag_tr(w, w + 256);
      MM(ak, f1.k, f1.x);
      f0.k = lds_frag_tr(w + 1024, w + 1024 + 256); f0.v = lds_frag_tr(w + 2048, w + 2048 + 256);
      MM(av, f1.x, f1.v);
    } else {
      MM(aq, f1.q, f1.x); MM(ak, f1.k, f1.x); MM(av, f1.x, f1.v);
    }
    qkv_hs<I + 1>(pa, pw, f0, f1, aq, ak, av, rxn, xno, ch, fx, go, tag, leader ST_PASS);
  }
}

__device__ __forceinline__ u64 qkv_phase(lptr lds, unsigned lds0, const char* __restrict__ gx, const char* __restrict__ gw,
                                         char* __restrict__ ws, int b, int wid, int lane ST_ARG) {
  const int xcd = b & 7, bi = b >> 3;
  const int rb = (xcd >> 1) * 8 + (bi & 7);
  const int cb = (xcd & 1) * 4 + (bi >> 3);
  const int rot = (rb + cb) & 7;
  const int lw = wid & 3;
  const rsrc_t rw = mk_rsrc(gw, D_ * 3 * D_ * 2);
  const unsigned vw = ((lane >> 2) & 7) * 3072 + (lane >> 5) * 64 + (lane & 3) * 16;
  const unsigned sw = (unsigned)(lw * 8) * 3072 + cb * 128;
  auto issue = [&](int i) {
    dma16x3(rw, vw, sw + ((i + 2 * rot) & 15) * (32 * 3072), lds0 + (i & 3) * QW_SLOT + lw * 3072);
  };
  u64 tag;
  if (wid >= 4) {
    const rsrc_t rx = mk_rsrc(gx, S_ * D_ * 2);
    u32x4 xa[8], xb[8];
    const unsigned vx = (lane >> 3) * 1024 + (lane & 7) * 16, sx = (unsigned)(rb * 64 + lw * 16) * 1024;
#pragma unroll
    for (int i = 0; i < 8; ++i) { BLOAD(xa[i], vx, rx, sx + ((i + rot) & 7) * 128); BLOAD(xb[i], vx, rx, sx + 8192 + ((i + rot) & 7) * 128); }
    BAR();
    tag = read_tag(ws);
    float ra, ma, rb_, mb;
    {
      const unsigned ONE2 = 0x3f803f80u;
      float s0 = 0.f, s1 = 0.f, q0 = 0.f, q1 = 0.f;
#define STAT(i) do { wait_vm2<14 - 2 * (i)>(xa[i], xb[i]); _Pragma("unroll") for (int j = 0; j < 4; ++j) { \
          s0 = dot2(xa[i][j], ONE2, s0); q0 = dot2(xa[i][j], xa[i][j], q0); s1 = dot2(xb[i][j], ONE2, s1); q1 = dot2(xb[i][j], xb[i][j], q1); } \
        asm volatile("" : "+v"(s0), "+v"(s1), "+v"(q0), "+v"(q1)); } while (0)
      STAT(0); STAT(1); STAT(2); STAT(3); STAT(4); STAT(5); STAT(6); STAT(7);
      ST(1);
      const float m0 = sum8(s0) * (1.0f / 512.0f), m1 = sum8(s1) * (1.0f / 512.0f);
      ra = __builtin_amdgcn_rsqf(__builtin_fmaxf(sum8(q0) * (1.0f / 512.0f) - m0 * m0, 0.0f) + 1e-5f);
      rb_ = __builtin_amdgcn_rsqf(__builtin_fmaxf(sum8(q1) * (1.0f / 512.0f) - m1 * m1, 0.0f) + 1e-5f);
      ma = -m0 * ra; mb = -m1 * rb_;
    }
    const lptr pxw = lds + QX_OFF + (lw >> 1) * 4096 + (lane & 7) * 512 + ((((lw & 1) * 16 + (lane >> 3)) ^ (lane & 7)) * 16);
    auto norm = [&](u32x4& v, float rs, float mr) {
      float lo[4], hi[4];
#pragma unroll
      for (int i = 0; i < 4; ++i) { lo[i] = bf_lo(v[i]); hi[i] = bf_hi(v[i]); }
      asm volatile("" : "+v"(lo[0]), "+v"(lo[1]), "+v"(lo[2]), "+v"(lo[3]), "+v"(hi[0]), "+v"(hi[1]), "+v"(hi[2]), "+v"(hi[3]));
#pragma unroll
      for (int i = 0; i < 4; ++i) { lo[i] = fma1(lo[i], rs, mr); hi[i] = fma1(hi[i], rs, mr); }
      asm volatile("" : "+v"(lo[0]), "+v"(lo[1]), "+v"(lo[2]), "+v"(lo[3]), "+v"(hi[0]), "+v"(hi[1]), "+v"(hi[2]), "+v"(hi[3]));
#pragma unroll
      for (int i = 0; i < 4; ++i) v[i] = pk2(lo[i], hi[i]);
      asm volatile("" : "+v"(v));
    };
    auto norm2 = [&](int j) { norm(xa[j], ra, ma); norm(xb[j], rb_, mb); };
    auto put_a = [&](int j) { *(LAS u32x4*)(pxw + (j % 3) * 8192) = xa[j]; };
    auto put_b = [&](int j) { *(LAS u32x4*)(pxw + (j % 3) * 8192 + 128) = xb[j]; };
    norm2(0); put_a(0); put_b(0);
    BAR();
    ST(2);
#define LB(V, L, work) do { asm volatile("s_waitcnt vmcnt(" #V ") lgkmcnt(" #L ")\n\ts_barrier" ::: "memory"); work; } while (0)
    norm2(1); put_a(1); put_b(1); norm2(2);
    LB(63, 15, put_a(2); issue(4); norm2(3); norm2(4));
    LB(63, 1, put_b(2); issue(5); norm2(5); norm2(6));
    LB(63, 15, put_a(3); issue(6); norm2(7));
    LB(6, 1, put_b(3); issue(7));
    LB(6, 15, put_a(4); issue(8));
    LB(6, 1, put_b(4); issue(9));
    LB(6, 15, put_a(5); issue(10));
    LB(6, 1, put_b(5); issue(11));
    LB(6, 15, put_a(6); issue(12));
    LB(6, 1, put_b(6); issue(13));
    LB(6, 15, put_a(7); issue(14));
    LB(6, 1, put_b(7); issue(15));
    LB(6, 15, );
    LB(3, 0, );
    LB(0, 0, );
  } else {
    BAR();
    issue(0); issue(1); issue(2); issue(3);
    tag = read_tag(ws);
    const int rt = wid & 1, ch = wid >> 1, r = lane & 31, h = lane >> 5;
    lptr pa[4];
#pragma unroll
    for (int g = 0; g < 4; ++g) pa[g] = lds + QX_OFF + rt * 4096 + (2 * g + h) * 512 + ((r ^ (2 * g + h)) * 16);
    const lptr pw = lds + h * 3072 + ch * 512 + ((lane & 15) >> 2) * 64 + ((lane >> 4) & 1) * 32 + (lane & 3) * 8;
    const int pair = cb * 2 + ch, tile = rb * 2 + rt;
    f32x16 aq = {}, ak = {}, av = {};
    QFrag f0, f1;
    WAIT_BAR(9);
    ST(2); STC(12); TR(0);
    f0.x = lds_frag(pa[0]); f0.q = lds_frag_tr(pw, pw + 256); f0.k = lds_frag_tr(pw + 1024, pw + 1024 + 256); f0.v = lds_frag_tr(pw + 2048, pw + 2048 + 256);
    qkv_hs<0>(pa, pw, f0, f1, aq, ak, av, mk_rsrc(ws + WS_XN, 2097152), (unsigned)(tile * 32 + rot * 4 + ch * 2) * 1024 + lane * 16, ch,
              ws + WS_FX + (rb * 8 + cb) * 8, ws + WS_GO + b * 128, tag, wid == 0 && lane == 0 ST_PASS);
    ST(3); STC(13);
    const rsrc_t rq = mk_rsrc(ws + WS_Q, 2097152 + 4194304);
    const unsigned ko = 2097152 + (unsigned)((2 * pair) * 64 + ((tile + 62) & 63)) * 2048 + lane * 16;
    BSTORE_DEV(pack8(ak, 0), rq, ko);
    BSTORE_DEV(pack8(ak, 8), rq, ko + 131072);
    const unsigned vo = 2097152 + (unsigned)(2 * pair + (r >> 4)) * 131072 + h * 256 + (r & 15) * 16;
    BSTORE_DEV(pack8(av, 0), rq, vo + ((tile + 63) & 63) * 2048 + 1024);
    BSTORE_DEV(pack8(av, 8), rq, vo + tile * 2048 + 1536);
#pragma unroll
    for (int i = 0; i < 16; ++i) aq[i] *= QSCALE;
    const unsigned qo = (unsigned)((2 * pair) * 64 + tile) * 1024 + lane * 16;
    BSTORE_DEV(pack8(aq, 0), rq, qo);
    BSTORE_DEV(pack8(aq, 8), rq, qo + 65536);
  }
  WAIT_BAR(0);
  ST(4);
  if (wid == 0 && lane == 0) set_flag(ws + WS_FQ + (cb * 32 + rb) * 8, tag);
  return tag;
}

constexpr int REC = 2080, CHUNK = 8 * REC;

__device__ __forceinline__ void attn_phase(lptr lds, unsigned lds0, char* __restrict__ ws, int b, int wid, int lane, u64 tag ST_ARG) {
  const int xcd = b & 7, bi = b >> 3;
  const int head = xcd * 4 + (bi >> 3), qt = (bi & 7) * 8 + wid;
  const int r = lane & 31, h = lane >> 5;
  const bf16x8 ONES = {0x3f80, 0x3f80, 0x3f80, 0x3f80, 0x3f80, 0x3f80, 0x3f80, 0x3f80};
  const bf16x8 PAD = ((r & 27) == 16) ? ONES : (bf16x8){};
  if (wid == 1) *(LAS bf16x8*)(lds + r * REC + 2048 + h * 16) = h ? (bf16x8){} : ONES;
  if (wid == 0) wait_flags(ws + WS_FQ + (head >> 2) * 256, 32, tag, lane);
  BAR();
  ST(5);
  const rsrc_t rkv = mk_rsrc(ws + WS_KV + (size_t)head * 131072, 131072), rq = mk_rsrc(ws + WS_Q, 2097152);
  const unsigned voff = lane * 16, vvo = h * 256 + (r & 15) * 16;
  u32x4 Qu, K0u, Ku, V0u;
  BLOAD_DEV(Qu, voff, rq, (unsigned)(head * 64 + qt) * 1024);
  BLOAD_DEV(K0u, voff, rkv, 62u * 2048);
  BLOAD_DEV(Ku, voff, rkv, 63u * 2048);
  BLOAD_DEV(V0u, vvo, rkv, 63u * 2048 + 1024);
  dma16_dev(rkv, voff, wid * 2048, lds0 + wid * REC);
  dma16_dev(rkv, voff, wid * 2048 + 1024, lds0 + wid * REC + 1024);
  dma16_dev(rkv, voff, 16384 + wid * 2048, lds0 + CHUNK + wid * REC);
  dma16_dev(rkv, voff, 16384 + wid * 2048 + 1024, lds0 + CHUNK + wid * REC + 1024);
  asm volatile("s_waitcnt vmcnt(4)" : "+v"(Qu), "+v"(K0u), "+v"(Ku), "+v"(V0u) :: "memory");
  const bf16x8 Q = __builtin_bit_cast(bf16x8, Qu);
  bf16x8 K = __builtin_bit_cast(bf16x8, Ku), V0 = (r >= 16) ? PAD : __builtin_bit_cast(bf16x8, V0u);
  f32x16 SA, SB, C, O = {};
  {
    const f32x16 z = {};
    SA = MFMA(__builtin_bit_cast(bf16x8, K0u), Q, z);
    SB = MFMA(K, Q, z);
  }
  float m = SA[0];
#pragma unroll
  for (int i = 1; i < 16; ++i) m = __builtin_fmaxf(m, SA[i]);
#pragma unroll
  for (int i = 0; i < 16; ++i) m = __builtin_fmaxf(m, SB[i]);
  m = xhalf_max(m) + MBIAS;
#pragma unroll
  for (int i = 0; i < 16; ++i) { C[i] = -m; SA[i] -= m; }
  bf16x8 V1 = {}, P1 = {};
  unsigned aK = lds0 + lane * 16;
  const unsigned apad = ((r & 27) == 16) ? 2048 : 2064;
  unsigned aV0 = lds0 + (r < 16 ? 1024 + h * 256 + r * 16 : apad);
  unsigned aV1 = lds0 + (r < 16 ? 1536 + h * 256 + r * 16 : apad);
  unsigned cnt = 0, soff = 32768 + wid * 2048, mbase = lds0 + wid * REC, t0, t1, keep;
#define EXP(x) "v_exp_f32 v" #x ", v" #x "\n\t"
#define CVT(d, a, b) "v_cvt_pk_bf16_f32 v" #d ", v" #a ", v" #b "\n\t"
#define STEP(c0, c1, c2, c3, c4, c5, c6, c7, c8, c9, c10, c11, c12, c13, c14, c15, SN, off) \
  "s_waitcnt lgkmcnt(1)\n\t" \
  "v_mfma_f32_32x32x16_bf16 " SN ", v[68:71], v[64:67], v[0:15]\n\t" \
  "ds_read_b128 v[68:71], v88 offset:" #off "\n\t" \
  EXP(c0) EXP(c1) EXP(c2) EXP(c3) \
  "v_mfma_f32_32x32x16_bf16 v[48:63], v[76:79], v[84:87], v[48:63]\n\t" \
  "ds_read_b128 v[76:79], v90 offset:" #off "\n\t" \
  EXP(c4) EXP(c5) CVT(80, c0, c1) CVT(81, c2, c3) EXP(c6) EXP(c7) EXP(c8) EXP(c9) CVT(82, c4, c5) CVT(83, c6, c7) \
  EXP(c10) EXP(c11) \
  "s_waitcnt lgkmcnt(2)\n\t" \
  "v_mfma_f32_32x32x16_bf16 v[48:63], v[72:75], v[80:83], v[48:63]\n\t" \
  "ds_read_b128 v[72:75], v89 offset:" #off "\n\t" \
  EXP(c12) EXP(c13) CVT(84, c8, c9) CVT(85, c10, c11) EXP(c14) EXP(c15) CVT(86, c12, c13) CVT(87, c14, c15)
#define STEP_A(off) STEP(16, 17, 18, 19, 20, 21, 22, 23, 24, 25, 26, 27, 28, 29, 30, 31, "v[32:47]", off)
#define STEP_B(off) STEP(32, 33, 34, 35, 36, 37, 38, 39, 40, 41, 42, 43, 44, 45, 46, 47, "v[16:31]", off)
  ST(6); STC(14);
  asm volatile(
      "s_mov_b32 %[keep], m0\n\t"
      "s_waitcnt lgkmcnt(0)\n\t"
      "s_nop 4\n\t"
      "L_attn_%=:\n\t"
      "s_waitcnt vmcnt(2)\n\t"
      "s_barrier\n\t"
      "s_add_u32 %[t0], %[cnt], 2\n\t"
      "s_and_b32 %[t0], %[t0], 3\n\t"
      "s_mul_i32 %[t0], %[t0], 16640\n\t"
      "s_add_u32 m0, %[t0], %[mbase]\n\t"
      "s_and_b32 %[t1], %[soff], 0x1ffff\n\t"
      "s_add_u32 %[soff], %[soff], 16384\n\t"
      "buffer_load_dwordx4 v91, %[rkv], %[t1] offen sc1 lds\n\t"
      "buffer_load_dwordx4 v91, %[rkv], %[t1] offen offset:1024 sc1 lds\n\t"
      STEP_A(0) STEP_B(2080) STEP_A(4160) STEP_B(6240) STEP_A(8320) STEP_B(10400) STEP_A(12480) STEP_B(14560)
      "s_add_u32 %[cnt], %[cnt], 1\n\t"
      "s_and_b32 %[t0], %[cnt], 3\n\t"
      "s_cmp_eq_u32 %[t0], 0\n\t"
      "s_movk_i32 %[t1], 16640\n\t"
      "s_cselect_b32 %[t0], 0xffff3d00, %[t1]\n\t"
      "v_add_u32 v88, %[t0], v88\n\t"
      "v_add_u32 v89, %[t0], v89\n\t"
      "v_add_u32 v90, %[t0], v90\n\t"
      "s_cmp_lt_u32 %[cnt], 8\n\t"
      "s_cbranch_scc1 L_attn_%=\n\t"
      "s_waitcnt vmcnt(0) lgkmcnt(0)\n\t"
      "v_mfma_f32_32x32x16_bf16 v[48:63], v[76:79], v[84:87], v[48:63]\n\t"
      "s_mov_b32 m0, %[keep]\n\t"
      "s_nop 15\n\t"
      : "+{v[48:63]}"(O), "+{v[16:31]}"(SA), "={v[32:47]}"(SB), "+{v[68:71]}"(K), "+{v[72:75]}"(V0), "+{v[76:79]}"(V1),
        "+{v[84:87]}"(P1), "+{v88}"(aK), "+{v89}"(aV0), "+{v90}"(aV1), [cnt] "+s"(cnt), [soff] "+s"(soff), [t0] "=&s"(t0),
        [t1] "=&s"(t1), [keep] "=&s"(keep)
      : "{v[0:15]}"(C), "{v[64:67]}"(Q), "{v91}"(voff), [rkv] "s"(rkv), [mbase] "s"(mbase)
      : "v80", "v81", "v82", "v83", "scc", "memory");
#undef EXP
#undef CVT
#undef STEP
  ST(7); STC(15);
  float l = O[8];
  float omax = 0.f;
#pragma unroll
  for (int i = 0; i < 8; ++i) omax = __builtin_fmaxf(omax, __builtin_fabsf(O[i]));
  bool bad = !(l < 0x1p120f) || !(xhalf_max(omax) > 0x1p-100f);
#pragma unroll
  for (int i = 0; i < 8; ++i) bad = bad || !(__builtin_fabsf(O[i]) < 0x1p126f);
  if (__builtin_expect(__any(bad), 0)) {
    const f32x16 z = {};
    float mt = -__builtin_inff();
#pragma unroll 1
    for (int t = 0; t < 64; ++t) {
      const f32x16 s = MFMA(__builtin_bit_cast(bf16x8, __builtin_amdgcn_raw_buffer_load_b128(rkv, voff, t * 2048, 16)), Q, z);
#pragma unroll
      for (int i = 0; i < 16; ++i) mt = __builtin_fmaxf(mt, s[i]);
    }
    mt = xhalf_max(mt);
    O = z;
#pragma unroll 1
    for (int t = 0; t < 64; ++t) {
      f32x16 s = MFMA(__builtin_bit_cast(bf16x8, __builtin_amdgcn_raw_buffer_load_b128(rkv, voff, ((t + 62) & 63) * 2048, 16)), Q, z);
      bf16x8 v0 = __builtin_bit_cast(bf16x8, __builtin_amdgcn_raw_buffer_load_b128(rkv, vvo, ((t + 63) & 63) * 2048 + 1024, 16));
      bf16x8 v1 = __builtin_bit_cast(bf16x8, __builtin_amdgcn_raw_buffer_load_b128(rkv, vvo, t * 2048 + 1536, 16));
      if (r >= 16) { v0 = PAD; v1 = PAD; }
#pragma unroll
      for (int i = 0; i < 16; ++i) s[i] = __builtin_amdgcn_exp2f(s[i] - mt);
      O = MFMA(v0, __builtin_bit_cast(bf16x8, pack8(s, 0)), O);
      O = MFMA(v1, __builtin_bit_cast(bf16x8, pack8(s, 8)), O);
    }
    l = O[8];
  }
  const float il = 1.0f / l;
#pragma unroll
  for (int i = 0; i < 8; ++i) O[i] *= il;
  BSTORE_DEV(pack8(O, 0), mk_rsrc(ws + WS_O, 2097152), (unsigned)(qt * 32 + head) * 1024 + lane * 16);
  WAIT_BAR(0);
  ST(8);
  if (wid == 0 && lane == 0) set_flag(ws + WS_FO + ((bi & 7) * 32 + head) * 8, tag);
}

struct GFrag { bf16x8 x0, x1, g, u; };
__device__ __forceinline__ u64 gateup_phase(lptr lds, unsigned lds0, const char* __restrict__ gwg, const char* __restrict__ gwu,
                                            const char* __restrict__ gwd, const char* __restrict__ gwo, char* __restrict__ ws,
                                            int b, int wid, int lane ST_ARG) {
  const int xcd = b & 7, bi = b >> 3;
  const u64 t_start = __builtin_amdgcn_s_memrealtime();
  if (bi >= 28) {
    const u64 tag = read_tag(ws);
    BAR();
    const int j = xcd * 4 + (bi - 28);
    if (wid == 0 && lane == 0) set_flag(ws + WS_FH + ((j >> 1) * 16 + 14 + (j & 1)) * 8, tag);
    while (__builtin_amdgcn_s_memrealtime() - t_start < G_TOUCH) __builtin_amdgcn_s_sleep(16);
    const int t0 = ((bi - 28) * 8 + wid) * 288 + lane;
    unsigned d[5];
#pragma unroll
    for (int i = 0; i < 5; ++i) {
      const int t = (i < 4 || lane < 32) ? t0 + i * 64 : t0;
      const char* p = (t < 7168 ? gwd + (size_t)(t >> 2) * 1024 : gwo + (size_t)((t - 7168) >> 2) * 1024) + (xcd & 1) * 512 + (t & 3) * 128;
      asm volatile("global_load_dword %0, %1, off" : "=v"(d[i]) : "v"(p) : "memory");
    }
    asm volatile("s_waitcnt vmcnt(0)" : "+v"(d[0]), "+v"(d[1]), "+v"(d[2]), "+v"(d[3]), "+v"(d[4]) :: "memory");
    return tag;
  }
  const int rb = (xcd >> 1) * 4 + (bi & 3);
  const int fb = (xcd & 1) * 7 + (bi >> 2);
  const int rot = (bi & 3) * 4 + (bi >> 2) * 2 + (xcd >> 2) + ((bi >> 2) > 3);
  const int rh2 = wid & 1, ft = wid >> 1, h = lane >> 5;
  const rsrc_t rx = mk_rsrc(ws + WS_XN, 2097152), rg = mk_rsrc(gwg, D_ * F_ * 2), ru = mk_rsrc(gwu, D_ * F_ * 2);
  const unsigned vx = lane * 16;
  const unsigned vw = ((lane >> 2) & 7) * (F_ * 2) + (lane >> 5) * 64 + (lane & 3) * 16;
  const unsigned sx = (unsigned)((rb * 4 + (wid >> 1)) * 32 + (wid & 1)) * 1024;
  const unsigned sw = (unsigned)((wid >> 1) * 8) * (F_ * 2) + fb * 256 + (wid & 1) * 128;
  const unsigned dw = lds0 + wid * 1024;
  auto issue_x = [&](int s, int slot) { dma16_dev(rx, vx, sx + ((s + rot) & 15) * 2048, dw + slot * 24576); };
  auto issue_g = [&](int s, int slot) { dma16(rg, vw, sw + ((s + rot) & 15) * (32 * F_ * 2), dw + slot * 24576 + 8192); };
  auto issue_u = [&](int s, int slot) { dma16(ru, vw, sw + ((s + rot) & 15) * (32 * F_ * 2), dw + slot * 24576 + 16384); };
  const u64 tag = read_tag(ws);
  if (wid == 0) wait_flags(ws + WS_GO + b * 128, 1, tag, lane);
  BAR();
  ST(9);
  issue_g(0, 0); issue_u(0, 0); issue_g(1, 1); issue_u(1, 1); issue_g(2, 2); issue_u(2, 2);
  if (wid == 0) wait_flags(ws + WS_FX + rb * 128, 16, tag, lane);
  BAR();
  ST(1);
  issue_x(0, 0); issue_x(1, 1); issue_x(2, 2);
  const lptr px = lds + rh2 * 4096 + lane * 16;
  const lptr pw = lds + 8192 + h * 2048 + (ft >> 1) * 1024 + (ft & 1) * 512 + ((lane & 15) >> 2) * 64 + ((lane >> 4) & 1) * 32 + (lane & 3) * 8;
  f32x16 g0 = {}, g1 = {}, u0 = {}, u1 = {};
  GFrag f0, f1;
#define GU_STEP(c, n, slot, kg, D0, D1, D2) do { \
    const lptr x_ = px + (slot) * 24576 + (kg) * 1024, w_ = pw + (slot) * 24576 + (kg) * 4096; \
    MM(g0, c.g, c.x0); n.x0 = lds_frag(x_); n.g = lds_frag_tr(w_, w_ + 256); \
    MM(g1, c.g, c.x1); n.x1 = lds_frag(x_ + 2048); n.u = lds_frag_tr(w_ + 8192, w_ + 8192 + 256); D0; \
    MM(u0, c.u, c.x0); D1; \
    MM(u1, c.u, c.x1); D2; } while (0)
#define GU_STAGE(s, slot, nslot, W) do { GU_STEP(f0, f1, slot, 1, , , ); WAIT_BAR(W); TRD(s); \
    GU_STEP(f1, f0, nslot, 0, issue_x((s) + 3, slot), issue_g((s) + 3, slot), issue_u((s) + 3, slot)); } while (0)
  WAIT_BAR(2);
  ST(2);
  f0.x0 = lds_frag(px); f0.x1 = lds_frag(px + 2048); f0.g = lds_frag_tr(pw, pw + 256); f0.u = lds_frag_tr(pw + 8192, pw + 8192 + 256);
  GU_STAGE(0, 0, 1, 1);
  for (int s = 1; s < 13; s += 3) { GU_STAGE(s, 1, 2, 3); GU_STAGE(s + 1, 2, 0, 3); GU_STAGE(s + 2, 0, 1, 3); }
  GU_STEP(f0, f1, 1, 1, , , ); WAIT_BAR(3); GU_STEP(f1, f0, 2, 0, , , );
  GU_STEP(f0, f1, 2, 1, , , ); WAIT_BAR(0); GU_STEP(f1, f0, 0, 0, , , );
  GU_STEP(f0, f1, 0, 1, , , );
  MM(g0, f1.g, f1.x0); MM(g1, f1.g, f1.x1); MM(u0, f1.u, f1.x0); MM(u1, f1.u, f1.x1);
  __builtin_amdgcn_s_setprio(2);
  ST(3);
#pragma unroll
  for (int i = 0; i < 16; ++i) {
    g0[i] = g0[i] * u0[i] * __builtin_amdgcn_rcpf(1.0f + __builtin_amdgcn_exp2f(g0[i] * -1.4426950408889634f));
    g1[i] = g1[i] * u1[i] * __builtin_amdgcn_rcpf(1.0f + __builtin_amdgcn_exp2f(g1[i] * -1.4426950408889634f));
  }
  const rsrc_t rh = mk_rsrc(ws + WS_H, 7340032);
  const unsigned ho = (unsigned)((rb * 4 + rh2 * 2) * 112 + fb * 8 + ft * 2) * 1024 + lane * 16;
  BSTORE_DEV(pack8(g0, 0), rh, ho);
  BSTORE_DEV(pack8(g0, 8), rh, ho + 1024);
  BSTORE_DEV(pack8(g1, 0), rh, ho + 112 * 1024);
  BSTORE_DEV(pack8(g1, 8), rh, ho + 113 * 1024);
  __builtin_amdgcn_s_setprio(PRIO_G);
  WAIT_BAR(0);
  ST(4);
  if (wid == 0 && lane == 0) set_flag(ws + WS_FH + (rb * 16 + fb) * 8, tag);
  return tag;
}

struct G64 {
  rsrc_t ra, rw; unsigned va, vw, sa, sw, ldsw; int rot;
  __device__ __forceinline__ int src(int j) const { const int t = j + rot; return t >= 14 ? t - 14 : t; }
  __device__ __forceinline__ void issue_w(int j) const {
    dma16(rw, vw, sw + src(j) * 131072, ldsw + (j & 3) * 2048);
    dma16(rw, vw, sw + src(j) * 131072 + 8192, ldsw + (j & 3) * 2048 + 1024);
  }
};
template <int J>
__device__ __forceinline__ void down_steps(const G64& g, lptr pw, u32x4 (&a0)[4], u32x4 (&a1)[4], f32x16& c00, f32x16& c01, f32x16& c10, f32x16& c11 ST_ARG) {
  constexpr int NJ = 14;
  if constexpr (J < NJ) {
    constexpr int later_pro = (J < 4) ? (3 - J) * 2 : 0;
    constexpr int first = (J < 4) ? 4 : J + 1, last = (J + 3 < NJ - 1) ? J + 3 : NJ - 1;
    constexpr int later_main = (last >= first) ? (last - first + 1) * 4 : 0;
    wait_vm2<later_pro + later_main>(a0[J & 3], a1[J & 3]);
    TRD(20 + J);
    const lptr p = pw + (J & 3) * 2048;
    const bf16x8 w0 = lds_frag_tr(p, p + 1024), w1 = lds_frag_tr(p + 512, p + 1536);
    const bf16x8 x0 = __builtin_bit_cast(bf16x8, a0[J & 3]), x1 = __builtin_bit_cast(bf16x8, a1[J & 3]);
    c00 = MFMA(w0, x0, c00); c01 = MFMA(w1, x0, c01); c10 = MFMA(w0, x1, c10); c11 = MFMA(w1, x1, c11);
    if constexpr (J + 4 < NJ) {
      asm volatile("s_waitcnt lgkmcnt(0)" : "+v"(c00), "+v"(c01), "+v"(c10), "+v"(c11) :: "memory");
      g.issue_w(J + 4);
      BLOAD_DEV(a0[J & 3], g.va, g.ra, g.sa + g.src(J + 4) * 8192);
      BLOAD_DEV(a1[J & 3], g.va, g.ra, g.sa + 112 * 1024 + g.src(J + 4) * 8192);
    }
    down_steps<J + 1>(g, pw, a0, a1, c00, c01, c10, c11 ST_PASS);
  }
}
#define V4(c, g_) (f32x4){c[4 * (g_)], c[4 * (g_) + 1], c[4 * (g_) + 2], c[4 * (g_) + 3]}
__device__ __forceinline__ void sum4(lptr lds, int t, f32x4& s0, f32x4& s1) {
  const LAS float* rd = (const LAS float*)lds + (t >> 3) * 68 + (t & 7) * 8;
#pragma unroll
  for (int p = 0; p < 4; ++p) { s0 += *(const LAS f32x4*)(rd + p * 4352); s1 += *(const LAS f32x4*)(rd + p * 4352 + 4); }
}

__device__ __forceinline__ void out_phase(lptr lds, unsigned lds0, const char* __restrict__ gx, const char* __restrict__ gwd,
                                          const char* __restrict__ gwo, char* __restrict__ ws, char* __restrict__ out,
                                          int b, int wid, int lane, u64 tag ST_ARG) {
  const int xcd = b & 7, bi = b >> 3;
  const int rb = (xcd >> 1) * 8 + (bi & 7);
  const int cb = (xcd & 1) * 4 + (bi >> 3);
  const int r = lane & 31, h = lane >> 5, t = wid * 64 + lane;
  const unsigned vw = ((lane >> 2) & 7) * 1024 + (lane >> 5) * 64 + (lane & 3) * 16;
  const unsigned ptr_off = (4 * h + ((lane & 15) >> 2)) * 64 + ((lane >> 4) & 1) * 32 + (lane & 3) * 8;
  const unsigned off = (unsigned)(rb * 64 + (t >> 3)) * 1024 + cb * 128 + (t & 7) * 16;
  f32x4 s0, s1;
  {
    G64 g;
    g.ra = mk_rsrc(ws + WS_H, 7340032); g.rw = mk_rsrc(gwd, F_ * D_ * 2); g.va = lane * 16; g.vw = vw;
    g.sa = (unsigned)(rb * 2 * 112 + wid) * 1024; g.sw = (unsigned)(wid * 16) * 1024 + cb * 128; g.ldsw = lds0 + wid * 8192;
    g.rot = (((bi >> 3) * 7 + 1) / 2 + (bi & 7) * 2) % 14;
    u32x4 xv;
    BLOAD(xv, off, mk_rsrc(gx, S_ * D_ * 2), 0u);
    g.issue_w(0); g.issue_w(1); g.issue_w(2); g.issue_w(3);
    if (wid == 0) wait_flags(ws + WS_FH + (rb >> 1) * 128, 14, tag, lane);
    BAR();
    ST(5);
    u32x4 a0[4], a1[4];
#pragma unroll
    for (int j = 0; j < 4; ++j) { BLOAD_DEV(a0[j], g.va, g.ra, g.sa + g.src(j) * 8192); BLOAD_DEV(a1[j], g.va, g.ra, g.sa + 112 * 1024 + g.src(j) * 8192); }
    f32x16 c00 = {}, c01 = {}, c10 = {}, c11 = {};
    down_steps<0>(g, lds + wid * 8192 + ptr_off, a0, a1, c00, c01, c10, c11 ST_PASS);
    BAR();
    ST(6);
    LAS f32x4* lin = (LAS f32x4*)(lds + (wid & 3) * 17408) + lane;
    if (wid >= 4) {
#pragma unroll
      for (int i = 0; i < 4; ++i) { lin[i * 64] = V4(c00, i); lin[256 + i * 64] = V4(c01, i); lin[512 + i * 64] = V4(c10, i); lin[768 + i * 64] = V4(c11, i); }
    }
    BAR();
    if (wid < 4) {
      f32x4 tt[16];
#pragma unroll
      for (int i = 0; i < 4; ++i) {
        tt[i] = lin[i * 64] + V4(c00, i); tt[4 + i] = lin[256 + i * 64] + V4(c01, i);
        tt[8 + i] = lin[512 + i * 64] + V4(c10, i); tt[12 + i] = lin[768 + i * 64] + V4(c11, i);
      }
      asm volatile("s_waitcnt lgkmcnt(0)" ::: "memory");
      LAS float* red = (LAS float*)(lds + wid * 17408) + r * 68 + 4 * h;
#pragma unroll
      for (int i = 0; i < 4; ++i) {
        *(LAS f32x4*)(red + 8 * i) = tt[i]; *(LAS f32x4*)(red + 32 + 8 * i) = tt[4 + i];
        *(LAS f32x4*)(red + 32 * 68 + 8 * i) = tt[8 + i]; *(LAS f32x4*)(red + 32 * 68 + 32 + 8 * i) = tt[12 + i];
      }
    }
    BAR();
    wait_vm1<0>(xv);
    s0 = (f32x4){bf_lo(xv[0]), bf_hi(xv[0]), bf_lo(xv[1]), bf_hi(xv[1])};
    s1 = (f32x4){bf_lo(xv[2]), bf_hi(xv[2]), bf_lo(xv[3]), bf_hi(xv[3])};
    sum4(lds, t, s0, s1);
    asm volatile("" : "+v"(s0), "+v"(s1));
    BAR();
    ST(7);
  }
  const rsrc_t rwo = mk_rsrc(gwo, D_ * D_ * 2);
#pragma unroll
  for (int i = 0; i < 8; ++i) dma16(rwo, vw, (unsigned)((wid * 8 + i) * 8) * 1024 + cb * 128, lds0 + (wid * 8 + i) * 1024);
  if (b == 0 && wid == 1) {
#pragma unroll 1
    for (int i = 0; i < 8; ++i) wait_flags(ws + WS_FQ + i * 512, 64, tag, lane);
    if (lane == 0) set_flag(ws + WS_EPOCH, tag);
  }
  if (wid == 0) wait_flags(ws + WS_FO + (rb >> 2) * 256, 32, tag, lane);
  WAIT_BAR(0);
  ST(8);
  const int rt = wid & 1, kq = wid >> 1;
  const rsrc_t ro = mk_rsrc(ws + WS_O, 2097152);
  u32x4 o[8];
#pragma unroll
  for (int j = 0; j < 8; ++j) BLOAD_DEV(o[j], lane * 16, ro, (unsigned)((rb * 2 + rt) * 32 + kq * 8 + j) * 1024);
  const lptr pw = lds + kq * 16384 + ptr_off;
  bf16x8 w0[8], w1[8];
#pragma unroll
  for (int j = 0; j < 4; ++j) { w0[j] = lds_frag_tr(pw + j * 2048, pw + j * 2048 + 1024); w1[j] = lds_frag_tr(pw + j * 2048 + 512, pw + j * 2048 + 1536); }
  f32x16 c0 = {}, c1 = {};
#define WO_STEP(j) do { wait_vm1<7 - (j)>(o[j]); MM(c0, w0[j], __builtin_bit_cast(bf16x8, o[j])); MM(c1, w1[j], __builtin_bit_cast(bf16x8, o[j])); \
    if ((j) < 4) { w0[(j) + 4] = lds_frag_tr(pw + ((j) + 4) * 2048, pw + ((j) + 4) * 2048 + 1024); \
                   w1[(j) + 4] = lds_frag_tr(pw + ((j) + 4) * 2048 + 512, pw + ((j) + 4) * 2048 + 1536); } } while (0)
  WO_STEP(0); WO_STEP(1); WO_STEP(2); WO_STEP(3);
  BAR();
  WO_STEP(4); WO_STEP(5); WO_STEP(6); WO_STEP(7);
  ST(10);
  LAS float* red = (LAS float*)(lds + kq * 17408) + (rt * 32 + r) * 68 + 4 * h;
#pragma unroll
  for (int i = 0; i < 4; ++i) { *(LAS f32x4*)(red + 8 * i) = V4(c0, i); *(LAS f32x4*)(red + 32 + 8 * i) = V4(c1, i); }
  BAR();
  sum4(lds, t, s0, s1);
  *(u32x4*)(out + off) = (u32x4){pk2(s0[0], s0[1]), pk2(s0[2], s0[3]), pk2(s1[0], s1[1]), pk2(s1[2], s1[3])};
}

__global__ __launch_bounds__(512, 4) void k_all(const char* __restrict__ gx, const char* __restrict__ gwqkv, const char* __restrict__ gwo,
                                                const char* __restrict__ gwg, const char* __restrict__ gwu, const char* __restrict__ gwd,
                                                char* __restrict__ ws, char* __restrict__ out) {
  extern __shared__ __attribute__((aligned(16))) char smem[];
  const lptr lds = (lptr)smem;
  const unsigned lds0 = (unsigned)(uintptr_t)lds;
  const int lane = threadIdx.x & 63, wid = RFL(threadIdx.x >> 6), b = blockIdx.x;
  ST_DECL; ST(0);
  if (b < 256) {
    __builtin_amdgcn_s_setprio(PRIO_A);
    const u64 tag = qkv_phase(lds, lds0, gx, gwqkv, ws, b, wid, lane ST_PASS);
    attn_phase(lds, lds0, ws, b, wid, lane, tag ST_PASS);
  } else {
    __builtin_amdgcn_s_setprio(PRIO_G);
    const u64 tag = gateup_phase(lds, lds0, gwg, gwu, gwd, gwo, ws, b - 256, wid, lane ST_PASS);
    out_phase(lds, lds0, gx, gwd, gwo, ws, out, b - 256, wid, lane, tag ST_PASS);
  }
  ST(11); ST_OUT(ws); TR_OUT(ws);
}

extern "C" void kernel_launch(void* const* d_in, const int* in_sizes, int n_in, void* d_out, int out_size, void* d_ws,
                              size_t ws_size, hipStream_t stream) {
  (void)hipFuncSetAttribute((const void*)k_all, hipFuncAttributeMaxDynamicSharedMemorySize, LDS_BYTES);
  k_all<<<512, 512, LDS_BYTES, stream>>>((const char*)d_in[0], (const char*)d_in[1], (const char*)d_in[2], (const char*)d_in[4],
                                         (const char*)d_in[5], (const char*)d_in[6], (char*)d_ws, (char*)d_out);
}
```

```cpp
#include <hip/hip_runtime.h>
#include <hip/hip_bf16.h>
#include <stdint.h>
#include <type_traits>

typedef __attribute__((ext_vector_type(8))) short bf16x8;
typedef __attribute__((ext_vector_type(4))) short s16x4;
typedef __attribute__((ext_vector_type(16))) float f32x16;
typedef __attribute__((ext_vector_type(4))) float f32x4;
typedef __attribute__((ext_vector_type(2))) float f32x2;
typedef __attribute__((ext_vector_type(4))) unsigned u32x4;
typedef __attribute__((ext_vector_type(2))) __bf16 bf2;
typedef unsigned long long u64;
typedef __amdgpu_buffer_rsrc_t rsrc_t;
#define LAS __attribute__((address_space(3)))
#define GAS __attribute__((address_space(1)))
typedef LAS char* lptr;

#define MFMA(a, b, c) __builtin_amdgcn_mfma_f32_32x32x16_bf16((a), (b), (c), 0, 0, 0)
#define MM(c, a, b) do { __builtin_amdgcn_sched_barrier(0); c = MFMA(a, b, c); asm volatile("" : "+v"(c) :: "memory"); \
                         __builtin_amdgcn_sched_barrier(0); } while (0)
#define RFL(x) __builtin_amdgcn_readfirstlane(x)

constexpr int S_ = 2048, D_ = 512, F_ = 1792;
constexpr size_t WS_EPOCH = 0;
constexpr size_t WS_FH = 2048;
constexpr size_t WS_FQ = 16384;
constexpr size_t WS_FX = 49152;
constexpr size_t WS_FO = 32768;
constexpr size_t WS_Q    = 65536;
constexpr size_t WS_KV   = WS_Q + 2097152;
constexpr size_t WS_O    = WS_KV + 4194304;
constexpr size_t WS_XN   = WS_O + 2097152;
constexpr size_t WS_H    = WS_XN + 4194304;
constexpr float QSCALE = 0.25f * 1.4426950408889634f;
constexpr float MBIAS = 80.0f;
constexpr int LDS_BYTES = 163840;
#define W2_AT 7
#define W3_AT 7
#define CPF_LANES 2
#define POLL_SLEEP 4
#define SPIN_CAP (1u << 16)

#define ST_DECL
#define ST(i)
#define STC(i)
#define TR(i)
#define TRD(i)
#define TR_OUT(wsp)
#define ST_OUT(wsp)
#define ST_ARG
#define ST_PASS

__device__ __forceinline__ rsrc_t mk_rsrc(const void* p, unsigned bytes) {
  return __builtin_amdgcn_make_buffer_rsrc((void*)p, (short)0, (int)bytes, 0x00020000);
}
__device__ __forceinline__ void dma16(rsrc_t r, unsigned voff, unsigned soff, unsigned lds) {
  unsigned keep;
  asm volatile("s_mov_b32 %0, m0\n\ts_mov_b32 m0, %1\n\ts_nop 0\n\tbuffer_load_dwordx4 %2, %3, %4 offen lds\n\ts_mov_b32 m0, %0"
               : "=&s"(keep) : "s"(lds), "v"(voff), "s"(r), "s"(soff) : "memory");
}
__device__ __forceinline__ void dma16_dev(rsrc_t r, unsigned voff, unsigned soff, unsigned lds) {
  unsigned keep;
  asm volatile("s_mov_b32 %0, m0\n\ts_mov_b32 m0, %1\n\ts_nop 0\n\tbuffer_load_dwordx4 %2, %3, %4 offen sc1 lds\n\ts_mov_b32 m0, %0"
               : "=&s"(keep) : "s"(lds), "v"(voff), "s"(r), "s"(soff) : "memory");
}
__device__ __forceinline__ void dma16x3(rsrc_t r, unsigned voff, unsigned soff, unsigned lds) {
  unsigned keep;
  asm volatile("s_mov_b32 %0, m0\n\ts_mov_b32 m0, %1\n\ts_nop 0\n\tbuffer_load_dwordx4 %2, %3, %4 offen lds\n\t"
               "buffer_load_dwordx4 %2, %3, %4 offen offset:1024 lds\n\tbuffer_load_dwordx4 %2, %3, %4 offen offset:2048 lds\n\ts_mov_b32 m0, %0"
               : "=&s"(keep) : "s"(lds), "v"(voff), "s"(r), "s"(soff) : "memory");
}
__device__ __forceinline__ float fma1(float a, float b, float c) {
  float d; asm("v_fma_f32 %0, %1, %2, %3" : "=v"(d) : "v"(a), "v"(b), "v"(c)); return d;
}
#define BLOAD(dst, voff, r, soff) asm volatile("buffer_load_dwordx4 %0, %1, %2, %3 offen" : "=v"(dst) : "v"(voff), "s"(r), "s"(soff) : "memory")
#define BLOAD_DEV(dst, voff, r, soff) asm volatile("buffer_load_dwordx4 %0, %1, %2, %3 offen sc1" : "=v"(dst) : "v"(voff), "s"(r), "s"(soff) : "memory")
#define BSTORE_DEV(val, r, voff) __builtin_amdgcn_raw_buffer_store_b128((val), (r), (voff), 0, 16)
#define WAIT_BAR(N) asm volatile("s_waitcnt vmcnt(" #N ") lgkmcnt(0)\n\ts_barrier" ::: "memory")
#define BAR() asm volatile("s_waitcnt lgkmcnt(0)\n\ts_barrier" ::: "memory")
template <int N> __device__ __forceinline__ void wait_bar() { asm volatile("s_waitcnt vmcnt(%0) lgkmcnt(0)\n\ts_barrier" :: "n"(N) : "memory"); }
template <int N> __device__ __forceinline__ void wait_vm2(u32x4& a, u32x4& b) {
  asm volatile("s_waitcnt vmcnt(%2)" : "+v"(a), "+v"(b) : "n"(N) : "memory");
}
template <int N> __device__ __forceinline__ void wait_vm1(u32x4& a) { asm volatile("s_waitcnt vmcnt(%1)" : "+v"(a) : "n"(N) : "memory"); }

__device__ __forceinline__ unsigned pk2(float lo, float hi) {
  f32x2 v = {lo, hi};
  return __builtin_bit_cast(unsigned, __builtin_convertvector(v, bf2));
}
__device__ __forceinline__ float bf_lo(unsigned u) { return __uint_as_float(u << 16); }
__device__ __forceinline__ float bf_hi(unsigned u) { return __uint_as_float(u & 0xffff0000u); }
__device__ __forceinline__ float xhalf_max(float v) {
  auto r = __builtin_amdgcn_permlane32_swap(__float_as_uint(v), __float_as_uint(v), false, false);
  return __builtin_fmaxf(__uint_as_float(r[0]), __uint_as_float(r[1]));
}
__device__ __forceinline__ bf16x8 lds_frag(lptr p) { return *(const LAS bf16x8*)p; }
typedef short v4i16_t __attribute__((ext_vector_type(4)));
__device__ __forceinline__ bf16x8 lds_frag_tr(lptr p0, lptr p1) {
  s16x4 a = __builtin_bit_cast(s16x4, __builtin_amdgcn_ds_read_tr16_b64_v4i16((LAS v4i16_t*)p0));
  s16x4 b = __builtin_bit_cast(s16x4, __builtin_amdgcn_ds_read_tr16_b64_v4i16((LAS v4i16_t*)p1));
  return (bf16x8){a[0], a[1], a[2], a[3], b[0], b[1], b[2], b[3]};
}
__device__ __forceinline__ float dot2(unsigned a, unsigned b, float c) {
  return __builtin_amdgcn_fdot2_f32_bf16(__builtin_bit_cast(bf2, a), __builtin_bit_cast(bf2, b), c, false);
}
__device__ __forceinline__ u32x4 pack8(const f32x16& a, int o) {
  return (u32x4){pk2(a[o], a[o + 1]), pk2(a[o + 2], a[o + 3]), pk2(a[o + 4], a[o + 5]), pk2(a[o + 6], a[o + 7])};
}
template <int CTRL> __device__ __forceinline__ float dpp_add(float x) {
  return x + __builtin_bit_cast(float, __builtin_amdgcn_mov_dpp(__builtin_bit_cast(int, x), CTRL, 0xf, 0xf, true));
}
__device__ __forceinline__ float sum8(float x) { return dpp_add<0x141>(dpp_add<0x4E>(dpp_add<0xB1>(x))); }

__device__ __forceinline__ u64 read_tag(const char* ws) {
  u64 e;
  asm volatile("s_load_dwordx2 %0, %1, 0x0\n\ts_waitcnt lgkmcnt(0)" : "=s"(e) : "s"(ws + WS_EPOCH) : "memory");
  e = (e ^ (e >> 31)) * 0x9E3779B97F4A7C15ull + 0x632BE59BD9B4E019ull;
  return e ^ (e >> 29);
}
__device__ __forceinline__ void set_flag(char* p, u64 tag) { __hip_atomic_store((GAS u64*)p, tag, __ATOMIC_RELAXED, __HIP_MEMORY_SCOPE_AGENT); }
__device__ __forceinline__ void wait_flags(const char* p, int n, u64 tag, int lane) {
  GAS u64* f = (GAS u64*)p + (lane < n ? lane : 0);
#pragma unroll 1
  for (unsigned spins = 0; spins < SPIN_CAP; ++spins) {
    if (__all(__hip_atomic_load(f, __ATOMIC_RELAXED, __HIP_MEMORY_SCOPE_AGENT) == tag)) break;
    __builtin_amdgcn_s_sleep(POLL_SLEEP);
  }
  asm volatile("" ::: "memory");
}

__device__ __forceinline__ void dma16x2(rsrc_t r, unsigned voff, unsigned soff0, unsigned soff1, unsigned lds) {
  unsigned keep;
  asm volatile("s_mov_b32 %0, m0\n\ts_mov_b32 m0, %1\n\ts_nop 0\n\tbuffer_load_dwordx4 %2, %3, %4 offen lds\n\t"
               "buffer_load_dwordx4 %2, %3, %5 offen offset:1024 lds\n\ts_mov_b32 m0, %0"
               : "=&s"(keep) : "s"(lds), "v"(voff), "s"(r), "s"(soff0), "s"(soff1 - 1024u) : "memory");
}
template <int N> __device__ __forceinline__ void wait_vm() { asm volatile("s_waitcnt vmcnt(%0)" :: "n"(N) : "memory"); }
#define V4(c, g_) (f32x4){c[4 * (g_)], c[4 * (g_) + 1], c[4 * (g_) + 2], c[4 * (g_) + 3]}

constexpr int WR = 65536, QSLOT = 24576;
constexpr int REC = 2080, CHUNK = 8 * REC;

struct F4 { bf16x8 x0, x1, w0, w1; };
struct QFrag { bf16x8 x, q, k, v; };

template <int G>
__device__ __forceinline__ void qkv_steps(const lptr (&pa)[4], lptr pw, lptr pw2, QFrag& c, QFrag& n, f32x16& aq, f32x16& ak, f32x16& av,
                                          char* fx, u64 tag, int lane ST_ARG) {
  if constexpr (G < 32) {
    constexpr int s = G >> 2, N = G + 1, ns = N >> 2;
    if constexpr ((G & 3) == 3) {
      if constexpr (s == 0) wait_bar<9>(); else if constexpr (s == 1) wait_bar<4>(); else if constexpr (s == 2) wait_bar<1>(); else BAR();
      if constexpr (s == 1) { if (lane == 0) set_flag(fx, tag); }
    }
    const lptr w = ((ns & 2) ? pw2 : pw) + (ns & 1) * QSLOT + (N & 3) * 6144;
    MM(aq, c.q, c.x);
    if constexpr (N < 32) { n.x = lds_frag(pa[N & 3] + ns * 4096); n.q = lds_frag_tr(w, w + 256); }
    MM(ak, c.k, c.x);
    if constexpr (N < 32) { n.k = lds_frag_tr(w + 1024, w + 1024 + 256); n.v = lds_frag_tr(w + 2048, w + 2048 + 256); }
    MM(av, c.x, c.v);
    qkv_steps<G + 1>(pa, pw, pw2, n, c, aq, ak, av, fx, tag, lane ST_PASS);
  }
}

#define EXP(x) "v_exp_f32 v" #x ", v" #x "\n\t"
#define CVT(d, a, b) "v_cvt_pk_bf16_f32 v" #d ", v" #a ", v" #b "\n\t"
#define STEP(c0, c1, c2, c3, c4, c5, c6, c7, c8, c9, c10, c11, c12, c13, c14, c15, SN, off, H0, H1) \
  H0 \
  "s_waitcnt lgkmcnt(1)\n\t" \
  "v_mfma_f32_32x32x16_bf16 " SN ", v[68:71], v[64:67], v[0:15]\n\t" \
  "ds_read_b128 v[68:71], v88 offset:" off "\n\t" \
  EXP(c0) EXP(c1) EXP(c2) EXP(c3) \
  "v_mfma_f32_32x32x16_bf16 v[48:63], v[76:79], v[84:87], v[48:63]\n\t" \
  "ds_read_b128 v[76:79], v90 offset:" off "\n\t" \
  H1 \
  EXP(c4) EXP(c5) CVT(80, c0, c1) CVT(81, c2, c3) EXP(c6) EXP(c7) EXP(c8) EXP(c9) CVT(82, c4, c5) CVT(83, c6, c7) \
  EXP(c10) EXP(c11) \
  "s_waitcnt lgkmcnt(2)\n\t" \
  "v_mfma_f32_32x32x16_bf16 v[48:63], v[72:75], v[80:83], v[48:63]\n\t" \
  "ds_read_b128 v[72:75], v89 offset:" off "\n\t" \
  EXP(c12) EXP(c13) CVT(84, c8, c9) CVT(85, c10, c11) EXP(c14) EXP(c15) CVT(86, c12, c13) CVT(87, c14, c15)
#define STEP_A(off, H0, H1) STEP(16, 17, 18, 19, 20, 21, 22, 23, 24, 25, 26, 27, 28, 29, 30, 31, "v[32:47]", off, H0, H1)
#define STEP_B(off, H0, H1) STEP(32, 33, 34, 35, 36, 37, 38, 39, 40, 41, 42, 43, 44, 45, 46, 47, "v[16:31]", off, H0, H1)
#define PAIR(base, o0, o1, A0, A1, B0, B1) STEP_A(base "+" #o0, A0, A1) STEP_B(base "+" #o1, B0, B1)
#define P_NONE(base, o0, o1) PAIR(base, o0, o1, "", "", "", "")
#define P_KV(base, o0, o1, m, so) PAIR(base, o0, o1, "s_mov_b32 m0, %[" #m "]\n\t", \
    "buffer_load_dwordx4 %[voff], %[rkv], %[" #so "] offen sc1 lds\n\t", "", "buffer_load_dwordx4 %[voff], %[rkv], %[" #so "] offen offset:1024 sc1 lds\n\t")
#define P_WD(base, o0, o1, m, so) PAIR(base, o0, o1, "s_mov_b32 m0, %[" #m "]\n\t", \
    "buffer_load_dwordx4 %[vw1], %[rwd], %[" #so "] offen lds\n\t", "s_add_u32 %[t0], %[" #so "], 7168\n\t", \
    "buffer_load_dwordx4 %[vw1], %[rwd], %[t0] offen offset:1024 lds\n\t")
#define P_H(base, o0, o1, da, db, so) PAIR(base, o0, o1, "", "buffer_load_dwordx4 %[" #da "], %[voff], %[rh], %[" #so "] offen sc1\n\t", \
    "", "buffer_load_dwordx4 %[" #db "], %[voff], %[rh], %[" #so "] offen offset:1024 sc1\n\t")
#define ATT_BEGIN "s_mov_b32 %[keep], m0\n\ts_nop 3\n\t"
#define ATT_END "s_mov_b32 m0, %[keep]\n\ts_waitcnt lgkmcnt(0)\n\ts_nop 3\n\t"
#define ATT_STATE "+{v[48:63]}"(O), "+{v[16:31]}"(SA), "+{v[32:47]}"(SB), "+{v[68:71]}"(K), "+{v[72:75]}"(V0), "+{v[76:79]}"(V1), \
                  "+{v[84:87]}"(P1), [t0] "=&s"(t0), [keep] "=&s"(keep)
#define ATT_IN "{v[0:15]}"(C), "{v[64:67]}"(Q), "{v88}"(aK), "{v89}"(aV0), "{v90}"(aV1), [voff] "v"(voff)
#define ATT_CLOB "v80", "v81", "v82", "v83", "scc", "memory"

__device__ __forceinline__ void sum4(lptr lds, int t, f32x4& s0, f32x4& s1) {
  const LAS float* rd = (const LAS float*)lds + (t >> 3) * 68 + (t & 7) * 8;
#pragma unroll
  for (int p = 0; p < 4; ++p) { s0 += *(const LAS f32x4*)(rd + p * 4352); s1 += *(const LAS f32x4*)(rd + p * 4352 + 4); }
}

__global__ __launch_bounds__(512, 2) void k_all(const char* __restrict__ gx, const char* __restrict__ gwqkv, const char* __restrict__ gwo,
                                                const char* __restrict__ gwg, const char* __restrict__ gwu, const char* __restrict__ gwd,
                                                char* __restrict__ ws, char* __restrict__ out) {
  extern __shared__ __attribute__((aligned(16))) char smem[];
  const lptr lds = (lptr)smem;
  const unsigned lds0 = (unsigned)(uintptr_t)lds;
  const int lane = threadIdx.x & 63, wid = RFL(threadIdx.x >> 6), b = blockIdx.x;
  const int xcd = b & 7, bi = b >> 3;
  const int rb = (xcd >> 1) * 8 + (bi & 7);
  const int cb = (xcd & 1) * 4 + (bi >> 3);
  const int rot = (rb + 2 * cb) & 7;
  const int head = xcd * 4 + (bi >> 3), qt = (bi & 7) * 8 + wid;
  const int r = lane & 31, h = lane >> 5;
  const unsigned trn = ((lane & 15) >> 2) * 64 + ((lane >> 4) & 1) * 32 + (lane & 3) * 8;
  const unsigned voff = lane * 16;
  ST_DECL; ST(0);

  const rsrc_t rx = mk_rsrc(gx, S_ * D_ * 2), rw = mk_rsrc(gwqkv, D_ * 3 * D_ * 2), rxn = mk_rsrc(ws + WS_XN + (cb >> 2) * 2097152, 2097152);
  u32x4 xr[8];
  {
    const unsigned vx = (lane >> 3) * 1024 + (lane & 7) * 16, sx = (unsigned)(rb * 64 + wid * 8) * 1024;
#pragma unroll
    for (int i = 0; i < 8; ++i) BLOAD(xr[i], vx, rx, sx + ((i + rot) & 7) * 128);
  }
  unsigned cpf;
  {
    u64 pc; unsigned len;
    asm volatile("s_getpc_b64 %0\nLk_here:\n\ts_mov_b32 %1, Lk_tail-Lk_here" : "=s"(pc), "=s"(len));
    const unsigned o = (unsigned)((bi * 8 + wid) * CPF_LANES + (lane < CPF_LANES ? lane : 0)) * 128;
    asm volatile("global_load_dword %0, %1, off" : "=v"(cpf) : "v"((const char*)pc + (o + 4 <= len ? o : 0)) : "memory");
  }
  BAR();
  const unsigned vw3 = ((lane >> 2) & 7) * 3072 + (lane >> 5) * 64 + (lane & 3) * 16;
  auto issue_wk = [&](int s, int kb) {
    dma16x3(rw, vw3, (unsigned)(kb * 8) * 3072 + cb * 128 + ((s + rot) & 7) * (64 * 3072), lds0 + WR + (s & 3) * QSLOT + kb * 3072);
  };
  auto issue_w = [&](int s) { issue_wk(s, wid); };
  issue_w(0); issue_w(1);
  const u64 tag = read_tag(ws);
  float rs, mr;
  {
    const unsigned ONE2 = 0x3f803f80u;
    float s0 = 0.f, s1 = 0.f, q0 = 0.f, q1 = 0.f;
#define STAT(i) do { wait_vm1<14 - (i)>(xr[i]); \
      s0 = dot2(xr[i][0], ONE2, s0); q0 = dot2(xr[i][0], xr[i][0], q0); s1 = dot2(xr[i][1], ONE2, s1); q1 = dot2(xr[i][1], xr[i][1], q1); \
      s0 = dot2(xr[i][2], ONE2, s0); q0 = dot2(xr[i][2], xr[i][2], q0); s1 = dot2(xr[i][3], ONE2, s1); q1 = dot2(xr[i][3], xr[i][3], q1); \
      asm volatile("" : "+v"(s0), "+v"(s1), "+v"(q0), "+v"(q1)); } while (0)
    STAT(0); STAT(1); STAT(2); STAT(3); STAT(4); STAT(5); STAT(6); STAT(7);
    ST(1);
    const float mean = sum8(s0 + s1) * (1.0f / 512.0f);
    rs = __builtin_amdgcn_rsqf(__builtin_fmaxf(sum8(q0 + q1) * (1.0f / 512.0f) - mean * mean, 0.0f) + 1e-5f);
    mr = -mean * rs;
  }
  const unsigned xo = (lane & 7) * 512 + ((((wid & 3) * 8 + (lane >> 3)) ^ (lane & 7)) * 16);
  const lptr pxw = lds + (wid >> 2) * 32768 + xo;
#pragma unroll
  for (int j = 0; j < 8; ++j) {
    u32x4 n;
#pragma unroll
    for (int i = 0; i < 4; ++i) n[i] = pk2(__builtin_fmaf(bf_lo(xr[j][i]), rs, mr), __builtin_fmaf(bf_hi(xr[j][i]), rs, mr));
    if (j < 2) BSTORE_DEV(n, rxn, (unsigned)(rb * 8 + ((rot + j) & 7)) * 8192 + (wid >> 2) * 4096 + xo);
    *(LAS u32x4*)(pxw + j * 4096) = n;
    if (j == W2_AT) issue_w(2);
    if (j == W3_AT) issue_w(3);
  }
  unsigned pf;
  {
    const int t = (b * 8 + wid) * 7 + (r < 7 ? r : 0);
    asm volatile("global_load_dword %0, %1, off" : "=v"(pf) : "v"((h ? gwu : gwg) + (size_t)t * 128) : "memory");
  }
  const bool has_ffn = cb < 7;
  const int fb = cb * 2 + (rb & 1);
  const rsrc_t rg = mk_rsrc(gwg, D_ * F_ * 2), ru = mk_rsrc(gwu, D_ * F_ * 2);
  const unsigned vwf = ((lane >> 2) & 7) * (F_ * 2) + (lane >> 5) * 64 + (lane & 3) * 16;
  auto gu_so = [&](int s, int pc) { return (unsigned)((pc >> 1) * 8) * (F_ * 2) + fb * 256 + (pc & 1) * 128 + (unsigned)((s + 2 * rot) & 15) * (32 * F_ * 2); };
  auto issue_gp = [&](int s, int slot, int pc) { dma16(rg, vwf, gu_so(s, pc), lds0 + WR + slot * QSLOT + pc * 1024); };
  auto issue_up = [&](int s, int slot, int pc) { dma16(ru, vwf, gu_so(s, pc), lds0 + WR + slot * QSLOT + 8192 + pc * 1024); };
  auto issue_xpp = [&](int u, int pc) {
    dma16_dev(rxn, voff, (unsigned)((rb ^ 1) * 8 + ((u + rot) & 7)) * 8192 + pc * 1024, lds0 + WR + (u & 3) * QSLOT + 16384 + pc * 1024);
  };
  auto issue_g = [&](int s, int slot) { issue_gp(s, slot, wid); };
  auto issue_u = [&](int s, int slot) { issue_up(s, slot, wid); };
  auto issue_xp = [&](int u) { issue_xpp(u, wid); };
  u64 fl;
  lptr pa[4];
#pragma unroll
  for (int g = 0; g < 4; ++g) pa[g] = lds + (2 * g + h) * 512 + ((r ^ (2 * g + h)) * 16);
  char* const fx_flag = ws + WS_FX + (rb * 8 + cb) * 8;
  const rsrc_t rq = mk_rsrc(ws + WS_Q, 2097152 + 4194304);
  u32x4 qs[6];
  unsigned ko, vo0, vo1, qo;
  WAIT_BAR(12);
  ST(2);
  if (wid < 4) {
    const int rt = wid & 1, ch = wid >> 1;
    lptr par[4];
#pragma unroll
    for (int g = 0; g < 4; ++g) par[g] = pa[g] + rt * 32768;
    lptr pw = lds + WR + h * 3072 + ch * 512 + trn, pw2 = pw + 2 * QSLOT;
    asm volatile("" : "+v"(pw2));
    const int pair = cb * 2 + ch, tile = rb * 2 + rt;
    f32x16 aq = {}, ak = {}, av = {};
    QFrag f0, f1;
    f0.x = lds_frag(par[0]); f0.q = lds_frag_tr(pw, pw + 256); f0.k = lds_frag_tr(pw + 1024, pw + 1024 + 256); f0.v = lds_frag_tr(pw + 2048, pw + 2048 + 256);
    qkv_steps<0>(par, pw, pw2, f0, f1, aq, ak, av, fx_flag, tag, threadIdx.x ST_PASS);
    ko = 2097152 + (unsigned)((2 * pair) * 64 + ((tile + 62) & 63)) * 2048 + voff;
    const unsigned vo = 2097152 + (unsigned)(2 * pair + (r >> 4)) * 131072 + h * 256 + (r & 15) * 16;
    vo0 = vo + ((tile + 63) & 63) * 2048 + 1024; vo1 = vo + tile * 2048 + 1536;
    qo = (unsigned)((2 * pair) * 64 + tile) * 1024 + voff;
#pragma unroll
    for (int i = 0; i < 16; ++i) aq[i] *= QSCALE;
    qs[0] = pack8(ak, 0); qs[1] = pack8(ak, 8); qs[2] = pack8(av, 0); qs[3] = pack8(av, 8); qs[4] = pack8(aq, 0); qs[5] = pack8(aq, 8);
  } else {
    const int p0 = (wid - 4) * 2, p1 = p0 + 1;
    auto w2 = [&](int s) { issue_wk(s, p0); issue_wk(s, p1); };
    auto gu2 = [&](int s) { issue_gp(s, s, p0); issue_gp(s, s, p1); issue_up(s, s, p0); issue_up(s, s, p1); };
    auto xp2 = [&](int u) { issue_xpp(u, p0); issue_xpp(u, p1); };
    WAIT_BAR(9);  TR(16); w2(4);
    WAIT_BAR(10); TR(17); w2(5);
    WAIT_BAR(13); TR(18); w2(6);
    WAIT_BAR(12); TR(19); w2(7);
    WAIT_BAR(12); TR(20);
    asm volatile("global_load_dwordx2 %0, %1, off sc1" : "=v"(fl) : "v"(ws + WS_FX + ((rb ^ 1) * 8 + (cb & 4) + (lane & 3)) * 8) : "memory");
    gu2(0);
    WAIT_BAR(11); TR(21); gu2(1);
    asm volatile("s_waitcnt vmcnt(8) lgkmcnt(0)\n\ts_barrier" : "+v"(fl) :: "memory");
    TR(22);
    const bool xok = __all(fl == tag);
    if (xok) xp2(0);
    gu2(2);
    BAR();      TR(23);
    if (xok) { xp2(1); wait_vm<6>(); }
    else { wait_flags(ws + WS_FX + ((rb ^ 1) * 8 + (cb & 4)) * 8, 4, tag, lane); xp2(0); xp2(1); wait_vm<2>(); }
    TR(24);
  }
  auto qstore = [&](int i) {
    if (wid < 4) BSTORE_DEV(qs[i], rq, i == 0 ? ko : i == 1 ? ko + 131072 : i == 2 ? vo0 : i == 3 ? vo1 : i == 4 ? qo : qo + 65536);
  };
  asm volatile("" :: "v"(pf), "v"(cpf));
  ST(3);
  char* const fq_flag = ws + WS_FQ + (cb * 32 + rb) * 8;

  f32x16 g0, g1, u0, u1;
  const int rh2 = wid & 1, ft = wid >> 1;
  if (has_ffn) {
    g0 = g1 = u0 = u1 = (f32x16){};
    const lptr pg = lds + WR + h * 2048 + (ft >> 1) * 1024 + (ft & 1) * 512 + trn;
    const unsigned xrt = rh2 ? 4096 : 32768;
    auto x_off = [&](int u) { return rh2 ? (unsigned)(WR + (u & 3) * QSLOT + 16384) : (unsigned)(u * 4096); };
    F4 f0, f1;
#define GU_READ(n, s, slot, kg) do { const lptr x_ = pa[((slot) & 1) * 2 + (kg)] + x_off((s) >> 1), w_ = pg + (slot) * QSLOT + (kg) * 4096; \
      n.x0 = lds_frag(x_); n.w0 = lds_frag_tr(w_, w_ + 256); n.x1 = lds_frag(x_ + xrt); n.w1 = lds_frag_tr(w_ + 8192, w_ + 8192 + 256); } while (0)
#define GU_STEP(c, RD, D0, D1, D2) do { \
      MM(g0, c.w0, c.x0); RD; MM(g1, c.w0, c.x1); D0; MM(u0, c.w1, c.x0); D1; MM(u1, c.w1, c.x1); D2; } while (0)
#define ABL_FILL 1
#define GU_BAR(W) wait_bar<W>()
#define GU_STAGE(s, slot, W, FILL_, XA, XB) do { const bool FILL = FILL_ && ABL_FILL; GU_STEP(f0, GU_READ(f1, s, slot, 1), , , XA); GU_BAR(W); \
      GU_STEP(f1, GU_READ(f0, (s) + 1, ((slot) + 1) & 3, 0), if (FILL) issue_g((s) + 4, slot), if (FILL) issue_u((s) + 4, slot), XB); } while (0)
    BAR();
    GU_READ(f0, 0, 0, 0);
    GU_STEP(f0, GU_READ(f1, 0, 0, 1), issue_g(3, 3), issue_u(3, 3), issue_xp(2)); BAR();
    GU_STEP(f1, GU_READ(f0, 1, 1, 0), issue_g(4, 0), issue_u(4, 0), qstore(0));
    GU_STAGE(1, 1, 6, true, issue_xp(3); qstore(1), issue_xp(4); qstore(2));
    GU_STAGE(2, 2, 7, true, qstore(3), qstore(4)); GU_STAGE(3, 3, 5, true, qstore(5), issue_xp(5));
    GU_STAGE(4, 0, 5, true, , ); GU_STAGE(5, 1, 5, true, , issue_xp(6));
    if (wid == 0 && lane == 0) set_flag(fq_flag, tag);
    GU_STAGE(6, 2, 5, true, , ); GU_STAGE(7, 3, 5, true, , issue_xp(7));
    GU_STAGE(8, 0, 5, true, , ); GU_STAGE(9, 1, 4, true, , ); GU_STAGE(10, 2, 4, true, , ); GU_STAGE(11, 3, 4, true, , );
    GU_STAGE(12, 0, 4, false, , ); GU_STAGE(13, 1, 0, false, , );
    asm volatile("global_load_dwordx2 %0, %1, off sc1" : "=v"(fl) : "v"(ws + WS_FQ + ((head >> 2) * 32 + r) * 8) : "memory");
    GU_STEP(f0, GU_READ(f1, 14, 2, 1), , , ); GU_STEP(f1, GU_READ(f0, 15, 3, 0), , , );
    GU_STEP(f0, GU_READ(f1, 15, 3, 1), , , );
    GU_STEP(f1, , , , );
    asm volatile("s_waitcnt vmcnt(0)" : "+v"(fl) :: "memory");
    ST(4);
  } else {
    qstore(0); qstore(1); qstore(2); qstore(3); qstore(4); qstore(5);
    WAIT_BAR(0);
    if (wid == 0 && lane == 0) set_flag(fq_flag, tag);
    fl = ~tag;
    unsigned d[2];
#pragma unroll
    for (int i = 0; i < 2; ++i) {
      const int t = ((rb * 8 + wid) * 2 + i) * 36 + (lane < 36 ? lane : 0);
      const char* p = (t < 14336) ? gwd + (size_t)t * 128 : gwo + (size_t)(t - 14336) * 128;
      asm volatile("global_load_dword %0, %1, off" : "=v"(d[i]) : "v"(p) : "memory");
    }
    asm volatile("s_waitcnt vmcnt(0)" : "+v"(d[0]), "+v"(d[1]) :: "memory");
  }

  const rsrc_t rh = mk_rsrc(ws + WS_H, 7340032);
  auto swiglu = [&] {
#pragma unroll
    for (int i = 0; i < 16; ++i) {
      g0[i] = g0[i] * u0[i] * __builtin_amdgcn_rcpf(1.0f + __builtin_amdgcn_exp2f(g0[i] * -1.4426950408889634f));
      g1[i] = g1[i] * u1[i] * __builtin_amdgcn_rcpf(1.0f + __builtin_amdgcn_exp2f(g1[i] * -1.4426950408889634f));
    }
    const unsigned ho = (unsigned)(((rb ^ rh2) * 2) * 112 + fb * 8 + ft * 2) * 1024 + voff;
    BSTORE_DEV(pack8(g0, 0), rh, ho);
    BSTORE_DEV(pack8(g0, 8), rh, ho + 1024);
    BSTORE_DEV(pack8(g1, 0), rh, ho + 112 * 1024);
    BSTORE_DEV(pack8(g1, 8), rh, ho + 113 * 1024);
  };
  const bool late_h = has_ffn;
  if (!__all(fl == tag)) wait_flags(ws + WS_FQ + (head >> 2) * 256, 32, tag, lane);
  BAR();
  ST(5);
  const rsrc_t rkv = mk_rsrc(ws + WS_KV + (size_t)head * 131072, 131072);
  const unsigned vvo = h * 256 + (r & 15) * 16;
  const bf16x8 ONES = {0x3f80, 0x3f80, 0x3f80, 0x3f80, 0x3f80, 0x3f80, 0x3f80, 0x3f80};
  const bf16x8 PAD = ((r & 27) == 16) ? ONES : (bf16x8){};
  u32x4 Qu, K0u, Ku, V0u;
  BLOAD_DEV(Qu, voff, mk_rsrc(ws + WS_Q, 2097152), (unsigned)(head * 64 + qt) * 1024);
  BLOAD_DEV(K0u, voff, rkv, 62u * 2048);
  BLOAD_DEV(Ku, voff, rkv, 63u * 2048);
  BLOAD_DEV(V0u, vvo, rkv, 63u * 2048 + 1024);
  const unsigned kvs = wid * 2048, kvm = lds0 + wid * REC;
  dma16_dev(rkv, voff, kvs, kvm); dma16_dev(rkv, voff, kvs + 1024, kvm + 1024);
  dma16_dev(rkv, voff, kvs + 16384, kvm + CHUNK); dma16_dev(rkv, voff, kvs + 16384 + 1024, kvm + CHUNK + 1024);
  if (wid == 7) *(LAS bf16x8*)(lds + r * REC + 2048 + h * 16) = h ? (bf16x8){} : ONES;
  if (late_h) swiglu();
  ST(6);
  asm volatile("s_waitcnt vmcnt(4)" : "+v"(Qu), "+v"(K0u), "+v"(Ku), "+v"(V0u) :: "memory");
  const bf16x8 Q = __builtin_bit_cast(bf16x8, Qu);
  bf16x8 K = __builtin_bit_cast(bf16x8, Ku), V0 = (r >= 16) ? PAD : __builtin_bit_cast(bf16x8, V0u);
  f32x16 SA, SB, C, O = {};
  {
    const f32x16 z = {};
    SA = MFMA(__builtin_bit_cast(bf16x8, K0u), Q, z);
    SB = MFMA(K, Q, z);
  }
  float m = SA[0];
#pragma unroll
  for (int i = 1; i < 16; ++i) m = __builtin_fmaxf(m, SA[i]);
#pragma unroll
  for (int i = 0; i < 16; ++i) m = __builtin_fmaxf(m, SB[i]);
  m = xhalf_max(m) + MBIAS;
#pragma unroll
  for (int i = 0; i < 16; ++i) { C[i] = -m; SA[i] -= m; }
  bf16x8 V1 = {}, P1 = {};
  const unsigned aK = lds0 + voff;
  const unsigned apad = ((r & 27) == 16) ? 2048 : 2064;
  const unsigned aV0 = lds0 + (r < 16 ? 1024 + vvo : apad);
  const unsigned aV1 = lds0 + (r < 16 ? 1536 + vvo : apad);
  unsigned t0, keep;
  const int rt = wid & 1, kq = wid >> 1;
  const int drot = (((bi >> 3) * 7 + 1) / 2 + (bi & 7) * 2) % 14;
  const rsrc_t rwd = mk_rsrc(gwd, F_ * D_ * 2), rwo = mk_rsrc(gwo, D_ * D_ * 2);
  const unsigned vw1 = ((lane >> 2) & 7) * 1024 + (lane >> 5) * 64 + (lane & 3) * 16;
  auto dsrc = [&](int st) { const int t = st + drot; return t >= 14 ? t - 14 : t; };
  auto d_so = [&](int st) { return (unsigned)((st < 14 ? dsrc(st) : st - 14) * 128 + wid * 16) * 1024 + cb * 128; };
  auto d_m = [&](int st) { return lds0 + ((st + 5) % 10) * 16384 + wid * 2048; };
  auto issue_d = [&](int st) { dma16x2(st < 14 ? rwd : rwo, vw1, d_so(st), d_so(st) + 8192, d_m(st)); };
  auto h_so = [&](int st) { return (unsigned)((rb * 2 + rt) * 112 + dsrc(st) * 8 + kq * 2) * 1024; };
  const int t = threadIdx.x;
  const unsigned off = (unsigned)(rb * 64 + (t >> 3)) * 1024 + cb * 128 + (t & 7) * 16;
  u32x4 ha[14], hb[14], xv;
  if (!late_h) wait_vm<2>();
  BAR();
  ST(7); STC(14);
  asm volatile(ATT_BEGIN P_KV("0", 0, 2080, m0_, s0_) P_KV("0", 4160, 6240, m1_, s1_) P_NONE("0", 8320, 10400) P_NONE("0", 12480, 14560) ATT_END
               : ATT_STATE : ATT_IN, [rkv] "s"(rkv), [m0_] "s"(kvm + 2 * CHUNK), [s0_] "s"(kvs + 2 * 16384), [m1_] "s"(kvm + 3 * CHUNK), [s1_] "s"(kvs + 3 * 16384) : ATT_CLOB);
  WAIT_BAR(4);
  if (wid == 0 && lane == 0) set_flag(ws + WS_FH + (rb * 8 + cb) * 8, tag);
  asm volatile(ATT_BEGIN P_KV("16640", 0, 2080, m0_, s0_) P_WD("16640", 4160, 6240, m1_, s1_) P_WD("16640", 8320, 10400, m2_, s2_) P_WD("16640", 12480, 14560, m3_, s3_) ATT_END
               : ATT_STATE : ATT_IN, [rkv] "s"(rkv), [rwd] "s"(rwd), [vw1] "v"(vw1), [m0_] "s"(kvm), [s0_] "s"(kvs + 4 * 16384),
                 [m1_] "s"(d_m(0)), [s1_] "s"(d_so(0)), [m2_] "s"(d_m(1)), [s2_] "s"(d_so(1)), [m3_] "s"(d_m(2)), [s3_] "s"(d_so(2)) : ATT_CLOB);
  WAIT_BAR(8);
  asm volatile(ATT_BEGIN P_KV("33280", 0, 2080, m0_, s0_) P_WD("33280", 4160, 6240, m1_, s1_) P_WD("33280", 8320, 10400, m2_, s2_)
               PAIR("33280", 12480, 14560, "", "global_load_dwordx2 %[fl], %[fa], off sc1\n\t", "", "") ATT_END
               : ATT_STATE, [fl] "=&v"(fl) : ATT_IN, [rkv] "s"(rkv), [rwd] "s"(rwd), [vw1] "v"(vw1), [m0_] "s"(kvm + CHUNK), [s0_] "s"(kvs + 5 * 16384),
                 [m1_] "s"(d_m(3)), [s1_] "s"(d_so(3)), [m2_] "s"(d_m(4)), [s2_] "s"(d_so(4)),
                 [fa] "v"(ws + WS_FH + ((rb >> 1) * 16 + (lane & 15)) * 8) : ATT_CLOB);
  asm volatile("s_waitcnt vmcnt(0) lgkmcnt(0)\n\ts_barrier" : "+v"(fl) :: "memory");
  if (!__all(fl == tag)) wait_flags(ws + WS_FH + (rb >> 1) * 128, 16, tag, lane);
#define H_OUT(n, st) [a##n] "=&v"(ha[st]), [b##n] "=&v"(hb[st])
  asm volatile(ATT_BEGIN P_KV("49920", 0, 2080, m0_, s0_) P_H("49920", 4160, 6240, a1, b1, s1_) P_H("49920", 8320, 10400, a2, b2, s2_) P_H("49920", 12480, 14560, a3, b3, s3_) ATT_END
               : ATT_STATE, H_OUT(1, 0), H_OUT(2, 1), H_OUT(3, 2) : ATT_IN, [rkv] "s"(rkv), [rh] "s"(rh), [m0_] "s"(kvm + 2 * CHUNK), [s0_] "s"(kvs + 6 * 16384),
                 [s1_] "s"(h_so(0)), [s2_] "s"(h_so(1)), [s3_] "s"(h_so(2)) : ATT_CLOB);
  BAR();
  asm volatile(ATT_BEGIN P_KV("0", 0, 2080, m0_, s0_) P_H("0", 4160, 6240, a1, b1, s1_) P_H("0", 8320, 10400, a2, b2, s2_) P_H("0", 12480, 14560, a3, b3, s3_) ATT_END
               : ATT_STATE, H_OUT(1, 3), H_OUT(2, 4), H_OUT(3, 5) : ATT_IN, [rkv] "s"(rkv), [rh] "s"(rh), [m0_] "s"(kvm + 3 * CHUNK), [s0_] "s"(kvs + 7 * 16384),
                 [s1_] "s"(h_so(3)), [s2_] "s"(h_so(4)), [s3_] "s"(h_so(5)) : ATT_CLOB);
  asm volatile(ATT_BEGIN P_H("16640", 0, 2080, a0, b0, s0_) P_H("16640", 4160, 6240, a1, b1, s1_) P_H("16640", 8320, 10400, a2, b2, s2_) P_H("16640", 12480, 14560, a3, b3, s3_) ATT_END
               : ATT_STATE, H_OUT(0, 6), H_OUT(1, 7), H_OUT(2, 8), H_OUT(3, 9) : ATT_IN, [rh] "s"(rh),
                 [s0_] "s"(h_so(6)), [s1_] "s"(h_so(7)), [s2_] "s"(h_so(8)), [s3_] "s"(h_so(9)) : ATT_CLOB);
  WAIT_BAR(8);
  asm volatile(ATT_BEGIN P_H("33280", 0, 2080, a0, b0, s0_) P_H("33280", 4160, 6240, a1, b1, s1_) P_H("33280", 8320, 10400, a2, b2, s2_) P_H("33280", 12480, 14560, a3, b3, s3_) ATT_END
               : ATT_STATE, H_OUT(0, 10), H_OUT(1, 11), H_OUT(2, 12), H_OUT(3, 13) : ATT_IN, [rh] "s"(rh),
                 [s0_] "s"(h_so(10)), [s1_] "s"(h_so(11)), [s2_] "s"(h_so(12)), [s3_] "s"(h_so(13)) : ATT_CLOB);
  BAR();
  BLOAD(xv, off, rx, 0u);
  asm volatile(ATT_BEGIN P_WD("49920", 0, 2080, m0_, s0_) P_WD("49920", 4160, 6240, m1_, s1_) P_WD("49920", 8320, 10400, m2_, s2_) P_NONE("49920", 12480, 14560) ATT_END
               : ATT_STATE : ATT_IN, [rwd] "s"(rwd), [vw1] "v"(vw1), [m0_] "s"(d_m(5)), [s0_] "s"(d_so(5)), [m1_] "s"(d_m(6)), [s1_] "s"(d_so(6)),
                 [m2_] "s"(d_m(7)), [s2_] "s"(d_so(7)) : ATT_CLOB);
  asm volatile("s_waitcnt vmcnt(0)" : "+v"(ha[0]), "+v"(ha[1]), "+v"(ha[2]), "+v"(ha[3]), "+v"(ha[4]), "+v"(ha[5]), "+v"(ha[6]), "+v"(ha[7]),
               "+v"(ha[8]), "+v"(ha[9]), "+v"(ha[10]), "+v"(ha[11]), "+v"(ha[12]), "+v"(ha[13]), "+v"(xv) :: "memory");
  asm volatile("" : "+v"(hb[0]), "+v"(hb[1]), "+v"(hb[2]), "+v"(hb[3]), "+v"(hb[4]), "+v"(hb[5]), "+v"(hb[6]), "+v"(hb[7]),
               "+v"(hb[8]), "+v"(hb[9]), "+v"(hb[10]), "+v"(hb[11]), "+v"(hb[12]), "+v"(hb[13]) :: "memory");
  O = MFMA(V1, P1, O);
  ST(8); STC(15);
  {
    float l = O[8];
    float omax = 0.f;
#pragma unroll
    for (int i = 0; i < 8; ++i) omax = __builtin_fmaxf(omax, __builtin_fabsf(O[i]));
    bool bad = !(l < 0x1p120f) || !(xhalf_max(omax) > 0x1p-100f);
#pragma unroll
    for (int i = 0; i < 8; ++i) bad = bad || !(__builtin_fabsf(O[i]) < 0x1p126f);
    if (__builtin_expect(__any(bad), 0)) {
      const f32x16 z = {};
      float mt = -__builtin_inff();
#pragma unroll 1
      for (int tt = 0; tt < 64; ++tt) {
        const f32x16 s = MFMA(__builtin_bit_cast(bf16x8, __builtin_amdgcn_raw_buffer_load_b128(rkv, voff, tt * 2048, 16)), Q, z);
#pragma unroll
        for (int i = 0; i < 16; ++i) mt = __builtin_fmaxf(mt, s[i]);
      }
      mt = xhalf_max(mt);
      O = z;
#pragma unroll 1
      for (int tt = 0; tt < 64; ++tt) {
        f32x16 s = MFMA(__builtin_bit_cast(bf16x8, __builtin_amdgcn_raw_buffer_load_b128(rkv, voff, ((tt + 62) & 63) * 2048, 16)), Q, z);
        bf16x8 v0 = __builtin_bit_cast(bf16x8, __builtin_amdgcn_raw_buffer_load_b128(rkv, vvo, ((tt + 63) & 63) * 2048 + 1024, 16));
        bf16x8 v1 = __builtin_bit_cast(bf16x8, __builtin_amdgcn_raw_buffer_load_b128(rkv, vvo, tt * 2048 + 1536, 16));
        if (r >= 16) { v0 = PAD; v1 = PAD; }
#pragma unroll
        for (int i = 0; i < 16; ++i) s[i] = __builtin_amdgcn_exp2f(s[i] - mt);
        O = MFMA(v0, __builtin_bit_cast(bf16x8, pack8(s, 0)), O);
        O = MFMA(v1, __builtin_bit_cast(bf16x8, pack8(s, 8)), O);
      }
      l = O[8];
    }
    const float il = 1.0f / l;
#pragma unroll
    for (int i = 0; i < 8; ++i) O[i] *= il;
  }
  const rsrc_t ro = mk_rsrc(ws + WS_O, 2097152);
  BSTORE_DEV(pack8(O, 0), ro, (unsigned)(qt * 32 + head) * 1024 + voff);

  f32x16 d0 = {}, d1 = {};
  const lptr pd = lds + kq * 4096 + h * 256 + trn;
  F4 e0, e1;
#define D_READ(n, st) do { const lptr w_ = pd + (((st) + 5) % 10) * 16384; \
    n.w0 = lds_frag_tr(w_, w_ + 1024); n.w1 = lds_frag_tr(w_ + 512, w_ + 1536); \
    n.x0 = lds_frag_tr(w_ + 2048, w_ + 3072); n.x1 = lds_frag_tr(w_ + 2560, w_ + 3584); } while (0)
#define D_STEPX(c, n, st, A, B, RD, X) do { \
    MM(d0, c.w0, __builtin_bit_cast(bf16x8, A)); if (RD) D_READ(n, (st) + 1); MM(d1, c.w1, __builtin_bit_cast(bf16x8, A)); \
    MM(d0, c.x0, __builtin_bit_cast(bf16x8, B)); X; MM(d1, c.x1, __builtin_bit_cast(bf16x8, B)); } while (0)
#define D_STEP(c, n, st, A, B, RD) D_STEPX(c, n, st, A, B, RD, )
  if (b == 193 && wid == 0) {
#pragma unroll 1
    for (int i = 0; i < 4; ++i) wait_flags(ws + WS_FH + i * 512, 64, tag, lane);
    if (lane == 0) set_flag(ws + WS_EPOCH, tag);
  }
  BAR();
  ST(9);
  TR(30);
  D_READ(e0, 0);
  D_STEPX(e0, e1, 0, ha[0], hb[0], true, issue_d(8)); D_STEPX(e1, e0, 1, ha[1], hb[1], true, issue_d(9)); D_STEP(e0, e1, 2, ha[2], hb[2], true);
  TR(31);
  wait_vm<4>();
  if (lane == 0) set_flag(ws + WS_FO + (qt * 32 + head) * 8, tag);
  BAR();
  TR(32);
  D_STEPX(e1, e0, 3, ha[3], hb[3], true, issue_d(10)); D_STEPX(e0, e1, 4, ha[4], hb[4], true, issue_d(11));
  D_STEPX(e1, e0, 5, ha[5], hb[5], true, issue_d(12)); D_STEPX(e0, e1, 6, ha[6], hb[6], true, issue_d(13));
  TR(33);
  WAIT_BAR(4);
  TR(34);
  D_STEPX(e1, e0, 7, ha[7], hb[7], true, issue_d(14)); D_STEPX(e0, e1, 8, ha[8], hb[8], true, issue_d(15));
  D_STEPX(e1, e0, 9, ha[9], hb[9], true, issue_d(16)); D_STEPX(e0, e1, 10, ha[10], hb[10], true, issue_d(17));
  TR(35);
  WAIT_BAR(6);
  TR(36);
  asm volatile("global_load_dwordx2 %0, %1, off sc1" : "=v"(fl) : "v"(ws + WS_FO + ((rb * 2 + rt) * 32 + r) * 8) : "memory");
  D_STEP(e1, e0, 11, ha[11], hb[11], true); D_STEP(e0, e1, 12, ha[12], hb[12], true);
  TR(37);
  asm volatile("s_waitcnt vmcnt(0)" : "+v"(fl) :: "memory");
  TR(38);
  u32x4 oa[4], ob[4];
  const bool oready = __all(fl == tag);
#define O_LOADS(base) do { _Pragma("unroll") for (int j = 0; j < 4; ++j) { const unsigned so = (base) + j * 8192; \
      BLOAD_DEV(oa[j], voff, ro, so); BLOAD_DEV(ob[j], voff, ro, so + 1024); } } while (0)
  const unsigned obase = (unsigned)((rb * 2 + rt) * 32 + kq * 2) * 1024;
  O_LOADS(oready ? obase : 0x7ff00000u);
  TR(39);
  BAR();
  TR(40);
  D_STEP(e1, e0, 13, ha[13], hb[13], true);
  ST(10);
#define O_WAIT() asm volatile("s_waitcnt vmcnt(0)" : "+v"(oa[0]), "+v"(oa[1]), "+v"(oa[2]), "+v"(oa[3]), "+v"(ob[0]), "+v"(ob[1]), "+v"(ob[2]), "+v"(ob[3]) :: "memory")
  O_WAIT();
  if (!oready) {
    wait_flags(ws + WS_FO + (rb * 2 + rt) * 256, 32, tag, lane);
    O_LOADS(obase);
    O_WAIT();
  }
  ST(11); TR(44);
  D_STEP(e0, e1, 14, oa[0], ob[0], true); D_STEP(e1, e0, 15, oa[1], ob[1], true); D_STEP(e0, e1, 16, oa[2], ob[2], true);
  D_STEP(e1, e0, 17, oa[3], ob[3], false);
  TR(45);
  const lptr red0 = lds + 49152;
  LAS float* red = (LAS float*)(red0 + kq * 17408) + (rt * 32 + r) * 68 + 4 * h;
#pragma unroll
  for (int i = 0; i < 4; ++i) { *(LAS f32x4*)(red + 8 * i) = V4(d0, i); *(LAS f32x4*)(red + 32 + 8 * i) = V4(d1, i); }
  TR(46);
  BAR();
  TR(47);
  f32x4 s0 = {bf_lo(xv[0]), bf_hi(xv[0]), bf_lo(xv[1]), bf_hi(xv[1])}, s1 = {bf_lo(xv[2]), bf_hi(xv[2]), bf_lo(xv[3]), bf_hi(xv[3])};
  sum4(red0, t, s0, s1);
  *(u32x4*)(out + off) = (u32x4){pk2(s0[0], s0[1]), pk2(s0[2], s0[3]), pk2(s1[0], s1[1]), pk2(s1[2], s1[3])};
  TR(48);
  asm volatile("Lk_tail:");
  ST(12); ST_OUT(ws); TR_OUT(ws);
}

extern "C" void kernel_launch(void* const* d_in, const int* in_sizes, int n_in, void* d_out, int out_size, void* d_ws,
                              size_t ws_size, hipStream_t stream) {
  (void)hipFuncSetAttribute((const void*)k_all, hipFuncAttributeMaxDynamicSharedMemorySize, LDS_BYTES);
  k_all<<<256, 512, LDS_BYTES, stream>>>((const char*)d_in[0], (const char*)d_in[1], (const char*)d_in[2], (const char*)d_in[4],
                                         (const char*)d_in[5], (const char*)d_in[6], (char*)d_ws, (char*)d_out);
}
```

```cpp
#define OUT_AUX 17
#include <hip/hip_runtime.h>
#include <hip/hip_bf16.h>
#include <stdint.h>
#include <type_traits>

typedef __attribute__((ext_vector_type(8))) short bf16x8;
typedef __attribute__((ext_vector_type(4))) short s16x4;
typedef __attribute__((ext_vector_type(16))) float f32x16;
typedef __attribute__((ext_vector_type(4))) float f32x4;
typedef __attribute__((ext_vector_type(2))) float f32x2;
typedef __attribute__((ext_vector_type(4))) unsigned u32x4;
typedef __attribute__((ext_vector_type(2))) __bf16 bf2;
typedef unsigned long long u64;
typedef __amdgpu_buffer_rsrc_t rsrc_t;
#define LAS __attribute__((address_space(3)))
#define GAS __attribute__((address_space(1)))
typedef LAS char* lptr;

#define MFMA(a, b, c) __builtin_amdgcn_mfma_f32_32x32x16_bf16((a), (b), (c), 0, 0, 0)
#define MM(c, a, b) do { __builtin_amdgcn_sched_barrier(0); c = MFMA(a, b, c); asm volatile("" : "+v"(c) :: "memory"); \
                         __builtin_amdgcn_sched_barrier(0); } while (0)
#define RFL(x) __builtin_amdgcn_readfirstlane(x)

constexpr int S_ = 2048, D_ = 512, F_ = 1792;
constexpr size_t WS_EPOCH = 0;
constexpr size_t WS_FH = 2048;
constexpr size_t WS_FQ = 16384;
constexpr size_t WS_FX = 49152;
constexpr size_t WS_FO = 32768;
constexpr size_t WS_Q    = 65536;
constexpr size_t WS_KV   = WS_Q + 2097152;
constexpr size_t WS_O    = WS_KV + 4194304;
constexpr size_t WS_XN   = WS_O + 2097152;
constexpr size_t WS_H    = WS_XN + 4194304;
constexpr float QSCALE = 0.25f * 1.4426950408889634f;
constexpr float MBIAS = 80.0f;
constexpr int LDS_BYTES = 163840;
#define W2_AT 7
#define W3_AT 7
#define CPF_LANES 2
#define T_EARLY 0
#define FQ_AT 0
#define POLL_SLEEP 1
#define SPIN_CAP (1u << 16)

#define ST_DECL
#define ST(i)
#define STC(i)
#define TR(i)
#define TRD(i)
#define TR_OUT(wsp)
#define ST_OUT(wsp)
#define ST_ARG
#define ST_PASS

__device__ __forceinline__ rsrc_t mk_rsrc(const void* p, unsigned bytes) {
  return __builtin_amdgcn_make_buffer_rsrc((void*)p, (short)0, (int)bytes, 0x00020000);
}
__device__ __forceinline__ void dma16(rsrc_t r, unsigned voff, unsigned soff, unsigned lds) {
  unsigned keep;
  asm volatile("s_mov_b32 %0, m0\n\ts_mov_b32 m0, %1\n\ts_nop 0\n\tbuffer_load_dwordx4 %2, %3, %4 offen lds\n\ts_mov_b32 m0, %0"
               : "=&s"(keep) : "s"(lds), "v"(voff), "s"(r), "s"(soff) : "memory");
}
__device__ __forceinline__ void dma16_dev(rsrc_t r, unsigned voff, unsigned soff, unsigned lds) {
  unsigned keep;
  asm volatile("s_mov_b32 %0, m0\n\ts_mov_b32 m0, %1\n\ts_nop 0\n\tbuffer_load_dwordx4 %2, %3, %4 offen sc1 lds\n\ts_mov_b32 m0, %0"
               : "=&s"(keep) : "s"(lds), "v"(voff), "s"(r), "s"(soff) : "memory");
}
__device__ __forceinline__ void dma16x3(rsrc_t r, unsigned voff, unsigned soff, unsigned lds) {
  unsigned keep;
  asm volatile("s_mov_b32 %0, m0\n\ts_mov_b32 m0, %1\n\ts_nop 0\n\tbuffer_load_dwordx4 %2, %3, %4 offen lds\n\t"
               "buffer_load_dwordx4 %2, %3, %4 offen offset:1024 lds\n\tbuffer_load_dwordx4 %2, %3, %4 offen offset:2048 lds\n\ts_mov_b32 m0, %0"
               : "=&s"(keep) : "s"(lds), "v"(voff), "s"(r), "s"(soff) : "memory");
}
__device__ __forceinline__ float fma1(float a, float b, float c) {
  float d; asm("v_fma_f32 %0, %1, %2, %3" : "=v"(d) : "v"(a), "v"(b), "v"(c)); return d;
}
#define BLOAD(dst, voff, r, soff) asm volatile("buffer_load_dwordx4 %0, %1, %2, %3 offen" : "=v"(dst) : "v"(voff), "s"(r), "s"(soff) : "memory")
#define BLOAD_DEV(dst, voff, r, soff) asm volatile("buffer_load_dwordx4 %0, %1, %2, %3 offen sc1" : "=v"(dst) : "v"(voff), "s"(r), "s"(soff) : "memory")
#define BSTORE_DEV(val, r, voff) __builtin_amdgcn_raw_buffer_store_b128((val), (r), (voff), 0, 16)
#define WAIT_BAR(N) asm volatile("s_waitcnt vmcnt(" #N ") lgkmcnt(0)\n\ts_barrier" ::: "memory")
#define BAR() asm volatile("s_waitcnt lgkmcnt(0)\n\ts_barrier" ::: "memory")
template <int N> __device__ __forceinline__ void wait_bar() { asm volatile("s_waitcnt vmcnt(%0) lgkmcnt(0)\n\ts_barrier" :: "n"(N) : "memory"); }
template <int N> __device__ __forceinline__ void wait_vm2(u32x4& a, u32x4& b) {
  asm volatile("s_waitcnt vmcnt(%0)" :: "n"(N) : "memory"); __builtin_amdgcn_sched_barrier(0); asm volatile("" : "+v"(a), "+v"(b) :: "memory");
}
template <int N> __device__ __forceinline__ void wait_vm1(u32x4& a) {
  asm volatile("s_waitcnt vmcnt(%0)" :: "n"(N) : "memory"); __builtin_amdgcn_sched_barrier(0); asm volatile("" : "+v"(a) :: "memory");
}

__device__ __forceinline__ unsigned pk2(float lo, float hi) {
  f32x2 v = {lo, hi};
  return __builtin_bit_cast(unsigned, __builtin_convertvector(v, bf2));
}
__device__ __forceinline__ float bf_lo(unsigned u) { return __uint_as_float(u << 16); }
__device__ __forceinline__ float bf_hi(unsigned u) { return __uint_as_float(u & 0xffff0000u); }
__device__ __forceinline__ float xhalf_max(float v) {
  auto r = __builtin_amdgcn_permlane32_swap(__float_as_uint(v), __float_as_uint(v), false, false);
  return __builtin_fmaxf(__uint_as_float(r[0]), __uint_as_float(r[1]));
}
__device__ __forceinline__ bf16x8 lds_frag(lptr p) { return *(const LAS bf16x8*)p; }
typedef short v4i16_t __attribute__((ext_vector_type(4)));
__device__ __forceinline__ bf16x8 lds_frag_tr(lptr p0, lptr p1) {
  s16x4 a = __builtin_bit_cast(s16x4, __builtin_amdgcn_ds_read_tr16_b64_v4i16((LAS v4i16_t*)p0));
  s16x4 b = __builtin_bit_cast(s16x4, __builtin_amdgcn_ds_read_tr16_b64_v4i16((LAS v4i16_t*)p1));
  return (bf16x8){a[0], a[1], a[2], a[3], b[0], b[1], b[2], b[3]};
}
__device__ __forceinline__ float dot2(unsigned a, unsigned b, float c) {
  return __builtin_amdgcn_fdot2_f32_bf16(__builtin_bit_cast(bf2, a), __builtin_bit_cast(bf2, b), c, false);
}
__device__ __forceinline__ u32x4 pack8(const f32x16& a, int o) {
  return (u32x4){pk2(a[o], a[o + 1]), pk2(a[o + 2], a[o + 3]), pk2(a[o + 4], a[o + 5]), pk2(a[o + 6], a[o + 7])};
}
template <int CTRL> __device__ __forceinline__ float dpp_add(float x) {
  return x + __builtin_bit_cast(float, __builtin_amdgcn_mov_dpp(__builtin_bit_cast(int, x), CTRL, 0xf, 0xf, true));
}
__device__ __forceinline__ float sum8(float x) { return dpp_add<0x141>(dpp_add<0x4E>(dpp_add<0xB1>(x))); }

__device__ __forceinline__ u64 read_tag(const char* ws) {
  u64 e;
  asm volatile("s_load_dwordx2 %0, %1, 0x0\n\ts_waitcnt lgkmcnt(0)" : "=s"(e) : "s"(ws + WS_EPOCH) : "memory");
  e = (e ^ (e >> 31)) * 0x9E3779B97F4A7C15ull + 0x632BE59BD9B4E019ull;
  return e ^ (e >> 29);
}
__device__ __forceinline__ void set_flag(char* p, u64 tag) { __hip_atomic_store((GAS u64*)p, tag, __ATOMIC_RELAXED, __HIP_MEMORY_SCOPE_AGENT); }
__device__ __forceinline__ void wait_flags(const char* p, int n, u64 tag, int lane) {
  GAS u64* f = (GAS u64*)p + (lane < n ? lane : 0);
#pragma unroll 1
  for (unsigned spins = 0; spins < SPIN_CAP; ++spins) {
    if (__all(__hip_atomic_load(f, __ATOMIC_RELAXED, __HIP_MEMORY_SCOPE_AGENT) == tag)) break;
    __builtin_amdgcn_s_sleep(POLL_SLEEP);
  }
  asm volatile("" ::: "memory");
}

__device__ __forceinline__ void dma16x2(rsrc_t r, unsigned voff, unsigned soff0, unsigned soff1, unsigned lds) {
  unsigned keep;
  asm volatile("s_mov_b32 %0, m0\n\ts_mov_b32 m0, %1\n\ts_nop 0\n\tbuffer_load_dwordx4 %2, %3, %4 offen lds\n\t"
               "buffer_load_dwordx4 %2, %3, %5 offen offset:1024 lds\n\ts_mov_b32 m0, %0"
               : "=&s"(keep) : "s"(lds), "v"(voff), "s"(r), "s"(soff0), "s"(soff1 - 1024u) : "memory");
}
template <int N> __device__ __forceinline__ void wait_vm() { asm volatile("s_waitcnt vmcnt(%0)" :: "n"(N) : "memory"); }
#define PIN_AFTER(W, ...) do { asm volatile(W ::: "memory"); __builtin_amdgcn_sched_barrier(0); asm volatile("" : __VA_ARGS__ :: "memory"); } while (0)
#define V4(c, g_) (f32x4){c[4 * (g_)], c[4 * (g_) + 1], c[4 * (g_) + 2], c[4 * (g_) + 3]}

constexpr int WR = 65536, QSLOT = 24576;
constexpr int GSLOT = 20480;
constexpr int REC = 2080, CHUNK = 4 * REC;
constexpr int KVA = WR + 3 * GSLOT, KVB = KVA - 4 * CHUNK;
#define NG 9

struct F4 { bf16x8 x0, x1, w0, w1; };
struct QFrag { bf16x8 x, q, k, v; };

template <int G>
__device__ __forceinline__ void qkv_steps(const lptr (&pa)[4], lptr pw, lptr pw2, QFrag& c, QFrag& n, f32x16& aq, f32x16& ak, f32x16& av,
                                          char* fx, u64 tag, int lane ST_ARG) {
  if constexpr (G < 32) {
    constexpr int s = G >> 2, N = G + 1, ns = N >> 2;
    if constexpr ((G & 3) == 3) {
      if constexpr (s == 0) wait_bar<9>(); else if constexpr (s == 1) wait_bar<4 - T_EARLY>(); else if constexpr (s == 2) wait_bar<1 - T_EARLY>(); else BAR();
      if constexpr (s == 1) { if (lane == 0) set_flag(fx, tag); }
    }
    const lptr w = ((ns & 2) ? pw2 : pw) + (ns & 1) * QSLOT + (N & 3) * 6144;
    MM(aq, c.q, c.x);
    if constexpr (N < 32) { n.x = lds_frag(pa[N & 3] + ns * 4096); n.q = lds_frag_tr(w, w + 256); }
    MM(ak, c.k, c.x);
    if constexpr (N < 32) { n.k = lds_frag_tr(w + 1024, w + 1024 + 256); n.v = lds_frag_tr(w + 2048, w + 2048 + 256); }
    MM(av, c.x, c.v);
    qkv_steps<G + 1>(pa, pw, pw2, n, c, aq, ak, av, fx, tag, lane ST_PASS);
  }
}

#define EXP(x) "v_exp_f32 v" #x ", v" #x "\n\t"
#define CVT(d, a, b) "v_cvt_pk_bf16_f32 v" #d ", v" #a ", v" #b "\n\t"
#define MF(d, a, b, c) "v_mfma_f32_32x32x16_bf16 " d ", " a ", " b ", " c "\n\t"
#define HALF(c0, c1, c2, c3, c4, c5, c6, c7, c8, c9, c10, c11, c12, c13, c14, c15, QK, PV1, PV0, X0, X1) \
  QK EXP(c0) EXP(c1) EXP(c2) EXP(c3) PV1 X0 \
  EXP(c4) EXP(c5) CVT(80, c0, c1) CVT(81, c2, c3) EXP(c6) EXP(c7) EXP(c8) EXP(c9) CVT(82, c4, c5) CVT(83, c6, c7) \
  EXP(c10) EXP(c11) PV0 X1 \
  EXP(c12) EXP(c13) CVT(84, c8, c9) CVT(85, c10, c11) EXP(c14) EXP(c15) CVT(86, c12, c13) CVT(87, c14, c15)
#define TILE(off, S, X0, X1, X2, X3) \
  S \
  HALF(16, 17, 18, 19, 20, 21, 22, 23, 24, 25, 26, 27, 28, 29, 30, 31, \
       MF("v[32:47]", "v[68:71]", "v[92:95]", "v[96:111]") "ds_read_b128 v[68:71], v88 offset:" off "\n\t", \
       MF("v[112:127]", "v[76:79]", "v[84:87]", "v[112:127]") "ds_read_b128 v[76:79], v90 offset:" off "\n\t", \
       "s_waitcnt lgkmcnt(2)\n\t" MF("v[48:63]", "v[72:75]", "v[80:83]", "v[48:63]"), X0, X1) \
  HALF(32, 33, 34, 35, 36, 37, 38, 39, 40, 41, 42, 43, 44, 45, 46, 47, \
       "s_waitcnt lgkmcnt(0)\n\t" MF("v[16:31]", "v[68:71]", "v[64:67]", "v[0:15]"), \
       MF("v[48:63]", "v[76:79]", "v[84:87]", "v[48:63]"), \
       MF("v[112:127]", "v[72:75]", "v[80:83]", "v[112:127]") "ds_read_b128 v[72:75], v89 offset:" off "\n\t", X2, X3)
#define T_NONE(off) TILE(off, "", "", "", "", "")
#define T_KV(off, m, so) TILE(off, "s_mov_b32 m0, %[" #m "]\n\t", "", "buffer_load_dwordx4 %[voff], %[rkv], %[" #so "] offen sc1 lds\n\t", \
    "buffer_load_dwordx4 %[voff], %[rkv], %[" #so "] offen offset:1024 sc1 lds\n\t", "")
#define T_WD(off, m, so) TILE(off, "s_mov_b32 m0, %[" #m "]\n\ts_add_u32 %[t0], %[" #so "], 7168\n\t", "", \
    "buffer_load_dwordx4 %[vw1], %[rwd], %[" #so "] offen lds\n\t", "buffer_load_dwordx4 %[vw1], %[rwd], %[t0] offen offset:1024 lds\n\t", "")
#define T_KVI(off, MO, SO) TILE(off, "s_add_u32 m0, %[kvm], " #MO "\n\ts_add_u32 %[t0], %[kvs], " #SO "\n\t", "", \
    "buffer_load_dwordx4 %[voff], %[rkv], %[t0] offen sc1 lds\n\t", "buffer_load_dwordx4 %[voff], %[rkv], %[t0] offen offset:1024 sc1 lds\n\t", "")
#define VB(n) "s_waitcnt vmcnt(" #n ")\n\ts_barrier\n\t"
#define SB_ "s_barrier\n\t"
#define AT(i)
#define AT_OP
#define AT_CL
#define HL(d, so, o) "buffer_load_dwordx4 %[" #d "], %[voff], %[rh], %[" #so "] offen " o "sc1\n\t"
#define T_H(off, da, db, so) TILE(off, "", "", HL(da, so, ""), HL(db, so, "offset:1024 "), "")
#define T_H2(off, da, db, so, dc, dd, sp) TILE(off, "", HL(da, so, ""), HL(db, so, "offset:1024 "), HL(dc, sp, ""), HL(dd, sp, "offset:1024 "))
#define ATT_BEGIN "s_mov_b32 %[keep], m0\n\ts_nop 3\n\t"
#define ATT_END "s_mov_b32 m0, %[keep]\n\ts_waitcnt lgkmcnt(0)\n\ts_nop 3\n\t"
#define ATT_STATE "+{v[48:63]}"(OA), "+{v[112:127]}"(OB), "+{v[16:31]}"(SA), "+{v[68:71]}"(K), "+{v[72:75]}"(V0), "+{v[76:79]}"(V1), \
                  "+{v[84:87]}"(P1), [t0] "=&s"(t0), [keep] "=&s"(keep) AT_OP
#define ATT_CONST "{v[0:15]}"(CA), "{v[96:111]}"(CB), "{v[64:67]}"(QA), "{v[92:95]}"(QB), [voff] "v"(voff)
#define ATT_IN ATT_CONST, "{v88}"(aK), "{v89}"(aV0), "{v90}"(aV1)
#define ATT_CLOB "v32", "v33", "v34", "v35", "v36", "v37", "v38", "v39", "v40", "v41", "v42", "v43", "v44", "v45", "v46", "v47", \
                 "v80", "v81", "v82", "v83", "scc", "memory" AT_CL

struct GFrag { bf16x8 x[4], g, u; };
template <int S, int KG>
__device__ __forceinline__ void gu_read(const lptr (&pa)[4], const lptr (&pp)[4], lptr pg, GFrag& n, int part) {
  const lptr x = pa[(S & 1) * 2 + KG] + (S >> 1) * 4096, y = pp[(S & 1) * 2 + KG] + (S % 3) * GSLOT, w = pg + (S % 3) * GSLOT + KG * 4096;
  if (part == 0) { n.x[0] = lds_frag(x); n.g = lds_frag_tr(w, w + 256); }
  if (part == 1) { n.x[1] = lds_frag(x + 32768); n.u = lds_frag_tr(w + 8192, w + 8192 + 256); }
  if (part == 2) { n.x[2] = lds_frag(y); n.x[3] = lds_frag(y + 2048); }
}
template <int S, class FILL>
__device__ __forceinline__ void gu_stages(const lptr (&pa)[4], const lptr (&pp)[4], lptr pg, GFrag& f0, GFrag& f1, f32x16 (&g)[4], f32x16 (&u)[4], FILL fill ST_ARG) {
  if constexpr (S < 16) {
#define GU8(c, R0, R1, R2, D) do { MM(g[0], c.g, c.x[0]); R0; MM(g[1], c.g, c.x[1]); R1; MM(u[0], c.u, c.x[0]); R2; MM(u[1], c.u, c.x[1]); \
      MM(g[2], c.g, c.x[2]); D; MM(u[2], c.u, c.x[2]); MM(g[3], c.g, c.x[3]); MM(u[3], c.u, c.x[3]); } while (0)
    GU8(f0, (gu_read<S, 1>(pa, pp, pg, f1, 0)), (gu_read<S, 1>(pa, pp, pg, f1, 1)), (gu_read<S, 1>(pa, pp, pg, f1, 2)), );
    if constexpr (S < 15) {
      if constexpr (S == 0) wait_bar<1>(); else if constexpr (S == 1 || S == 14) wait_bar<0>(); else wait_bar<5>();
      TR(17 + S);
      GU8(f1, (gu_read<S + 1, 0>(pa, pp, pg, f0, 0)), (gu_read<S + 1, 0>(pa, pp, pg, f0, 1)), (gu_read<S + 1, 0>(pa, pp, pg, f0, 2)),
          if (S >= NG - 2 && S <= 12) fill(S + 3));
    } else {
      GU8(f1, , , , );
    }
    gu_stages<S + 1>(pa, pp, pg, f0, f1, g, u, fill ST_PASS);
  }
}

__global__ __launch_bounds__(512, 2) void k_all(const char* __restrict__ gx, const char* __restrict__ gwqkv, const char* __restrict__ gwo,
                                                const char* __restrict__ gwg, const char* __restrict__ gwu, const char* __restrict__ gwd,
                                                char* __restrict__ ws, char* __restrict__ out) {
  extern __shared__ __attribute__((aligned(16))) char smem[];
  const lptr lds = (lptr)smem;
  const unsigned lds0 = (unsigned)(uintptr_t)lds;
  const int lane = threadIdx.x & 63, wid = RFL(threadIdx.x >> 6), b = blockIdx.x;
  const int xcd = b & 7, bi = b >> 3;
  const int rb = (xcd >> 1) * 8 + (bi & 7);
  const int cb = (xcd & 1) * 4 + (bi >> 3);
  const int rot = (rb + 2 * cb) & 7;
  const int head = xcd * 4 + (bi >> 3);
  const int r = lane & 31, h = lane >> 5;
  const unsigned trn = ((lane & 15) >> 2) * 64 + ((lane >> 4) & 1) * 32 + (lane & 3) * 8;
  const unsigned voff = lane * 16;
  ST_DECL; ST(0);

  const rsrc_t rx = mk_rsrc(gx, S_ * D_ * 2), rw = mk_rsrc(gwqkv, D_ * 3 * D_ * 2), rxn = mk_rsrc(ws + WS_XN + (cb >> 2) * 2097152, 2097152);
  u32x4 xr[8];
  {
    const unsigned vx = (lane >> 3) * 1024 + (lane & 7) * 16, sx = (unsigned)(rb * 64 + wid * 8) * 1024;
#pragma unroll
    for (int i = 0; i < 8; ++i) BLOAD(xr[i], vx, rx, sx + ((i + rot) & 7) * 128);
  }
  unsigned cpf;
  {
    u64 pc; unsigned len;
    asm volatile("s_getpc_b64 %0\nLk_here:\n\ts_mov_b32 %1, Lk_tail-Lk_here" : "=s"(pc), "=s"(len));
    const unsigned o = (unsigned)((bi * 8 + wid) * CPF_LANES + (lane < CPF_LANES ? lane : 0)) * 128;
    asm volatile("global_load_dword %0, %1, off" : "=v"(cpf) : "v"((const char*)pc + (o + 4 <= len ? o : 0)) : "memory");
  }
  BAR();
  const unsigned vw3 = ((lane >> 2) & 7) * 3072 + (lane >> 5) * 64 + (lane & 3) * 16;
  auto issue_wk = [&](int s, int kb) {
    const unsigned so = (unsigned)(kb * 8) * 3072 + ((s + rot) & 7) * (64 * 3072);
    dma16x3(rw, vw3, so + cb * 128, lds0 + WR + (s & 3) * QSLOT + kb * 3072);
  };
  auto issue_w = [&](int s) { issue_wk(s, wid); };
  issue_w(0); issue_w(1);
  unsigned pf;
#define TOUCH() asm volatile("global_load_dword %0, %1, off" : "=v"(pf) : "v"((h ? gwu : gwg) + (size_t)((b * 8 + wid) * 7 + (r < 7 ? r : 0)) * 128) : "memory")
  const u64 tag = read_tag(ws);
  float rs, mr;
  {
    const unsigned ONE2 = 0x3f803f80u;
    float s0 = 0.f, s1 = 0.f, q0 = 0.f, q1 = 0.f;
#define STAT(i) do { wait_vm1<14 + T_EARLY - (i)>(xr[i]); \
      s0 = dot2(xr[i][0], ONE2, s0); q0 = dot2(xr[i][0], xr[i][0], q0); s1 = dot2(xr[i][1], ONE2, s1); q1 = dot2(xr[i][1], xr[i][1], q1); \
      s0 = dot2(xr[i][2], ONE2, s0); q0 = dot2(xr[i][2], xr[i][2], q0); s1 = dot2(xr[i][3], ONE2, s1); q1 = dot2(xr[i][3], xr[i][3], q1); \
      asm volatile("" : "+v"(s0), "+v"(s1), "+v"(q0), "+v"(q1)); } while (0)
    STAT(0); STAT(1); STAT(2); STAT(3); STAT(4); STAT(5); STAT(6); STAT(7);
    ST(1);
    const float mean = sum8(s0 + s1) * (1.0f / 512.0f);
    rs = __builtin_amdgcn_rsqf(__builtin_fmaxf(sum8(q0 + q1) * (1.0f / 512.0f) - mean * mean, 0.0f) + 1e-5f);
    mr = -mean * rs;
  }
  const unsigned xo = (lane & 7) * 512 + ((((wid & 3) * 8 + (lane >> 3)) ^ (lane & 7)) * 16);
  const lptr pxw = lds + (wid >> 2) * 32768 + xo;
#pragma unroll
  for (int j = 0; j < 8; ++j) {
    u32x4 n;
#pragma unroll
    for (int i = 0; i < 4; ++i) n[i] = pk2(__builtin_fmaf(bf_lo(xr[j][i]), rs, mr), __builtin_fmaf(bf_hi(xr[j][i]), rs, mr));
    if (j < 2) BSTORE_DEV(n, rxn, (unsigned)(rb * 8 + ((rot + j) & 7)) * 8192 + (wid >> 2) * 4096 + xo);
    *(LAS u32x4*)(pxw + j * 4096) = n;
    if (j == W2_AT) issue_w(2);
    if (j == W3_AT) issue_w(3);
  }
  TOUCH();
  const bool has_ffn = cb < 7;
  const int fb = cb * 2 + (rb & 1), w4 = wid & 3;
  const rsrc_t rg = mk_rsrc(gwg - 1024, D_ * F_ * 2 + 1024), ru = mk_rsrc(gwu - 1024, D_ * F_ * 2 + 1024);
  const unsigned vwf = ((lane >> 2) & 7) * (F_ * 2) + (lane >> 5) * 64 + (lane & 3) * 16;
  auto gu_w = [&](int s) {
    const unsigned so = (unsigned)(w4 * 8) * (F_ * 2) + fb * 256 + (unsigned)((s + 2 * rot) & 15) * (32 * F_ * 2) + 1024;
    const unsigned d = lds0 + WR + (s % 3) * GSLOT + w4 * 2048;
    dma16x2(rg, vwf, so, so + 128, d); dma16x2(ru, vwf, so, so + 128, d + 8192);
  };
  auto gu_x = [&](int s) {
    dma16_dev(rxn, voff, (unsigned)((rb ^ 1) * 8 + (((s >> 1) + rot) & 7)) * 8192 + (w4 >> 1) * 4096 + (s & 1) * 2048 + (w4 & 1) * 1024,
              lds0 + WR + (s % 3) * GSLOT + 16384 + w4 * 1024);
  };
  auto gu_fill = [&](int s) { gu_w(s); gu_x(s); };
  u64 fl;
  lptr pa[4];
#pragma unroll
  for (int g = 0; g < 4; ++g) pa[g] = lds + (2 * g + h) * 512 + ((r ^ (2 * g + h)) * 16);
  WAIT_BAR(12);
  ST(2);

  const rsrc_t rkv = mk_rsrc(ws + WS_KV + (size_t)head * 131072, 131072), rh = mk_rsrc(ws + WS_H, 7340032);
  const unsigned vvo = h * 256 + (r & 15) * 16;
  const bf16x8 ONES = {0x3f80, 0x3f80, 0x3f80, 0x3f80, 0x3f80, 0x3f80, 0x3f80, 0x3f80};
  const bf16x8 PAD = ((r & 27) == 16) ? ONES : (bf16x8){};
  const int qt0 = (bi & 7) * 8 + w4 * 2;
  const unsigned ring = lds0 + (wid < 4 ? KVA : KVB), kt0 = wid < 4 ? 0 : 32;
  const unsigned kvm = ring + w4 * REC, kvs = (kt0 + w4) * 2048;
  u32x4 QAu, QBu, S0u, S1u, Ku, V0u;
  auto att_loads = [&] {
    const rsrc_t rq = mk_rsrc(ws + WS_Q, 2097152);
    BLOAD_DEV(QAu, voff, rq, (unsigned)(head * 64 + qt0) * 1024);
    BLOAD_DEV(QBu, voff, rq, (unsigned)(head * 64 + qt0 + 1) * 1024);
    BLOAD_DEV(S0u, voff, rkv, 63u * 2048);
    BLOAD_DEV(S1u, voff, rkv, 0u);
    BLOAD_DEV(Ku, voff, rkv, ((kt0 + 63) & 63) * 2048);
    BLOAD_DEV(V0u, vvo, rkv, ((kt0 + 63) & 63) * 2048 + 1024);
    dma16_dev(rkv, voff, kvs, kvm); dma16_dev(rkv, voff, kvs + 1024, kvm + 1024);
    dma16_dev(rkv, voff, kvs + 8192, kvm + CHUNK); dma16_dev(rkv, voff, kvs + 8192 + 1024, kvm + CHUNK + 1024);
    if (w4 == 0 && r < 16) *(LAS bf16x8*)(lds + (ring - lds0) + r * REC + 2048 + h * 16) = h ? (bf16x8){} : ONES;
  };
#define ATT_WAIT(N) do { asm volatile("s_waitcnt vmcnt(" #N ")" ::: "memory"); __builtin_amdgcn_sched_barrier(0); asm volatile("" : "+v"(QAu), "+v"(QBu), "+v"(S0u), "+v"(S1u), "+v"(Ku), "+v"(V0u) :: "memory"); } while (0)
  bf16x8 QA, QB, K, V0, V1, P1;
  f32x16 SA, CA, CB, OA, OB;
  auto att_init = [&] {
    QA = __builtin_bit_cast(bf16x8, QAu); QB = __builtin_bit_cast(bf16x8, QBu); K = __builtin_bit_cast(bf16x8, Ku);
    V0 = (r >= 16) ? PAD : __builtin_bit_cast(bf16x8, V0u);
    const f32x16 z = {};
    const f32x16 a0 = MFMA(__builtin_bit_cast(bf16x8, S0u), QA, z), a1 = MFMA(__builtin_bit_cast(bf16x8, S1u), QA, z);
    const f32x16 b0 = MFMA(__builtin_bit_cast(bf16x8, S0u), QB, z), b1 = MFMA(__builtin_bit_cast(bf16x8, S1u), QB, z);
    float ma = a0[0], mb = b0[0];
#pragma unroll
    for (int i = 0; i < 16; ++i) { ma = __builtin_fmaxf(__builtin_fmaxf(ma, a0[i]), a1[i]); mb = __builtin_fmaxf(__builtin_fmaxf(mb, b0[i]), b1[i]); }
    ma = -(xhalf_max(ma) + MBIAS); mb = -(xhalf_max(mb) + MBIAS);
#pragma unroll
    for (int i = 0; i < 16; ++i) { CA[i] = ma; CB[i] = mb; }
    SA = MFMA(K, QA, CA);
    asm volatile("s_nop 15" : "+v"(SA));
    V1 = (bf16x8){}; P1 = (bf16x8){}; OA = z; OB = z;
  };
  const unsigned apad = ((r & 27) == 16) ? 2048 : 2064;
  const unsigned aK = ring + voff, aV0 = ring + (r < 16 ? 1024 + vvo : apad), aV1 = ring + (r < 16 ? 1536 + vvo : apad);
  unsigned t0, keep;

  if (wid < 4) {
    {
      const int rt = wid & 1, ch = wid >> 1;
      lptr par[4];
#pragma unroll
      for (int g = 0; g < 4; ++g) par[g] = pa[g] + rt * 32768;
      lptr pw = lds + WR + h * 3072 + ch * 512 + trn, pw2 = pw + 2 * QSLOT;
      asm volatile("" : "+v"(pw2));
      const int pair = cb * 2 + ch, tile = rb * 2 + rt;
      f32x16 aq = {}, ak = {}, av = {};
      QFrag f0, f1;
      f0.x = lds_frag(par[0]); f0.q = lds_frag_tr(pw, pw + 256); f0.k = lds_frag_tr(pw + 1024, pw + 1024 + 256); f0.v = lds_frag_tr(pw + 2048, pw + 2048 + 256);
      qkv_steps<0>(par, pw, pw2, f0, f1, aq, ak, av, ws + WS_FX + (rb * 8 + cb) * 8, tag, threadIdx.x ST_PASS);
      ST(3);
      const rsrc_t rq = mk_rsrc(ws + WS_Q, 2097152 + 4194304);
      const unsigned ko = 2097152 + (unsigned)((2 * pair) * 64 + ((tile + 63) & 63)) * 2048 + voff;
      BSTORE_DEV(pack8(ak, 0), rq, ko);
      BSTORE_DEV(pack8(ak, 8), rq, ko + 131072);
      const unsigned vo = 2097152 + (unsigned)(2 * pair + (r >> 4)) * 131072 + vvo;
      BSTORE_DEV(pack8(av, 0), rq, vo + ((tile + 63) & 63) * 2048 + 1024);
      BSTORE_DEV(pack8(av, 8), rq, vo + tile * 2048 + 1536);
#pragma unroll
      for (int i = 0; i < 16; ++i) aq[i] *= QSCALE;
      const unsigned qo = (unsigned)((2 * pair) * 64 + tile) * 1024 + voff;
      BSTORE_DEV(pack8(aq, 0), rq, qo);
      BSTORE_DEV(pack8(aq, 8), rq, qo + 65536);
    }
    BAR();
#pragma unroll 1
    for (int s = 0; s <= NG - 3; ++s) {
      if (s < 2) BAR(); else WAIT_BAR(5);
      if (s == FQ_AT) {
        wait_vm<5 * FQ_AT>();
        if (lane == 0) set_flag(ws + WS_FQ + ((cb * 2 + (wid >> 1)) * 64 + rb * 2 + (wid & 1)) * 8, tag);
      }
      gu_fill(s + 3);
    }
    asm volatile("global_load_dwordx2 %0, %1, off sc1" : "=v"(fl) : "v"(ws + WS_FQ + ((head >> 1) * 64 + lane) * 8) : "memory");
    WAIT_BAR(6);
    wait_vm<0>(); __builtin_amdgcn_sched_barrier(0); asm volatile("" : "+v"(fl) :: "memory");
    if (!__all(fl == tag)) wait_flags(ws + WS_FQ + (head >> 1) * 512, 64, tag, lane);
    ST(4);
    att_loads();
    BAR();
    ATT_WAIT(4);
    att_init();
    ST(5);
    asm volatile(ATT_BEGIN "s_setprio 2\n\t"
                 VB(2) T_KVI("0", 16640, 16384) T_NONE("2080") SB_ T_NONE("4160") T_NONE("6240")
                 VB(2) T_KVI("8320", 24960, 24576) T_NONE("10400") SB_ T_NONE("12480") T_NONE("14560")
                 VB(2) T_KVI("16640", 0, 32768) T_NONE("18720") SB_ T_NONE("20800") T_NONE("22880")
                 VB(2) T_KVI("24960", 8320, 40960) T_NONE("27040") SB_ T_NONE("29120") T_NONE("31200")
                 ATT_END
                 : ATT_STATE : ATT_IN, [rkv] "s"(rkv), [kvm] "s"(kvm), [kvs] "s"(kvs) : ATT_CLOB);
    __builtin_amdgcn_s_setprio(0);
    wait_vm<2>();
  } else {
    auto w2 = [&](int s) { issue_wk(s, w4 * 2); issue_wk(s, w4 * 2 + 1); };
    WAIT_BAR(9);  w2(4);
    WAIT_BAR(10); w2(5);
    WAIT_BAR(13); w2(6);
    WAIT_BAR(12); w2(7);
    WAIT_BAR(12);
    asm volatile("global_load_dwordx2 %0, %1, off sc1" : "=v"(fl) : "v"(ws + WS_FX + ((rb ^ 1) * 8 + (cb & 4) + (lane & 3)) * 8) : "memory");
    gu_w(0);
    WAIT_BAR(11); gu_w(1);
    do { asm volatile("s_waitcnt vmcnt(8) lgkmcnt(0)\n\ts_barrier" ::: "memory"); __builtin_amdgcn_sched_barrier(0); asm volatile("" : "+v"(fl) :: "memory"); } while (0);
    const bool xok = __all(fl == tag);
    if (xok) gu_x(0);
    gu_w(2);
    if (xok) { gu_x(1); gu_x(2); }
    BAR();
    if (xok) wait_vm<6>();
    else { wait_flags(ws + WS_FX + ((rb ^ 1) * 8 + (cb & 4)) * 8, 4, tag, lane); gu_x(0); gu_x(1); gu_x(2); wait_vm<2>(); }
    ST(3);
    BAR();
    f32x16 g[4], u[4];
    if (has_ffn) {
      lptr pp[4];
#pragma unroll
      for (int i = 0; i < 4; ++i) { pp[i] = pa[i] + (WR + 16384 - (i >> 1) * 2048); asm volatile("" : "+v"(pp[i])); g[i] = (f32x16){}; u[i] = (f32x16){}; }
      const lptr pg = lds + WR + h * 2048 + (w4 >> 1) * 1024 + (w4 & 1) * 512 + trn;
      GFrag f0, f1;
      gu_read<0, 0>(pa, pp, pg, f0, 0); gu_read<0, 0>(pa, pp, pg, f0, 1); gu_read<0, 0>(pa, pp, pg, f0, 2);
      TR(16);
      gu_stages<0>(pa, pp, pg, f0, f1, g, u, gu_fill ST_PASS);
      TR(32);
    } else {
      unsigned d[5];
#pragma unroll 1
      for (int i = 0; i < NG + 1; ++i) BAR();
#pragma unroll
      for (int i = 0; i < 5; ++i) {
        const int t = (rb * 4 + w4) * 144 + i * 32 + (r + i * 32 < 144 ? r : 0);
        const char* p = (t < 14336) ? gwd + (size_t)t * 128 : gwo + (size_t)(t - 14336) * 128;
        asm volatile("global_load_dword %0, %1, off" : "=v"(d[i]) : "v"(p) : "memory");
      }
#pragma unroll 1
      for (int i = NG + 1; i < 15; ++i) BAR();
      do { asm volatile("s_waitcnt vmcnt(0)" ::: "memory"); __builtin_amdgcn_sched_barrier(0); asm volatile("" : "+v"(d[0]), "+v"(d[1]), "+v"(d[2]), "+v"(d[3]), "+v"(d[4]) :: "memory"); } while (0);
    }
    ST(4);
    BAR();
    __builtin_amdgcn_s_setprio(3);
    TR(33);
    att_loads();
    TR(34);
    auto swiglu = [&](int i) {
#pragma unroll
      for (int j = 0; j < 16; ++j) g[i][j] = g[i][j] * u[i][j] * __builtin_amdgcn_rcpf(1.0f + __builtin_amdgcn_exp2f(g[i][j] * -1.4426950408889634f));
      const unsigned ho = has_ffn ? (unsigned)(((rb ^ (i >> 1)) * 2 + (i & 1)) * 112 + fb * 8 + w4 * 2) * 1024 + voff : 0x7ff00000u;
      BSTORE_DEV(pack8(g[i], 0), rh, ho);
      BSTORE_DEV(pack8(g[i], 8), rh, ho + 1024);
    };
    if (has_ffn) { swiglu(0); swiglu(1); }
    TR(35);
#pragma unroll 1
    for (int i = 0; i < NG - 8; ++i) BAR();
    TR(36);
    if (has_ffn) { swiglu(2); swiglu(3); } else wait_vm<0>();
    TR(37);
    ATT_WAIT(8);
    att_init();
    TR(38);
    ST(5);
  }
  asm volatile("" :: "v"(pf), "v"(cpf));
  const int rt = wid & 1, kq = wid >> 1;
  const int drot = (((bi >> 3) * 7 + 1) / 2 + (bi & 7) * 2) % 14;
  const rsrc_t rwd = mk_rsrc(gwd, F_ * D_ * 2), rwo = mk_rsrc(gwo, D_ * D_ * 2), ro = mk_rsrc(ws + WS_O, 2097152);
  const unsigned vw1 = ((lane >> 2) & 7) * 1024 + (lane >> 5) * 64 + (lane & 3) * 16;
  auto dsrc = [&](int st) { const int t = st + drot; return t >= 14 ? t - 14 : t; };
#define DADDR(st) ((st) < 5 ? (st) * 16384 : (st) == 5 || (st) == 12 ? KVA : (st) < 12 ? ((st) - 6) * 16384 : (st) == 13 ? 0 : \
                   (st) == 14 ? 147456 : (st) == 15 ? 98304 : ((st) - 15) * 16384)
  auto d_so = [&](int st, int pc) { return (unsigned)((st < 14 ? dsrc(st) : st - 14) * 128 + pc * 16) * 1024 + cb * 128; };
  auto d_m = [&](int st, int pc) { return lds0 + DADDR(st) + pc * 2048; };
  auto issue_dp = [&](int st, int pc) { dma16x2(st < 14 ? rwd : rwo, vw1, d_so(st, pc), d_so(st, pc) + 8192, d_m(st, pc)); };
  auto h_late = [&](int e) { return (unsigned)((rb * 2 + rt) * 112 + dsrc(7 + e) * 8 + kq * 2) * 1024; };
  auto h_so = [&](int e) {
    return wid < 4 ? (unsigned)((rb * 2 + rt) * 112 + dsrc(e >> 1) * 8 + kq * 4 + (e & 1) * 2) * 1024 : h_late(e);
  };
  const unsigned kvd = kvs + (wid < 4 ? 4 * 8192 : 0);
  u32x4 ha[14], hb[14], xv;
  const unsigned kve = wid < 4 ? 0x7ff00000u : kvd;
  LAS f32x4* ex = (LAS f32x4*)(lds + 147456 + w4 * 4096) + lane;
  LAS float* el = (LAS float*)(lds + 145408 + w4 * 512) + lane;
  TR(39);
  ST(7); STC(14);
  asm volatile(ATT_BEGIN "s_setprio 0\n\ts_waitcnt lgkmcnt(0)\n\t" SB_ AT(40)
               T_KVI("0", 16640, 16384) T_WD("2080", m0_, s0_) T_WD("4160", m1_, s1_) T_WD("6240", m2_, s2_)
               AT(41) VB(8) AT(42)
               "buffer_store_dwordx2 %[tag], %[fho], %[rfl], 0 offen sc1\n\t"
               T_KVI("8320", 24960, 24576) T_WD("10400", m3_, s3_) T_WD("12480", m4_, s4_) T_NONE("14560")
               AT(43) VB(7) AT(44)
               T_KV("16640", kvm, s5_) T_NONE("18720") T_NONE("20800")
               TILE("22880", "", "buffer_load_dwordx2 %[fl], %[fhi], %[rfl], 0 offen sc1\n\t", "", "", "") AT(45)
               ATT_END
               : ATT_STATE, [fl] "=&v"(fl)
               : ATT_IN, [rkv] "s"(rkv), [rwd] "s"(rwd), [vw1] "v"(vw1), [kvm] "s"(kvm), [kvs] "s"(kvd),
                 [m0_] "s"(d_m(0, wid)), [s0_] "s"(d_so(0, wid)), [m1_] "s"(d_m(1, wid)), [s1_] "s"(d_so(1, wid)), [m2_] "s"(d_m(2, wid)), [s2_] "s"(d_so(2, wid)),
                 [m3_] "s"(d_m(3, wid)), [s3_] "s"(d_so(3, wid)), [m4_] "s"(d_m(4, wid)), [s4_] "s"(d_so(4, wid)), [s5_] "s"(kve + 4 * 8192),
                 [rfl] "s"(mk_rsrc(ws + WS_FH, 2048)), [tag] "v"(tag), [fho] "v"(threadIdx.x == 0 ? (unsigned)(rb * 8 + cb) * 8 : 0x7ff00000u),
                 [fhi] "v"((unsigned)((rb >> 1) * 16 + (lane & 15)) * 8) : ATT_CLOB);
  PIN_AFTER("s_waitcnt vmcnt(0)", "+v"(fl));
  if (__builtin_expect(!__all(fl == tag), 0)) wait_flags(ws + WS_FH + (rb >> 1) * 128, 16, tag, lane);
#define H_OUT(n, st) [a##n] "=&v"(ha[st]), [b##n] "=&v"(hb[st])
#define H_IN(n, st) [s##n] "s"(h_so(st))
  f32x4 p0, p1, p2, p3;
  float pl0, pl1;
  asm volatile(ATT_BEGIN SB_ AT(46)
               TILE("24960", "s_add_u32 m0, %[kvm], 8320\n\ts_add_u32 %[t0], %[kvs], 40960\n\t", "buffer_load_dwordx4 %[voff], %[rkv], %[t0] offen sc1 lds\n\t",
                    "buffer_load_dwordx4 %[voff], %[rkv], %[t0] offen offset:1024 sc1 lds\n\t", HL(a0, s0, ""), HL(b0, s0, "offset:1024 "))
               T_H2("27040", a1, b1, s1, a2, b2, s2) T_H2("29120", a3, b3, s3, a4, b4, s4) T_H2("31200", a5, b5, s5, a6, b6, s6)
               AT(47) "s_cmp_lt_u32 %[wid], 4\n\ts_cbranch_scc1 L_skip_%=\n\t"
               "s_setprio 2\n\t"
               SB_ AT(48) T_KVI("0", 16640, 49152) T_NONE("2080") SB_ AT(49) T_NONE("4160") T_NONE("6240")
               VB(2) AT(50) T_KVI("8320", 24960, 57344)
               TILE("10400", "", "", "", "ds_read_b128 %[p0], %[ex]\n\tds_read_b128 %[p1], %[ex] offset:1024\n\tds_read_b32 %[pl0], %[el]\n\t", "")
               SB_ AT(51)
               TILE("12480", "", "", "", "ds_read_b128 %[p2], %[ex] offset:2048\n\tds_read_b128 %[p3], %[ex] offset:3072\n\tds_read_b32 %[pl1], %[el] offset:256\n\t", "")
               T_NONE("14560")
               VB(2) AT(52) T_NONE("16640") T_NONE("18720") SB_ AT(53) T_NONE("20800") T_NONE("22880")
               VB(0) AT(54) T_NONE("24960") T_NONE("27040") SB_ AT(55) T_NONE("29120") T_NONE("31200") AT(56)
               "L_skip_%=:\n\t"
               ATT_END
               : ATT_STATE, H_OUT(0, 0), H_OUT(1, 1), H_OUT(2, 2), H_OUT(3, 3), H_OUT(4, 4), H_OUT(5, 5), H_OUT(6, 6),
                 [p0] "=&v"(p0), [p1] "=&v"(p1), [p2] "=&v"(p2), [p3] "=&v"(p3), [pl0] "=&v"(pl0), [pl1] "=&v"(pl1)
               : ATT_IN, [rkv] "s"(rkv), [kvm] "s"(kvm), [kvs] "s"(kve), [wid] "s"(wid), [ex] "v"(ex), [el] "v"(el),
                 [rh] "s"(rh), H_IN(0, 0), H_IN(1, 1), H_IN(2, 2), H_IN(3, 3), H_IN(4, 4), H_IN(5, 5), H_IN(6, 6) : ATT_CLOB);
  ST(6);

  const int t = threadIdx.x;
  const unsigned off = (unsigned)(rb * 64 + (t >> 3)) * 1024 + cb * 128 + (t & 7) * 16;
  f32x16 d0, d1;
  F4 e0, e1;
#define D_PTR(pd, a) ((a) < 65536 ? pd[0] + (a) : (a) < KVA ? pd[1] + ((a) - 65536) : pd[2] + ((a) - KVA))
#define D_READ_A(n, w_) do { n.w0 = lds_frag_tr(w_, w_ + 1024); n.w1 = lds_frag_tr(w_ + 512, w_ + 1536); } while (0)
#define D_READ_B(n, w_) do { n.x0 = lds_frag_tr(w_ + 2048, w_ + 3072); n.x1 = lds_frag_tr(w_ + 2560, w_ + 3584); } while (0)
#define D_STEPX(c, n, nxt, A, B, RD, X, Y) do { \
    MM(d0, c.w0, __builtin_bit_cast(bf16x8, A)); if (RD) D_READ_A(n, (nxt)); MM(d1, c.w1, __builtin_bit_cast(bf16x8, A)); if (RD) D_READ_B(n, (nxt)); \
    MM(d0, c.x0, __builtin_bit_cast(bf16x8, B)); X; MM(d1, c.x1, __builtin_bit_cast(bf16x8, B)); Y; } while (0)
#define H_LOAD(e, so) do { BLOAD_DEV(ha[e], voff, rh, so); BLOAD_DEV(hb[e], voff, rh, (so) + 1024); } while (0)
  PIN_AFTER("s_waitcnt vmcnt(0)", "+v"(ha[0]), "+v"(ha[1]), "+v"(ha[2]), "+v"(ha[3]), "+v"(ha[4]), "+v"(ha[5]), "+v"(ha[6]),
            "+v"(hb[0]), "+v"(hb[1]), "+v"(hb[2]), "+v"(hb[3]), "+v"(hb[4]), "+v"(hb[5]), "+v"(hb[6]));
  OB = MFMA(V1, P1, OB);
  if (wid < 4) {
    lptr pe[3];
    pe[0] = lds + kq * 8192 + h * 256 + trn; pe[1] = pe[0] + 65536; pe[2] = pe[0] + KVA;
    d0 = d1 = (f32x16){};
    asm volatile("" : "+v"(pe[0]), "+v"(pe[1]), "+v"(pe[2]));
#define EP(e) D_PTR(pe, DADDR((e) >> 1) + ((e) & 1) * 4096)
#define E_DMA(st, i) issue_dp(st, wid * 2 + (i))
#define E_DMA2(st) do { E_DMA(st, 0); E_DMA(st, 1); } while (0)
#define E_STAGE(k, X0, Y0, X1, Y1) do { D_STEPX(e0, e1, EP(2 * (k) + 1), ha[2 * (k)], hb[2 * (k)], true, X0, Y0); \
      D_STEPX(e1, e0, EP(2 * (k) + 2), ha[2 * (k) + 1], hb[2 * (k) + 1], (k) < 6, X1, Y1); } while (0)
#define HE(e) H_LOAD(e, h_so(e))
#define HL_(e) H_LOAD(e, h_late(e))
    D_READ_A(e0, EP(0)); D_READ_B(e0, EP(0));
    E_STAGE(0, HE(7), HE(8), HE(9), );
    BAR();
    ex[0] = V4(OA, 0); ex[64] = V4(OA, 1); ex[128] = V4(OB, 0); ex[192] = V4(OB, 1); el[0] = OA[8]; el[64] = OB[8];
    E_STAGE(1, E_DMA(5, 0), E_DMA(5, 1), E_DMA(6, 0), E_DMA(6, 1));
    BAR();
    E_STAGE(2, E_DMA(7, 0), E_DMA(7, 1), HE(10), HE(11));
    PIN_AFTER("s_waitcnt vmcnt(8) lgkmcnt(0)\n\ts_barrier", "+v"(ha[7]), "+v"(hb[7]), "+v"(ha[8]), "+v"(hb[8]), "+v"(ha[9]), "+v"(hb[9]));
    E_STAGE(3, E_DMA(8, 0), E_DMA(8, 1), HE(12), HE(13));
    BAR();
    E_STAGE(4, E_DMA(9, 0), E_DMA(9, 1), E_DMA(11, 0), E_DMA(11, 1));
    PIN_AFTER("s_waitcnt vmcnt(8) lgkmcnt(0)\n\ts_barrier", "+v"(ha[10]), "+v"(hb[10]), "+v"(ha[11]), "+v"(hb[11]), "+v"(ha[12]), "+v"(hb[12]),
              "+v"(ha[13]), "+v"(hb[13]));
    E_STAGE(5, E_DMA(10, 0), E_DMA(10, 1), HL_(0), HL_(1));
    BAR();
    E_STAGE(6, E_DMA(12, 0), E_DMA(12, 1), HL_(2), HL_(3));
    BAR();
    E_DMA2(13); E_DMA2(14); HL_(4);
    BAR();
    E_DMA2(15); HL_(5); HL_(6);
    if (b == 193 && wid == 0) {
#pragma unroll 1
      for (int i = 0; i < 4; ++i) wait_flags(ws + WS_FH + i * 512, 64, tag, lane);
      if (lane == 0) set_flag(ws + WS_EPOCH, tag);
    }
    PIN_AFTER("s_waitcnt vmcnt(0)", "+v"(ha[0]), "+v"(ha[1]), "+v"(ha[2]), "+v"(ha[3]), "+v"(ha[4]), "+v"(ha[5]), "+v"(ha[6]),
              "+v"(hb[0]), "+v"(hb[1]), "+v"(hb[2]), "+v"(hb[3]), "+v"(hb[4]), "+v"(hb[5]), "+v"(hb[6]));
    ST(8);
  } else {
    ST(8); STC(15);
    auto finish_o = [&](f32x16 O, const bf16x8& Q, int i) {
      float l = O[8], mag = __builtin_fabsf(O[0]);
#pragma unroll
      for (int j = 1; j < 8; ++j) mag += __builtin_fabsf(O[j]);
      const bool bad = !(l < 0x1p120f) || !(mag < 0x1p126f) || !(xhalf_max(mag) > 0x1p-100f);
      if (__builtin_expect(__any(bad), 0)) {
        const f32x16 z = {};
        float mt = -__builtin_inff();
#pragma unroll 1
        for (int tt = 0; tt < 64; ++tt) {
          const f32x16 s = MFMA(__builtin_bit_cast(bf16x8, __builtin_amdgcn_raw_buffer_load_b128(rkv, voff, tt * 2048, 16)), Q, z);
#pragma unroll
          for (int j = 0; j < 16; ++j) mt = __builtin_fmaxf(mt, s[j]);
        }
        mt = xhalf_max(mt);
        O = z;
#pragma unroll 1
        for (int tt = 0; tt < 64; ++tt) {
          f32x16 s = MFMA(__builtin_bit_cast(bf16x8, __builtin_amdgcn_raw_buffer_load_b128(rkv, voff, ((tt + 63) & 63) * 2048, 16)), Q, z);
          bf16x8 v0 = __builtin_bit_cast(bf16x8, __builtin_amdgcn_raw_buffer_load_b128(rkv, vvo, ((tt + 63) & 63) * 2048 + 1024, 16));
          bf16x8 v1 = __builtin_bit_cast(bf16x8, __builtin_amdgcn_raw_buffer_load_b128(rkv, vvo, tt * 2048 + 1536, 16));
          if (r >= 16) { v0 = PAD; v1 = PAD; }
#pragma unroll
          for (int j = 0; j < 16; ++j) s[j] = __builtin_amdgcn_exp2f(s[j] - mt);
          O = MFMA(v0, __builtin_bit_cast(bf16x8, pack8(s, 0)), O);
          O = MFMA(v1, __builtin_bit_cast(bf16x8, pack8(s, 8)), O);
        }
        l = O[8];
      }
      const float il = __builtin_amdgcn_rcpf(l);
#pragma unroll
      for (int j = 0; j < 8; ++j) O[j] *= il;
      BSTORE_DEV(pack8(O, 0), ro, (unsigned)((qt0 + i) * 32 + head) * 1024 + voff);
    };
#pragma unroll
    for (int j = 0; j < 4; ++j) { OA[j] += p0[j]; OA[4 + j] += p1[j]; OB[j] += p2[j]; OB[4 + j] += p3[j]; }
    OA[8] += pl0; OB[8] += pl1;
    finish_o(OA, QA, 0); finish_o(OB, QB, 1);
    d0 = d1 = (f32x16){};
    asm volatile("" : "+v"(d0), "+v"(d1));
    __builtin_amdgcn_s_setprio(0);
  }
  lptr pd[3];
  pd[0] = lds + kq * 4096 + h * 256 + trn; pd[1] = pd[0] + 65536; pd[2] = pd[0] + KVA;
  asm volatile("" : "+v"(pd[0]), "+v"(pd[1]), "+v"(pd[2]));
#define DP(st) D_PTR(pd, DADDR(st))
#define FO_LOAD() asm volatile("global_load_dwordx2 %0, %1, off sc1" : "=v"(fl) : "v"(ws + WS_FO + ((rb * 2 + rt) * 32 + r) * 8) : "memory")
  u32x4 oa[4], ob[4];
#define O_LOAD(j, base) do { BLOAD_DEV(oa[j], voff, ro, (base) + (j) * 8192); BLOAD_DEV(ob[j], voff, ro, (base) + (j) * 8192 + 1024); } while (0)
  const unsigned obase = (unsigned)((rb * 2 + rt) * 32 + kq * 2) * 1024;
  BAR();
  ST(9);
  D_READ_A(e0, DP(7)); D_READ_B(e0, DP(7));
  if (wid >= 4) {
    wait_vm<0>();
    if (lane < 2) set_flag(ws + WS_FO + ((qt0 + lane) * 32 + head) * 8, tag);
  }
  D_STEPX(e0, e1, DP(8), ha[0], hb[0], true, , ); D_STEPX(e1, e0, DP(9), ha[1], hb[1], true, , );
  BAR();
  D_STEPX(e0, e1, DP(10), ha[2], hb[2], true, issue_dp(16, wid), BLOAD(xv, off, rx, 0u)); D_STEPX(e1, e0, DP(11), ha[3], hb[3], true, issue_dp(17, wid), );
  D_STEPX(e0, e1, DP(12), ha[4], hb[4], true, FO_LOAD(), ); D_STEPX(e1, e0, DP(13), ha[5], hb[5], true, , );
  PIN_AFTER("s_waitcnt vmcnt(0) lgkmcnt(0)\n\ts_barrier", "+v"(fl), "+v"(xv));
  const bool oready = __all(fl == tag);
  if (!oready) FO_LOAD();
  D_STEPX(e0, e1, DP(14), ha[6], hb[6], true, , );
  ST(10);
  if (!oready) {
    PIN_AFTER("s_waitcnt vmcnt(0)", "+v"(fl));
    if (!__all(fl == tag)) wait_flags(ws + WS_FO + (rb * 2 + rt) * 256, 32, tag, lane);
  }
  ST(11);
  O_LOAD(0, obase); O_LOAD(1, obase); O_LOAD(2, obase); O_LOAD(3, obase);
  wait_vm2<6>(oa[0], ob[0]); D_STEPX(e1, e0, DP(15), oa[0], ob[0], true, , );
  wait_vm2<4>(oa[1], ob[1]); D_STEPX(e0, e1, DP(16), oa[1], ob[1], true, , );
  wait_vm2<2>(oa[2], ob[2]); D_STEPX(e1, e0, DP(17), oa[2], ob[2], true, , );
  wait_vm2<0>(oa[3], ob[3]); D_STEPX(e0, e1, DP(17), oa[3], ob[3], false, , );
  {
    const lptr red = lds + (kq == 3 ? KVA : 49152 + kq * 16384) + (rt * 32 + r) * 256;
#pragma unroll
    for (int i = 0; i < 4; ++i) {
      *(LAS f32x4*)(red + (((2 * i + h) ^ (r & 15)) * 16)) = V4(d0, i);
      *(LAS f32x4*)(red + (((8 + 2 * i + h) ^ (r & 15)) * 16)) = V4(d1, i);
    }
  }
  BAR();
  f32x4 s0 = {bf_lo(xv[0]), bf_hi(xv[0]), bf_lo(xv[1]), bf_hi(xv[1])}, s1 = {bf_lo(xv[2]), bf_hi(xv[2]), bf_lo(xv[3]), bf_hi(xv[3])};
  {
    const int row = t >> 3, c = (t & 7) * 2;
    const lptr rd0 = lds + 49152 + row * 256 + ((c ^ (row & 15)) * 16), rd1 = lds + 49152 + row * 256 + (((c + 1) ^ (row & 15)) * 16);
#pragma unroll
    for (int p = 0; p < 4; ++p) {
      const int o = p == 3 ? KVA - 49152 : p * 16384;
      s0 += *(const LAS f32x4*)(rd0 + o); s1 += *(const LAS f32x4*)(rd1 + o);
    }
  }
  __builtin_amdgcn_raw_buffer_store_b128((u32x4){pk2(s0[0], s0[1]), pk2(s0[2], s0[3]), pk2(s1[0], s1[1]), pk2(s1[2], s1[3])}, mk_rsrc(out, S_ * D_ * 2), off, 0, OUT_AUX);
  asm volatile("Lk_tail:");
  ST(12); ST_OUT(ws); TR_OUT(ws);
}

extern "C" void kernel_launch(void* const* d_in, const int* in_sizes, int n_in, void* d_out, int out_size, void* d_ws,
                              size_t ws_size, hipStream_t stream) {
  (void)hipFuncSetAttribute((const void*)k_all, hipFuncAttributeMaxDynamicSharedMemorySize, LDS_BYTES);
  k_all<<<256, 512, LDS_BYTES, stream>>>((const char*)d_in[0], (const char*)d_in[1], (const char*)d_in[2], (const char*)d_in[4],
                                         (const char*)d_in[5], (const char*)d_in[6], (char*)d_ws, (char*)d_out);
}
```

```cpp
#include <hip/hip_runtime.h>
#include <hip/hip_bf16.h>
#include <stdint.h>
#include <type_traits>

typedef __attribute__((ext_vector_type(8))) short bf16x8;
typedef __attribute__((ext_vector_type(4))) short s16x4;
typedef __attribute__((ext_vector_type(16))) float f32x16;
typedef __attribute__((ext_vector_type(4))) float f32x4;
typedef __attribute__((ext_vector_type(2))) float f32x2;
typedef __attribute__((ext_vector_type(4))) unsigned u32x4;
typedef __attribute__((ext_vector_type(2))) __bf16 bf2;
typedef unsigned long long u64;
typedef __amdgpu_buffer_rsrc_t rsrc_t;
#define LAS __attribute__((address_space(3)))
#define GAS __attribute__((address_space(1)))
typedef LAS char* lptr;

#define MFMA(a, b, c) __builtin_amdgcn_mfma_f32_32x32x16_bf16((a), (b), (c), 0, 0, 0)
#define MM(c, a, b) do { __builtin_amdgcn_sched_barrier(0); c = MFMA(a, b, c); asm volatile("" : "+v"(c) :: "memory"); \
                         __builtin_amdgcn_sched_barrier(0); } while (0)
#define RFL(x) __builtin_amdgcn_readfirstlane(x)

constexpr int S_ = 2048, D_ = 512, F_ = 1792;
constexpr size_t WS_EPOCH = 0;
constexpr size_t WS_FH = 2048;
constexpr size_t WS_FQ = 16384;
constexpr size_t WS_FX = 49152;
constexpr size_t WS_FO = 32768;
constexpr size_t WS_Q    = 65536;
constexpr size_t WS_KV   = WS_Q + 2097152;
constexpr size_t WS_O    = WS_KV + 4194304;
constexpr size_t WS_XN   = WS_O + 2097152;
constexpr size_t WS_H    = WS_XN + 4194304;
constexpr float QSCALE = 0.25f * 1.4426950408889634f;
constexpr float MBIAS = 80.0f;
constexpr int LDS_BYTES = 163840;
#define W2_AT 7
#define W3_AT 7
#define CPF_LANES 2
#define OUT_AUX 0
#define T_EARLY 0
#define FQ_AT 0
#define POLL_SLEEP 1
#define SPIN_CAP (1u << 16)

#define ST_DECL
#define ST(i)
#define STC(i)
#define TR(i)
#define TRD(i)
#define TR_OUT(wsp)
#define ST_OUT(wsp)
#define ST_ARG
#define ST_PASS

__device__ __forceinline__ rsrc_t mk_rsrc(const void* p, unsigned bytes) {
  return __builtin_amdgcn_make_buffer_rsrc((void*)p, (short)0, (int)bytes, 0x00020000);
}
__device__ __forceinline__ void dma16(rsrc_t r, unsigned voff, unsigned soff, unsigned lds) {
  unsigned keep;
  asm volatile("s_mov_b32 %0, m0\n\ts_mov_b32 m0, %1\n\ts_nop 0\n\tbuffer_load_dwordx4 %2, %3, %4 offen lds\n\ts_mov_b32 m0, %0"
               : "=&s"(keep) : "s"(lds), "v"(voff), "s"(r), "s"(soff) : "memory");
}
__device__ __forceinline__ void dma16_dev(rsrc_t r, unsigned voff, unsigned soff, unsigned lds) {
  unsigned keep;
  asm volatile("s_mov_b32 %0, m0\n\ts_mov_b32 m0, %1\n\ts_nop 0\n\tbuffer_load_dwordx4 %2, %3, %4 offen sc1 lds\n\ts_mov_b32 m0, %0"
               : "=&s"(keep) : "s"(lds), "v"(voff), "s"(r), "s"(soff) : "memory");
}
__device__ __forceinline__ void dma16x3(rsrc_t r, unsigned voff, unsigned soff, unsigned lds) {
  unsigned keep;
  asm volatile("s_mov_b32 %0, m0\n\ts_mov_b32 m0, %1\n\ts_nop 0\n\tbuffer_load_dwordx4 %2, %3, %4 offen lds\n\t"
               "buffer_load_dwordx4 %2, %3, %4 offen offset:1024 lds\n\tbuffer_load_dwordx4 %2, %3, %4 offen offset:2048 lds\n\ts_mov_b32 m0, %0"
               : "=&s"(keep) : "s"(lds), "v"(voff), "s"(r), "s"(soff) : "memory");
}
__device__ __forceinline__ float fma1(float a, float b, float c) {
  float d; asm("v_fma_f32 %0, %1, %2, %3" : "=v"(d) : "v"(a), "v"(b), "v"(c)); return d;
}
#define BLOAD(dst, voff, r, soff) asm volatile("buffer_load_dwordx4 %0, %1, %2, %3 offen" : "=v"(dst) : "v"(voff), "s"(r), "s"(soff) : "memory")
#define BLOAD_DEV(dst, voff, r, soff) asm volatile("buffer_load_dwordx4 %0, %1, %2, %3 offen sc1" : "=v"(dst) : "v"(voff), "s"(r), "s"(soff) : "memory")
#define BSTORE_DEV(val, r, voff) __builtin_amdgcn_raw_buffer_store_b128((val), (r), (voff), 0, 16)
#define WAIT_BAR(N) asm volatile("s_waitcnt vmcnt(" #N ") lgkmcnt(0)\n\ts_barrier" ::: "memory")
#define BAR() asm volatile("s_waitcnt lgkmcnt(0)\n\ts_barrier" ::: "memory")
template <int N> __device__ __forceinline__ void wait_bar() { asm volatile("s_waitcnt vmcnt(%0) lgkmcnt(0)\n\ts_barrier" :: "n"(N) : "memory"); }
template <int N> __device__ __forceinline__ void wait_vm2(u32x4& a, u32x4& b) {
  asm volatile("s_waitcnt vmcnt(%0)" :: "n"(N) : "memory"); __builtin_amdgcn_sched_barrier(0); asm volatile("" : "+v"(a), "+v"(b) :: "memory");
}
template <int N> __device__ __forceinline__ void wait_vm1(u32x4& a) {
  asm volatile("s_waitcnt vmcnt(%0)" :: "n"(N) : "memory"); __builtin_amdgcn_sched_barrier(0); asm volatile("" : "+v"(a) :: "memory");
}

__device__ __forceinline__ unsigned pk2(float lo, float hi) {
  f32x2 v = {lo, hi};
  return __builtin_bit_cast(unsigned, __builtin_convertvector(v, bf2));
}
__device__ __forceinline__ float bf_lo(unsigned u) { return __uint_as_float(u << 16); }
__device__ __forceinline__ float bf_hi(unsigned u) { return __uint_as_float(u & 0xffff0000u); }
__device__ __forceinline__ float xhalf_max(float v) {
  auto r = __builtin_amdgcn_permlane32_swap(__float_as_uint(v), __float_as_uint(v), false, false);
  return __builtin_fmaxf(__uint_as_float(r[0]), __uint_as_float(r[1]));
}
__device__ __forceinline__ bf16x8 lds_frag(lptr p) { return *(const LAS bf16x8*)p; }
typedef short v4i16_t __attribute__((ext_vector_type(4)));
__device__ __forceinline__ bf16x8 lds_frag_tr(lptr p0, lptr p1) {
  s16x4 a = __builtin_bit_cast(s16x4, __builtin_amdgcn_ds_read_tr16_b64_v4i16((LAS v4i16_t*)p0));
  s16x4 b = __builtin_bit_cast(s16x4, __builtin_amdgcn_ds_read_tr16_b64_v4i16((LAS v4i16_t*)p1));
  return (bf16x8){a[0], a[1], a[2], a[3], b[0], b[1], b[2], b[3]};
}
__device__ __forceinline__ float dot2(unsigned a, unsigned b, float c) {
  return __builtin_amdgcn_fdot2_f32_bf16(__builtin_bit_cast(bf2, a), __builtin_bit_cast(bf2, b), c, false);
}
__device__ __forceinline__ u32x4 pack8(const f32x16& a, int o) {
  return (u32x4){pk2(a[o], a[o + 1]), pk2(a[o + 2], a[o + 3]), pk2(a[o + 4], a[o + 5]), pk2(a[o + 6], a[o + 7])};
}
template <int CTRL> __device__ __forceinline__ float dpp_add(float x) {
  return x + __builtin_bit_cast(float, __builtin_amdgcn_mov_dpp(__builtin_bit_cast(int, x), CTRL, 0xf, 0xf, true));
}
__device__ __forceinline__ float sum8(float x) { return dpp_add<0x141>(dpp_add<0x4E>(dpp_add<0xB1>(x))); }

__device__ __forceinline__ u64 read_tag(const char* ws) {
  u64 e;
  asm volatile("s_load_dwordx2 %0, %1, 0x0\n\ts_waitcnt lgkmcnt(0)" : "=s"(e) : "s"(ws + WS_EPOCH) : "memory");
  e = (e ^ (e >> 31)) * 0x9E3779B97F4A7C15ull + 0x632BE59BD9B4E019ull;
  return e ^ (e >> 29);
}
__device__ __forceinline__ void set_flag(char* p, u64 tag) { __hip_atomic_store((GAS u64*)p, tag, __ATOMIC_RELAXED, __HIP_MEMORY_SCOPE_AGENT); }
__device__ __forceinline__ void wait_flags(const char* p, int n, u64 tag, int lane) {
  GAS u64* f = (GAS u64*)p + (lane < n ? lane : 0);
#pragma unroll 1
  for (unsigned spins = 0; spins < SPIN_CAP; ++spins) {
    if (__all(__hip_atomic_load(f, __ATOMIC_RELAXED, __HIP_MEMORY_SCOPE_AGENT) == tag)) break;
    __builtin_amdgcn_s_sleep(POLL_SLEEP);
  }
  asm volatile("" ::: "memory");
}

__device__ __forceinline__ void dma16x2(rsrc_t r, unsigned voff, unsigned soff0, unsigned soff1, unsigned lds) {
  unsigned keep;
  asm volatile("s_mov_b32 %0, m0\n\ts_mov_b32 m0, %1\n\ts_nop 0\n\tbuffer_load_dwordx4 %2, %3, %4 offen lds\n\t"
               "buffer_load_dwordx4 %2, %3, %5 offen offset:1024 lds\n\ts_mov_b32 m0, %0"
               : "=&s"(keep) : "s"(lds), "v"(voff), "s"(r), "s"(soff0), "s"(soff1 - 1024u) : "memory");
}
template <int N> __device__ __forceinline__ void wait_vm() { asm volatile("s_waitcnt vmcnt(%0)" :: "n"(N) : "memory"); }
#define PIN_AFTER(W, ...) do { asm volatile(W ::: "memory"); __builtin_amdgcn_sched_barrier(0); asm volatile("" : __VA_ARGS__ :: "memory"); } while (0)
#define V4(c, g_) (f32x4){c[4 * (g_)], c[4 * (g_) + 1], c[4 * (g_) + 2], c[4 * (g_) + 3]}

constexpr int WR = 65536, QSLOT = 24576;
constexpr int GSLOT = 20480;
constexpr int REC = 2080, CHUNK = 4 * REC;
constexpr int KVA = WR + 3 * GSLOT, KVB = KVA - 4 * CHUNK;
#define NG 9

struct F4 { bf16x8 x0, x1, w0, w1; };
struct QFrag { bf16x8 x, q, k, v; };

template <int G>
__device__ __forceinline__ void qkv_steps(const lptr (&pa)[4], lptr pw, lptr pw2, QFrag& c, QFrag& n, f32x16& aq, f32x16& ak, f32x16& av,
                                          char* fx, u64 tag, int lane ST_ARG) {
  if constexpr (G < 32) {
    constexpr int s = G >> 2, N = G + 1, ns = N >> 2;
    if constexpr ((G & 3) == 3) {
      if constexpr (s == 0) wait_bar<9>(); else if constexpr (s == 1) wait_bar<4 - T_EARLY>(); else if constexpr (s == 2) wait_bar<1 - T_EARLY>(); else BAR();
      if constexpr (s == 1) { if (lane == 0) set_flag(fx, tag); }
    }
    const lptr w = ((ns & 2) ? pw2 : pw) + (ns & 1) * QSLOT + (N & 3) * 6144;
    MM(aq, c.q, c.x);
    if constexpr (N < 32) { n.x = lds_frag(pa[N & 3] + ns * 4096); n.q = lds_frag_tr(w, w + 256); }
    MM(ak, c.k, c.x);
    if constexpr (N < 32) { n.k = lds_frag_tr(w + 1024, w + 1024 + 256); n.v = lds_frag_tr(w + 2048, w + 2048 + 256); }
    MM(av, c.x, c.v);
    qkv_steps<G + 1>(pa, pw, pw2, n, c, aq, ak, av, fx, tag, lane ST_PASS);
  }
}

#define EXP(x) "v_exp_f32 v" #x ", v" #x "\n\t"
#define CVT(d, a, b) "v_cvt_pk_bf16_f32 v" #d ", v" #a ", v" #b "\n\t"
#define MF(d, a, b, c) "v_mfma_f32_32x32x16_bf16 " d ", " a ", " b ", " c "\n\t"
#define HALF(c0, c1, c2, c3, c4, c5, c6, c7, c8, c9, c10, c11, c12, c13, c14, c15, QK, PV1, PV0, X0, X1) \
  QK EXP(c0) EXP(c1) EXP(c2) EXP(c3) PV1 X0 \
  EXP(c4) EXP(c5) CVT(80, c0, c1) CVT(81, c2, c3) EXP(c6) EXP(c7) EXP(c8) EXP(c9) CVT(82, c4, c5) CVT(83, c6, c7) \
  EXP(c10) EXP(c11) PV0 X1 \
  EXP(c12) EXP(c13) CVT(84, c8, c9) CVT(85, c10, c11) EXP(c14) EXP(c15) CVT(86, c12, c13) CVT(87, c14, c15)
#define TILE(off, S, X0, X1, X2, X3) \
  S \
  HALF(16, 17, 18, 19, 20, 21, 22, 23, 24, 25, 26, 27, 28, 29, 30, 31, \
       MF("v[32:47]", "v[68:71]", "v[92:95]", "v[96:111]") "ds_read_b128 v[68:71], v88 offset:" off "\n\t", \
       MF("v[112:127]", "v[76:79]", "v[84:87]", "v[112:127]") "ds_read_b128 v[76:79], v90 offset:" off "\n\t", \
       "s_waitcnt lgkmcnt(2)\n\t" MF("v[48:63]", "v[72:75]", "v[80:83]", "v[48:63]"), X0, X1) \
  HALF(32, 33, 34, 35, 36, 37, 38, 39, 40, 41, 42, 43, 44, 45, 46, 47, \
       "s_waitcnt lgkmcnt(0)\n\t" MF("v[16:31]", "v[68:71]", "v[64:67]", "v[0:15]"), \
       MF("v[48:63]", "v[76:79]", "v[84:87]", "v[48:63]"), \
       MF("v[112:127]", "v[72:75]", "v[80:83]", "v[112:127]") "ds_read_b128 v[72:75], v89 offset:" off "\n\t", X2, X3)
#define T_NONE(off) TILE(off, "", "", "", "", "")
#define T_KV(off, m, so) TILE(off, "s_mov_b32 m0, %[" #m "]\n\t", "", "buffer_load_dwordx4 %[voff], %[rkv], %[" #so "] offen sc1 lds\n\t", \
    "buffer_load_dwordx4 %[voff], %[rkv], %[" #so "] offen offset:1024 sc1 lds\n\t", "")
#define T_WD(off, m, so) TILE(off, "s_mov_b32 m0, %[" #m "]\n\ts_add_u32 %[t0], %[" #so "], 7168\n\t", "", \
    "buffer_load_dwordx4 %[vw1], %[rwd], %[" #so "] offen lds\n\t", "buffer_load_dwordx4 %[vw1], %[rwd], %[t0] offen offset:1024 lds\n\t", "")
#define T_KVI(off, MO, SO) TILE(off, "s_add_u32 m0, %[kvm], " #MO "\n\ts_add_u32 %[t0], %[kvs], " #SO "\n\t", "", \
    "buffer_load_dwordx4 %[voff], %[rkv], %[t0] offen sc1 lds\n\t", "buffer_load_dwordx4 %[voff], %[rkv], %[t0] offen offset:1024 sc1 lds\n\t", "")
#define VB(n) "s_waitcnt vmcnt(" #n ")\n\ts_barrier\n\t"
#define SB_ "s_barrier\n\t"
#define AT(i)
#define AT_OP
#define AT_CL
#define HL(d, so, o) "buffer_load_dwordx4 %[" #d "], %[voff], %[rh], %[" #so "] offen " o "sc1\n\t"
#define T_H(off, da, db, so) TILE(off, "", "", HL(da, so, ""), HL(db, so, "offset:1024 "), "")
#define T_H2(off, da, db, so, dc, dd, sp) TILE(off, "", HL(da, so, ""), HL(db, so, "offset:1024 "), HL(dc, sp, ""), HL(dd, sp, "offset:1024 "))
#define ATT_BEGIN "s_mov_b32 %[keep], m0\n\ts_nop 3\n\t"
#define ATT_END "s_mov_b32 m0, %[keep]\n\ts_waitcnt lgkmcnt(0)\n\ts_nop 3\n\t"
#define ATT_STATE "+{v[48:63]}"(OA), "+{v[112:127]}"(OB), "+{v[16:31]}"(SA), "+{v[68:71]}"(K), "+{v[72:75]}"(V0), "+{v[76:79]}"(V1), \
                  "+{v[84:87]}"(P1), [t0] "=&s"(t0), [keep] "=&s"(keep) AT_OP
#define ATT_CONST "{v[0:15]}"(CA), "{v[96:111]}"(CB), "{v[64:67]}"(QA), "{v[92:95]}"(QB), [voff] "v"(voff)
#define ATT_IN ATT_CONST, "{v88}"(aK), "{v89}"(aV0), "{v90}"(aV1)
#define ATT_CLOB "v32", "v33", "v34", "v35", "v36", "v37", "v38", "v39", "v40", "v41", "v42", "v43", "v44", "v45", "v46", "v47", \
                 "v80", "v81", "v82", "v83", "scc", "memory" AT_CL

struct GFrag { bf16x8 x[4], g, u; };
template <int S, int KG>
__device__ __forceinline__ void gu_read(const lptr (&pa)[4], const lptr (&pp)[4], lptr pg, GFrag& n, int part) {
  const lptr x = pa[(S & 1) * 2 + KG] + (S >> 1) * 4096, y = pp[(S & 1) * 2 + KG] + (S % 3) * GSLOT, w = pg + (S % 3) * GSLOT + KG * 4096;
  if (part == 0) { n.x[0] = lds_frag(x); n.g = lds_frag_tr(w, w + 256); }
  if (part == 1) { n.x[1] = lds_frag(x + 32768); n.u = lds_frag_tr(w + 8192, w + 8192 + 256); }
  if (part == 2) { n.x[2] = lds_frag(y); n.x[3] = lds_frag(y + 2048); }
}
template <int S, class FILL>
__device__ __forceinline__ void gu_stages(const lptr (&pa)[4], const lptr (&pp)[4], lptr pg, GFrag& f0, GFrag& f1, f32x16 (&g)[4], f32x16 (&u)[4], FILL fill ST_ARG) {
  if constexpr (S < 16) {
#define GU8(c, R0, R1, R2, D) do { MM(g[0], c.g, c.x[0]); R0; MM(g[1], c.g, c.x[1]); R1; MM(u[0], c.u, c.x[0]); R2; MM(u[1], c.u, c.x[1]); \
      MM(g[2], c.g, c.x[2]); D; MM(u[2], c.u, c.x[2]); MM(g[3], c.g, c.x[3]); MM(u[3], c.u, c.x[3]); } while (0)
    GU8(f0, (gu_read<S, 1>(pa, pp, pg, f1, 0)), (gu_read<S, 1>(pa, pp, pg, f1, 1)), (gu_read<S, 1>(pa, pp, pg, f1, 2)), );
    if constexpr (S < 15) {
      if constexpr (S == 0) wait_bar<1>(); else if constexpr (S == 1 || S == 14) wait_bar<0>(); else wait_bar<5>();
      TR(17 + S);
      GU8(f1, (gu_read<S + 1, 0>(pa, pp, pg, f0, 0)), (gu_read<S + 1, 0>(pa, pp, pg, f0, 1)), (gu_read<S + 1, 0>(pa, pp, pg, f0, 2)),
          if (S >= NG - 2 && S <= 12) fill(S + 3));
    } else {
      GU8(f1, , , , );
    }
    gu_stages<S + 1>(pa, pp, pg, f0, f1, g, u, fill ST_PASS);
  }
}

__global__ __launch_bounds__(512, 2) void k_all(const char* __restrict__ gx, const char* __restrict__ gwqkv, const char* __restrict__ gwo,
                                                const char* __restrict__ gwg, const char* __restrict__ gwu, const char* __restrict__ gwd,
                                                char* __restrict__ ws, char* __restrict__ out) {
  extern __shared__ __attribute__((aligned(16))) char smem[];
  const lptr lds = (lptr)smem;
  const unsigned lds0 = (unsigned)(uintptr_t)lds;
  const int lane = threadIdx.x & 63, wid = RFL(threadIdx.x >> 6), b = blockIdx.x;
  const int xcd = b & 7, bi = b >> 3;
  const int rb = (xcd >> 1) * 8 + (bi & 7);
  const int cb = (xcd & 1) * 4 + (bi >> 3);
  const int rot = (rb + 2 * cb) & 7;
  const int head = xcd * 4 + (bi >> 3);
  const int r = lane & 31, h = lane >> 5;
  const unsigned trn = ((lane & 15) >> 2) * 64 + ((lane >> 4) & 1) * 32 + (lane & 3) * 8;
  const unsigned voff = lane * 16;
  ST_DECL;
  unsigned cpf;
  {
    u64 pc; unsigned len;
    asm volatile("s_getpc_b64 %0\nLk_here:\n\ts_mov_b32 %1, Lk_tail-Lk_here" : "=s"(pc), "=s"(len));
    const unsigned o = (unsigned)((bi * 8 + wid) * CPF_LANES + (lane < CPF_LANES ? lane : 0)) * 128;
    asm volatile("global_load_dword %0, %1, off" : "=v"(cpf) : "v"((const char*)pc + (o + 4 <= len ? o : 0)) : "memory");
  }
  __builtin_amdgcn_sched_barrier(0);
  ST(0);

  const rsrc_t rx = mk_rsrc(gx, S_ * D_ * 2), rw = mk_rsrc(gwqkv, D_ * 3 * D_ * 2), rxn = mk_rsrc(ws + WS_XN + (cb >> 2) * 2097152, 2097152);
  u32x4 xr[8];
  {
    const unsigned vx = (lane >> 3) * 1024 + (lane & 7) * 16, sx = (unsigned)(rb * 64 + wid * 8) * 1024;
#pragma unroll
    for (int i = 0; i < 8; ++i) BLOAD(xr[i], vx, rx, sx + ((i + rot) & 7) * 128);
  }
  BAR();
  const unsigned vw3 = ((lane >> 2) & 7) * 3072 + (lane >> 5) * 64 + (lane & 3) * 16;
  auto issue_wk = [&](int s, int kb) {
    const unsigned so = (unsigned)(kb * 8) * 3072 + ((s + rot) & 7) * (64 * 3072);
    dma16x3(rw, vw3, so + cb * 128, lds0 + WR + (s & 3) * QSLOT + kb * 3072);
  };
  auto issue_w = [&](int s) { issue_wk(s, wid); };
  issue_w(0); issue_w(1);
  unsigned pf;
#define TOUCH() asm volatile("global_load_dword %0, %1, off" : "=v"(pf) : "v"((h ? gwu : gwg) + (size_t)((b * 8 + wid) * 7 + (r < 7 ? r : 0)) * 128) : "memory")
  const u64 tag = read_tag(ws);
  float rs, mr;
  {
    const unsigned ONE2 = 0x3f803f80u;
    float s0 = 0.f, s1 = 0.f, q0 = 0.f, q1 = 0.f;
#define STAT(i) do { wait_vm1<13 + T_EARLY - (i)>(xr[i]); \
      s0 = dot2(xr[i][0], ONE2, s0); q0 = dot2(xr[i][0], xr[i][0], q0); s1 = dot2(xr[i][1], ONE2, s1); q1 = dot2(xr[i][1], xr[i][1], q1); \
      s0 = dot2(xr[i][2], ONE2, s0); q0 = dot2(xr[i][2], xr[i][2], q0); s1 = dot2(xr[i][3], ONE2, s1); q1 = dot2(xr[i][3], xr[i][3], q1); \
      asm volatile("" : "+v"(s0), "+v"(s1), "+v"(q0), "+v"(q1)); } while (0)
    STAT(0); STAT(1); STAT(2); STAT(3); STAT(4); STAT(5); STAT(6); STAT(7);
    ST(1);
    const float mean = sum8(s0 + s1) * (1.0f / 512.0f);
    rs = __builtin_amdgcn_rsqf(__builtin_fmaxf(sum8(q0 + q1) * (1.0f / 512.0f) - mean * mean, 0.0f) + 1e-5f);
    mr = -mean * rs;
  }
  const unsigned xo = (lane & 7) * 512 + ((((wid & 3) * 8 + (lane >> 3)) ^ (lane & 7)) * 16);
  const lptr pxw = lds + (wid >> 2) * 32768 + xo;
#pragma unroll
  for (int j = 0; j < 8; ++j) {
    u32x4 n;
#pragma unroll
    for (int i = 0; i < 4; ++i) n[i] = pk2(__builtin_fmaf(bf_lo(xr[j][i]), rs, mr), __builtin_fmaf(bf_hi(xr[j][i]), rs, mr));
    if (j < 2) BSTORE_DEV(n, rxn, (unsigned)(rb * 8 + ((rot + j) & 7)) * 8192 + (wid >> 2) * 4096 + xo);
    *(LAS u32x4*)(pxw + j * 4096) = n;
    if (j == W2_AT) issue_w(2);
    if (j == W3_AT) issue_w(3);
  }
  TOUCH();
  const bool has_ffn = cb < 7;
  const int fb = cb * 2 + (rb & 1), w4 = wid & 3;
  const rsrc_t rg = mk_rsrc(gwg - 1024, D_ * F_ * 2 + 1024), ru = mk_rsrc(gwu - 1024, D_ * F_ * 2 + 1024);
  const unsigned vwf = ((lane >> 2) & 7) * (F_ * 2) + (lane >> 5) * 64 + (lane & 3) * 16;
  auto gu_w = [&](int s) {
    const unsigned so = (unsigned)(w4 * 8) * (F_ * 2) + fb * 256 + (unsigned)((s + 2 * rot) & 15) * (32 * F_ * 2) + 1024;
    const unsigned d = lds0 + WR + (s % 3) * GSLOT + w4 * 2048;
    dma16x2(rg, vwf, so, so + 128, d); dma16x2(ru, vwf, so, so + 128, d + 8192);
  };
  auto gu_x = [&](int s) {
    dma16_dev(rxn, voff, (unsigned)((rb ^ 1) * 8 + (((s >> 1) + rot) & 7)) * 8192 + (w4 >> 1) * 4096 + (s & 1) * 2048 + (w4 & 1) * 1024,
              lds0 + WR + (s % 3) * GSLOT + 16384 + w4 * 1024);
  };
  auto gu_fill = [&](int s) { gu_w(s); gu_x(s); };
  u64 fl;
  lptr pa[4];
#pragma unroll
  for (int g = 0; g < 4; ++g) pa[g] = lds + (2 * g + h) * 512 + ((r ^ (2 * g + h)) * 16);
  WAIT_BAR(12);
  ST(2);

  const rsrc_t rkv = mk_rsrc(ws + WS_KV + (size_t)head * 131072, 131072), rh = mk_rsrc(ws + WS_H, 7340032);
  const unsigned vvo = h * 256 + (r & 15) * 16;
  const bf16x8 ONES = {0x3f80, 0x3f80, 0x3f80, 0x3f80, 0x3f80, 0x3f80, 0x3f80, 0x3f80};
  const bf16x8 PAD = ((r & 27) == 16) ? ONES : (bf16x8){};
  const int qt0 = (bi & 7) * 8 + w4 * 2;
  const unsigned ring = lds0 + (wid < 4 ? KVA : KVB), kt0 = wid < 4 ? 0 : 32;
  const unsigned kvm = ring + w4 * REC, kvs = (kt0 + w4) * 2048;
  u32x4 QAu, QBu, S0u, S1u, Ku, V0u;
  auto att_loads = [&] {
    const rsrc_t rq = mk_rsrc(ws + WS_Q, 2097152);
    BLOAD_DEV(QAu, voff, rq, (unsigned)(head * 64 + qt0) * 1024);
    BLOAD_DEV(QBu, voff, rq, (unsigned)(head * 64 + qt0 + 1) * 1024);
    BLOAD_DEV(S0u, voff, rkv, 63u * 2048);
    BLOAD_DEV(S1u, voff, rkv, 0u);
    BLOAD_DEV(Ku, voff, rkv, ((kt0 + 63) & 63) * 2048);
    BLOAD_DEV(V0u, vvo, rkv, ((kt0 + 63) & 63) * 2048 + 1024);
    dma16_dev(rkv, voff, kvs, kvm); dma16_dev(rkv, voff, kvs + 1024, kvm + 1024);
    dma16_dev(rkv, voff, kvs + 8192, kvm + CHUNK); dma16_dev(rkv, voff, kvs + 8192 + 1024, kvm + CHUNK + 1024);
    if (w4 == 0 && r < 16) *(LAS bf16x8*)(lds + (ring - lds0) + r * REC + 2048 + h * 16) = h ? (bf16x8){} : ONES;
  };
#define ATT_WAIT(N) do { asm volatile("s_waitcnt vmcnt(" #N ")" ::: "memory"); __builtin_amdgcn_sched_barrier(0); asm volatile("" : "+v"(QAu), "+v"(QBu), "+v"(S0u), "+v"(S1u), "+v"(Ku), "+v"(V0u) :: "memory"); } while (0)
  bf16x8 QA, QB, K, V0, V1, P1;
  f32x16 SA, CA, CB, OA, OB;
  auto att_init = [&] {
    QA = __builtin_bit_cast(bf16x8, QAu); QB = __builtin_bit_cast(bf16x8, QBu); K = __builtin_bit_cast(bf16x8, Ku);
    V0 = (r >= 16) ? PAD : __builtin_bit_cast(bf16x8, V0u);
    const f32x16 z = {};
    const f32x16 a0 = MFMA(__builtin_bit_cast(bf16x8, S0u), QA, z), a1 = MFMA(__builtin_bit_cast(bf16x8, S1u), QA, z);
    const f32x16 b0 = MFMA(__builtin_bit_cast(bf16x8, S0u), QB, z), b1 = MFMA(__builtin_bit_cast(bf16x8, S1u), QB, z);
    float ma = a0[0], mb = b0[0];
#pragma unroll
    for (int i = 0; i < 16; ++i) { ma = __builtin_fmaxf(__builtin_fmaxf(ma, a0[i]), a1[i]); mb = __builtin_fmaxf(__builtin_fmaxf(mb, b0[i]), b1[i]); }
    ma = -(xhalf_max(ma) + MBIAS); mb = -(xhalf_max(mb) + MBIAS);
#pragma unroll
    for (int i = 0; i < 16; ++i) { CA[i] = ma; CB[i] = mb; }
    SA = MFMA(K, QA, CA);
    asm volatile("s_nop 15" : "+v"(SA));
    V1 = (bf16x8){}; P1 = (bf16x8){}; OA = z; OB = z;
  };
  const unsigned apad = ((r & 27) == 16) ? 2048 : 2064;
  const unsigned aK = ring + voff, aV0 = ring + (r < 16 ? 1024 + vvo : apad), aV1 = ring + (r < 16 ? 1536 + vvo : apad);
  unsigned t0, keep;

  if (wid < 4) {
    {
      const int rt = wid & 1, ch = wid >> 1;
      lptr par[4];
#pragma unroll
      for (int g = 0; g < 4; ++g) par[g] = pa[g] + rt * 32768;
      lptr pw = lds + WR + h * 3072 + ch * 512 + trn, pw2 = pw + 2 * QSLOT;
      asm volatile("" : "+v"(pw2));
      const int pair = cb * 2 + ch, tile = rb * 2 + rt;
      f32x16 aq = {}, ak = {}, av = {};
      QFrag f0, f1;
      f0.x = lds_frag(par[0]); f0.q = lds_frag_tr(pw, pw + 256); f0.k = lds_frag_tr(pw + 1024, pw + 1024 + 256); f0.v = lds_frag_tr(pw + 2048, pw + 2048 + 256);
      qkv_steps<0>(par, pw, pw2, f0, f1, aq, ak, av, ws + WS_FX + (rb * 8 + cb) * 8, tag, threadIdx.x ST_PASS);
      ST(3);
      const rsrc_t rq = mk_rsrc(ws + WS_Q, 2097152 + 4194304);
      const unsigned ko = 2097152 + (unsigned)((2 * pair) * 64 + ((tile + 63) & 63)) * 2048 + voff;
      BSTORE_DEV(pack8(ak, 0), rq, ko);
      BSTORE_DEV(pack8(ak, 8), rq, ko + 131072);
      const unsigned vo = 2097152 + (unsigned)(2 * pair + (r >> 4)) * 131072 + vvo;
      BSTORE_DEV(pack8(av, 0), rq, vo + ((tile + 63) & 63) * 2048 + 1024);
      BSTORE_DEV(pack8(av, 8), rq, vo + tile * 2048 + 1536);
#pragma unroll
      for (int i = 0; i < 16; ++i) aq[i] *= QSCALE;
      const unsigned qo = (unsigned)((2 * pair) * 64 + tile) * 1024 + voff;
      BSTORE_DEV(pack8(aq, 0), rq, qo);
      BSTORE_DEV(pack8(aq, 8), rq, qo + 65536);
    }
    BAR();
#pragma unroll 1
    for (int s = 0; s <= NG - 3; ++s) {
      if (s < 2) BAR(); else WAIT_BAR(5);
      if (s == FQ_AT) {
        wait_vm<5 * FQ_AT>();
        if (lane == 0) set_flag(ws + WS_FQ + ((cb * 2 + (wid >> 1)) * 64 + rb * 2 + (wid & 1)) * 8, tag);
      }
      gu_fill(s + 3);
    }
    asm volatile("global_load_dwordx2 %0, %1, off sc1" : "=v"(fl) : "v"(ws + WS_FQ + ((head >> 1) * 64 + lane) * 8) : "memory");
    WAIT_BAR(6);
    wait_vm<0>(); __builtin_amdgcn_sched_barrier(0); asm volatile("" : "+v"(fl) :: "memory");
    if (!__all(fl == tag)) wait_flags(ws + WS_FQ + (head >> 1) * 512, 64, tag, lane);
    ST(4);
    att_loads();
    BAR();
    ATT_WAIT(4);
    att_init();
    ST(5);
    asm volatile(ATT_BEGIN "s_setprio 2\n\t"
                 VB(2) T_KVI("0", 16640, 16384) T_NONE("2080") SB_ T_NONE("4160") T_NONE("6240")
                 VB(2) T_KVI("8320", 24960, 24576) T_NONE("10400") SB_ T_NONE("12480") T_NONE("14560")
                 VB(2) T_KVI("16640", 0, 32768) T_NONE("18720") SB_ T_NONE("20800") T_NONE("22880")
                 VB(2) T_KVI("24960", 8320, 40960) T_NONE("27040") SB_ T_NONE("29120") T_NONE("31200")
                 ATT_END
                 : ATT_STATE : ATT_IN, [rkv] "s"(rkv), [kvm] "s"(kvm), [kvs] "s"(kvs) : ATT_CLOB);
    __builtin_amdgcn_s_setprio(0);
    wait_vm<2>();
  } else {
    auto w2 = [&](int s) { issue_wk(s, w4 * 2); issue_wk(s, w4 * 2 + 1); };
    WAIT_BAR(9);  w2(4);
    WAIT_BAR(10); w2(5);
    WAIT_BAR(13); w2(6);
    WAIT_BAR(12); w2(7);
    WAIT_BAR(12);
    asm volatile("global_load_dwordx2 %0, %1, off sc1" : "=v"(fl) : "v"(ws + WS_FX + ((rb ^ 1) * 8 + (cb & 4) + (lane & 3)) * 8) : "memory");
    gu_w(0);
    WAIT_BAR(11); gu_w(1);
    do { asm volatile("s_waitcnt vmcnt(8) lgkmcnt(0)\n\ts_barrier" ::: "memory"); __builtin_amdgcn_sched_barrier(0); asm volatile("" : "+v"(fl) :: "memory"); } while (0);
    const bool xok = __all(fl == tag);
    if (xok) gu_x(0);
    gu_w(2);
    if (xok) { gu_x(1); gu_x(2); }
    BAR();
    if (xok) wait_vm<6>();
    else { wait_flags(ws + WS_FX + ((rb ^ 1) * 8 + (cb & 4)) * 8, 4, tag, lane); gu_x(0); gu_x(1); gu_x(2); wait_vm<2>(); }
    ST(3);
    BAR();
    f32x16 g[4], u[4];
    if (has_ffn) {
      lptr pp[4];
#pragma unroll
      for (int i = 0; i < 4; ++i) { pp[i] = pa[i] + (WR + 16384 - (i >> 1) * 2048); asm volatile("" : "+v"(pp[i])); g[i] = (f32x16){}; u[i] = (f32x16){}; }
      const lptr pg = lds + WR + h * 2048 + (w4 >> 1) * 1024 + (w4 & 1) * 512 + trn;
      GFrag f0, f1;
      gu_read<0, 0>(pa, pp, pg, f0, 0); gu_read<0, 0>(pa, pp, pg, f0, 1); gu_read<0, 0>(pa, pp, pg, f0, 2);
      TR(16);
      gu_stages<0>(pa, pp, pg, f0, f1, g, u, gu_fill ST_PASS);
      TR(32);
    } else {
      unsigned d[5];
#pragma unroll 1
      for (int i = 0; i < NG + 1; ++i) BAR();
#pragma unroll
      for (int i = 0; i < 5; ++i) {
        const int t = (rb * 4 + w4) * 144 + i * 32 + (r + i * 32 < 144 ? r : 0);
        const char* p = (t < 14336) ? gwd + (size_t)t * 128 : gwo + (size_t)(t - 14336) * 128;
        asm volatile("global_load_dword %0, %1, off" : "=v"(d[i]) : "v"(p) : "memory");
      }
#pragma unroll 1
      for (int i = NG + 1; i < 15; ++i) BAR();
      do { asm volatile("s_waitcnt vmcnt(0)" ::: "memory"); __builtin_amdgcn_sched_barrier(0); asm volatile("" : "+v"(d[0]), "+v"(d[1]), "+v"(d[2]), "+v"(d[3]), "+v"(d[4]) :: "memory"); } while (0);
    }
    ST(4);
    BAR();
    __builtin_amdgcn_s_setprio(3);
    TR(33);
    att_loads();
    TR(34);
    auto swiglu = [&](int i) {
#pragma unroll
      for (int j = 0; j < 16; ++j) g[i][j] = g[i][j] * u[i][j] * __builtin_amdgcn_rcpf(1.0f + __builtin_amdgcn_exp2f(g[i][j] * -1.4426950408889634f));
      const unsigned ho = has_ffn ? (unsigned)(((rb ^ (i >> 1)) * 2 + (i & 1)) * 112 + fb * 8 + w4 * 2) * 1024 + voff : 0x7ff00000u;
      BSTORE_DEV(pack8(g[i], 0), rh, ho);
      BSTORE_DEV(pack8(g[i], 8), rh, ho + 1024);
    };
    if (has_ffn) { swiglu(0); swiglu(1); }
    TR(35);
#pragma unroll 1
    for (int i = 0; i < NG - 8; ++i) BAR();
    TR(36);
    if (has_ffn) { swiglu(2); swiglu(3); } else wait_vm<0>();
    TR(37);
    ATT_WAIT(8);
    att_init();
    TR(38);
    ST(5);
  }
  asm volatile("" :: "v"(pf), "v"(cpf));
  const int rt = wid & 1, kq = wid >> 1;
  const int drot = (((bi >> 3) * 7 + 1) / 2 + (bi & 7) * 2) % 14;
  const rsrc_t rwd = mk_rsrc(gwd, F_ * D_ * 2), rwo = mk_rsrc(gwo, D_ * D_ * 2), ro = mk_rsrc(ws + WS_O, 2097152);
  const unsigned vw1 = ((lane >> 2) & 7) * 1024 + (lane >> 5) * 64 + (lane & 3) * 16;
  auto dsrc = [&](int st) { const int t = st + drot; return t >= 14 ? t - 14 : t; };
#define DADDR(st) ((st) < 5 ? (st) * 16384 : (st) == 5 || (st) == 12 ? KVA : (st) < 12 ? ((st) - 6) * 16384 : (st) == 13 ? 0 : \
                   (st) == 14 ? 147456 : (st) == 15 ? 98304 : ((st) - 15) * 16384)
  auto d_so = [&](int st, int pc) { return (unsigned)((st < 14 ? dsrc(st) : st - 14) * 128 + pc * 16) * 1024 + cb * 128; };
  auto d_m = [&](int st, int pc) { return lds0 + DADDR(st) + pc * 2048; };
  auto issue_dp = [&](int st, int pc) { dma16x2(st < 14 ? rwd : rwo, vw1, d_so(st, pc), d_so(st, pc) + 8192, d_m(st, pc)); };
  auto h_late = [&](int e) { return (unsigned)((rb * 2 + rt) * 112 + dsrc(7 + e) * 8 + kq * 2) * 1024; };
  auto h_so = [&](int e) {
    return wid < 4 ? (unsigned)((rb * 2 + rt) * 112 + dsrc(e >> 1) * 8 + kq * 4 + (e & 1) * 2) * 1024 : h_late(e);
  };
  const unsigned kvd = kvs + (wid < 4 ? 4 * 8192 : 0);
  u32x4 ha[14], hb[14], xv;
  const unsigned kve = wid < 4 ? 0x7ff00000u : kvd;
  LAS f32x4* ex = (LAS f32x4*)(lds + 147456 + w4 * 4096) + lane;
  LAS float* el = (LAS float*)(lds + 145408 + w4 * 512) + lane;
  TR(39);
  ST(7); STC(14);
  asm volatile(ATT_BEGIN "s_setprio 0\n\ts_waitcnt lgkmcnt(0)\n\t" SB_ AT(40)
               T_KVI("0", 16640, 16384) T_WD("2080", m0_, s0_) T_WD("4160", m1_, s1_) T_WD("6240", m2_, s2_)
               AT(41) VB(8) AT(42)
               "buffer_store_dwordx2 %[tag], %[fho], %[rfl], 0 offen sc1\n\t"
               T_KVI("8320", 24960, 24576) T_WD("10400", m3_, s3_) T_WD("12480", m4_, s4_) T_NONE("14560")
               AT(43) VB(7) AT(44)
               T_KV("16640", kvm, s5_) T_NONE("18720") T_NONE("20800")
               TILE("22880", "", "buffer_load_dwordx2 %[fl], %[fhi], %[rfl], 0 offen sc1\n\t", "", "", "") AT(45)
               ATT_END
               : ATT_STATE, [fl] "=&v"(fl)
               : ATT_IN, [rkv] "s"(rkv), [rwd] "s"(rwd), [vw1] "v"(vw1), [kvm] "s"(kvm), [kvs] "s"(kvd),
                 [m0_] "s"(d_m(0, wid)), [s0_] "s"(d_so(0, wid)), [m1_] "s"(d_m(1, wid)), [s1_] "s"(d_so(1, wid)), [m2_] "s"(d_m(2, wid)), [s2_] "s"(d_so(2, wid)),
                 [m3_] "s"(d_m(3, wid)), [s3_] "s"(d_so(3, wid)), [m4_] "s"(d_m(4, wid)), [s4_] "s"(d_so(4, wid)), [s5_] "s"(kve + 4 * 8192),
                 [rfl] "s"(mk_rsrc(ws + WS_FH, 2048)), [tag] "v"(tag), [fho] "v"(threadIdx.x == 0 ? (unsigned)(rb * 8 + cb) * 8 : 0x7ff00000u),
                 [fhi] "v"((unsigned)((rb >> 1) * 16 + (lane & 15)) * 8) : ATT_CLOB);
  PIN_AFTER("s_waitcnt vmcnt(0)", "+v"(fl));
  if (__builtin_expect(!__all(fl == tag), 0)) wait_flags(ws + WS_FH + (rb >> 1) * 128, 16, tag, lane);
#define H_OUT(n, st) [a##n] "=&v"(ha[st]), [b##n] "=&v"(hb[st])
#define H_IN(n, st) [s##n] "s"(h_so(st))
  f32x4 p0, p1, p2, p3;
  float pl0, pl1;
  asm volatile(ATT_BEGIN SB_ AT(46)
               TILE("24960", "s_add_u32 m0, %[kvm], 8320\n\ts_add_u32 %[t0], %[kvs], 40960\n\t", "buffer_load_dwordx4 %[voff], %[rkv], %[t0] offen sc1 lds\n\t",
                    "buffer_load_dwordx4 %[voff], %[rkv], %[t0] offen offset:1024 sc1 lds\n\t", HL(a0, s0, ""), HL(b0, s0, "offset:1024 "))
               T_H2("27040", a1, b1, s1, a2, b2, s2) T_H2("29120", a3, b3, s3, a4, b4, s4) T_H2("31200", a5, b5, s5, a6, b6, s6)
               AT(47) "s_cmp_lt_u32 %[wid], 4\n\ts_cbranch_scc1 L_skip_%=\n\t"
               "s_setprio 2\n\t"
               SB_ AT(48) T_KVI("0", 16640, 49152) T_NONE("2080") SB_ AT(49) T_NONE("4160") T_NONE("6240")
               VB(2) AT(50) T_KVI("8320", 24960, 57344)
               TILE("10400", "", "", "", "ds_read_b128 %[p0], %[ex]\n\tds_read_b128 %[p1], %[ex] offset:1024\n\tds_read_b32 %[pl0], %[el]\n\t", "")
               SB_ AT(51)
               TILE("12480", "", "", "", "ds_read_b128 %[p2], %[ex] offset:2048\n\tds_read_b128 %[p3], %[ex] offset:3072\n\tds_read_b32 %[pl1], %[el] offset:256\n\t", "")
               T_NONE("14560")
               VB(2) AT(52) T_NONE("16640") T_NONE("18720") SB_ AT(53) T_NONE("20800") T_NONE("22880")
               VB(0) AT(54) T_NONE("24960") T_NONE("27040") SB_ AT(55) T_NONE("29120") T_NONE("31200") AT(56)
               "L_skip_%=:\n\t"
               ATT_END
               : ATT_STATE, H_OUT(0, 0), H_OUT(1, 1), H_OUT(2, 2), H_OUT(3, 3), H_OUT(4, 4), H_OUT(5, 5), H_OUT(6, 6),
                 [p0] "=&v"(p0), [p1] "=&v"(p1), [p2] "=&v"(p2), [p3] "=&v"(p3), [pl0] "=&v"(pl0), [pl1] "=&v"(pl1)
               : ATT_IN, [rkv] "s"(rkv), [kvm] "s"(kvm), [kvs] "s"(kve), [wid] "s"(wid), [ex] "v"(ex), [el] "v"(el),
                 [rh] "s"(rh), H_IN(0, 0), H_IN(1, 1), H_IN(2, 2), H_IN(3, 3), H_IN(4, 4), H_IN(5, 5), H_IN(6, 6) : ATT_CLOB);
  ST(6);

  const int t = threadIdx.x;
  const unsigned off = (unsigned)(rb * 64 + (t >> 3)) * 1024 + cb * 128 + (t & 7) * 16;
  f32x16 d0, d1;
  F4 e0, e1;
#define D_PTR(pd, a) ((a) < 65536 ? pd[0] + (a) : (a) < KVA ? pd[1] + ((a) - 65536) : pd[2] + ((a) - KVA))
#define D_READ_A(n, w_) do { n.w0 = lds_frag_tr(w_, w_ + 1024); n.w1 = lds_frag_tr(w_ + 512, w_ + 1536); } while (0)
#define D_READ_B(n, w_) do { n.x0 = lds_frag_tr(w_ + 2048, w_ + 3072); n.x1 = lds_frag_tr(w_ + 2560, w_ + 3584); } while (0)
#define D_STEPX(c, n, nxt, A, B, RD, X, Y) do { \
    MM(d0, c.w0, __builtin_bit_cast(bf16x8, A)); if (RD) D_READ_A(n, (nxt)); MM(d1, c.w1, __builtin_bit_cast(bf16x8, A)); if (RD) D_READ_B(n, (nxt)); \
    MM(d0, c.x0, __builtin_bit_cast(bf16x8, B)); X; MM(d1, c.x1, __builtin_bit_cast(bf16x8, B)); Y; } while (0)
#define H_LOAD(e, so) do { BLOAD_DEV(ha[e], voff, rh, so); BLOAD_DEV(hb[e], voff, rh, (so) + 1024); } while (0)
  PIN_AFTER("s_waitcnt vmcnt(0)", "+v"(ha[0]), "+v"(ha[1]), "+v"(ha[2]), "+v"(ha[3]), "+v"(ha[4]), "+v"(ha[5]), "+v"(ha[6]),
            "+v"(hb[0]), "+v"(hb[1]), "+v"(hb[2]), "+v"(hb[3]), "+v"(hb[4]), "+v"(hb[5]), "+v"(hb[6]));
  OB = MFMA(V1, P1, OB);
  if (wid < 4) {
    lptr pe[3];
    pe[0] = lds + kq * 8192 + h * 256 + trn; pe[1] = pe[0] + 65536; pe[2] = pe[0] + KVA;
    d0 = d1 = (f32x16){};
    asm volatile("" : "+v"(pe[0]), "+v"(pe[1]), "+v"(pe[2]));
#define EP(e) D_PTR(pe, DADDR((e) >> 1) + ((e) & 1) * 4096)
#define E_DMA(st, i) issue_dp(st, wid * 2 + (i))
#define E_DMA2(st) do { E_DMA(st, 0); E_DMA(st, 1); } while (0)
#define E_STAGE(k, X0, Y0, X1, Y1) do { D_STEPX(e0, e1, EP(2 * (k) + 1), ha[2 * (k)], hb[2 * (k)], true, X0, Y0); \
      D_STEPX(e1, e0, EP(2 * (k) + 2), ha[2 * (k) + 1], hb[2 * (k) + 1], (k) < 6, X1, Y1); } while (0)
#define HE(e) H_LOAD(e, h_so(e))
#define HL_(e) H_LOAD(e, h_late(e))
    D_READ_A(e0, EP(0)); D_READ_B(e0, EP(0));
    E_STAGE(0, HE(7), HE(8), HE(9), );
    BAR();
    ex[0] = V4(OA, 0); ex[64] = V4(OA, 1); ex[128] = V4(OB, 0); ex[192] = V4(OB, 1); el[0] = OA[8]; el[64] = OB[8];
    E_STAGE(1, E_DMA(5, 0), E_DMA(5, 1), E_DMA(6, 0), E_DMA(6, 1));
    BAR();
    E_STAGE(2, E_DMA(7, 0), E_DMA(7, 1), HE(10), HE(11));
    PIN_AFTER("s_waitcnt vmcnt(8) lgkmcnt(0)\n\ts_barrier", "+v"(ha[7]), "+v"(hb[7]), "+v"(ha[8]), "+v"(hb[8]), "+v"(ha[9]), "+v"(hb[9]));
    E_STAGE(3, E_DMA(8, 0), E_DMA(8, 1), HE(12), HE(13));
    BAR();
    E_STAGE(4, E_DMA(9, 0), E_DMA(9, 1), E_DMA(11, 0), E_DMA(11, 1));
    PIN_AFTER("s_waitcnt vmcnt(8) lgkmcnt(0)\n\ts_barrier", "+v"(ha[10]), "+v"(hb[10]), "+v"(ha[11]), "+v"(hb[11]), "+v"(ha[12]), "+v"(hb[12]),
              "+v"(ha[13]), "+v"(hb[13]));
    E_STAGE(5, E_DMA(10, 0), E_DMA(10, 1), HL_(0), HL_(1));
    BAR();
    E_STAGE(6, E_DMA(12, 0), E_DMA(12, 1), HL_(2), HL_(3));
    BAR();
    E_DMA2(13); E_DMA2(14); HL_(4);
    BAR();
    E_DMA2(15); HL_(5); HL_(6);
    if (b == 193 && wid == 0) {
#pragma unroll 1
      for (int i = 0; i < 4; ++i) wait_flags(ws + WS_FH + i * 512, 64, tag, lane);
      if (lane == 0) set_flag(ws + WS_EPOCH, tag);
    }
    PIN_AFTER("s_waitcnt vmcnt(0)", "+v"(ha[0]), "+v"(ha[1]), "+v"(ha[2]), "+v"(ha[3]), "+v"(ha[4]), "+v"(ha[5]), "+v"(ha[6]),
              "+v"(hb[0]), "+v"(hb[1]), "+v"(hb[2]), "+v"(hb[3]), "+v"(hb[4]), "+v"(hb[5]), "+v"(hb[6]));
    ST(8);
  } else {
    ST(8); STC(15);
    auto finish_o = [&](f32x16 O, const bf16x8& Q, int i) {
      float l = O[8], mag = __builtin_fabsf(O[0]);
#pragma unroll
      for (int j = 1; j < 8; ++j) mag += __builtin_fabsf(O[j]);
      const bool bad = !(l < 0x1p120f) || !(mag < 0x1p126f) || !(xhalf_max(mag) > 0x1p-100f);
      if (__builtin_expect(__any(bad), 0)) {
        const f32x16 z = {};
        float mt = -__builtin_inff();
#pragma unroll 1
        for (int tt = 0; tt < 64; ++tt) {
          const f32x16 s = MFMA(__builtin_bit_cast(bf16x8, __builtin_amdgcn_raw_buffer_load_b128(rkv, voff, tt * 2048, 16)), Q, z);
#pragma unroll
          for (int j = 0; j < 16; ++j) mt = __builtin_fmaxf(mt, s[j]);
        }
        mt = xhalf_max(mt);
        O = z;
#pragma unroll 1
        for (int tt = 0; tt < 64; ++tt) {
          f32x16 s = MFMA(__builtin_bit_cast(bf16x8, __builtin_amdgcn_raw_buffer_load_b128(rkv, voff, ((tt + 63) & 63) * 2048, 16)), Q, z);
          bf16x8 v0 = __builtin_bit_cast(bf16x8, __builtin_amdgcn_raw_buffer_load_b128(rkv, vvo, ((tt + 63) & 63) * 2048 + 1024, 16));
          bf16x8 v1 = __builtin_bit_cast(bf16x8, __builtin_amdgcn_raw_buffer_load_b128(rkv, vvo, tt * 2048 + 1536, 16));
          if (r >= 16) { v0 = PAD; v1 = PAD; }
#pragma unroll
          for (int j = 0; j < 16; ++j) s[j] = __builtin_amdgcn_exp2f(s[j] - mt);
          O = MFMA(v0, __builtin_bit_cast(bf16x8, pack8(s, 0)), O);
          O = MFMA(v1, __builtin_bit_cast(bf16x8, pack8(s, 8)), O);
        }
        l = O[8];
      }
      const float il = __builtin_amdgcn_rcpf(l);
#pragma unroll
      for (int j = 0; j < 8; ++j) O[j] *= il;
      BSTORE_DEV(pack8(O, 0), ro, (unsigned)((qt0 + i) * 32 + head) * 1024 + voff);
    };
#pragma unroll
    for (int j = 0; j < 4; ++j) { OA[j] += p0[j]; OA[4 + j] += p1[j]; OB[j] += p2[j]; OB[4 + j] += p3[j]; }
    OA[8] += pl0; OB[8] += pl1;
    finish_o(OA, QA, 0); finish_o(OB, QB, 1);
    d0 = d1 = (f32x16){};
    asm volatile("" : "+v"(d0), "+v"(d1));
    __builtin_amdgcn_s_setprio(0);
  }
  lptr pd[3];
  pd[0] = lds + kq * 4096 + h * 256 + trn; pd[1] = pd[0] + 65536; pd[2] = pd[0] + KVA;
  asm volatile("" : "+v"(pd[0]), "+v"(pd[1]), "+v"(pd[2]));
#define DP(st) D_PTR(pd, DADDR(st))
#define FO_LOAD() asm volatile("global_load_dwordx2 %0, %1, off sc1" : "=v"(fl) : "v"(ws + WS_FO + ((rb * 2 + rt) * 32 + r) * 8) : "memory")
  u32x4 oa[4], ob[4];
#define O_LOAD(j, base) do { BLOAD_DEV(oa[j], voff, ro, (base) + (j) * 8192); BLOAD_DEV(ob[j], voff, ro, (base) + (j) * 8192 + 1024); } while (0)
  const unsigned obase = (unsigned)((rb * 2 + rt) * 32 + kq * 2) * 1024;
  BAR();
  ST(9);
  D_READ_A(e0, DP(7)); D_READ_B(e0, DP(7));
  if (wid >= 4) {
    wait_vm<0>();
    if (lane < 2) set_flag(ws + WS_FO + ((qt0 + lane) * 32 + head) * 8, tag);
  }
  D_STEPX(e0, e1, DP(8), ha[0], hb[0], true, , ); D_STEPX(e1, e0, DP(9), ha[1], hb[1], true, , );
  BAR();
  D_STEPX(e0, e1, DP(10), ha[2], hb[2], true, issue_dp(16, wid), BLOAD(xv, off, rx, 0u)); D_STEPX(e1, e0, DP(11), ha[3], hb[3], true, issue_dp(17, wid), );
  D_STEPX(e0, e1, DP(12), ha[4], hb[4], true, FO_LOAD(), ); D_STEPX(e1, e0, DP(13), ha[5], hb[5], true, , );
  PIN_AFTER("s_waitcnt vmcnt(0) lgkmcnt(0)\n\ts_barrier", "+v"(fl), "+v"(xv));
  const bool oready = __all(fl == tag);
  if (!oready) FO_LOAD();
  D_STEPX(e0, e1, DP(14), ha[6], hb[6], true, , );
  ST(10);
  if (!oready) {
    PIN_AFTER("s_waitcnt vmcnt(0)", "+v"(fl));
    if (!__all(fl == tag)) wait_flags(ws + WS_FO + (rb * 2 + rt) * 256, 32, tag, lane);
  }
  ST(11);
  O_LOAD(0, obase); O_LOAD(1, obase); O_LOAD(2, obase); O_LOAD(3, obase);
  wait_vm2<6>(oa[0], ob[0]); D_STEPX(e1, e0, DP(15), oa[0], ob[0], true, , );
  wait_vm2<4>(oa[1], ob[1]); D_STEPX(e0, e1, DP(16), oa[1], ob[1], true, , );
  wait_vm2<2>(oa[2], ob[2]); D_STEPX(e1, e0, DP(17), oa[2], ob[2], true, , );
  wait_vm2<0>(oa[3], ob[3]); D_STEPX(e0, e1, DP(17), oa[3], ob[3], false, , );
  {
    const lptr red = lds + (kq == 3 ? KVA : 49152 + kq * 16384) + (rt * 32 + r) * 256;
#pragma unroll
    for (int i = 0; i < 4; ++i) {
      *(LAS f32x4*)(red + (((2 * i + h) ^ (r & 15)) * 16)) = V4(d0, i);
      *(LAS f32x4*)(red + (((8 + 2 * i + h) ^ (r & 15)) * 16)) = V4(d1, i);
    }
  }
  BAR();
  f32x4 s0 = {bf_lo(xv[0]), bf_hi(xv[0]), bf_lo(xv[1]), bf_hi(xv[1])}, s1 = {bf_lo(xv[2]), bf_hi(xv[2]), bf_lo(xv[3]), bf_hi(xv[3])};
  {
    const int row = t >> 3, c = (t & 7) * 2;
    const lptr rd0 = lds + 49152 + row * 256 + ((c ^ (row & 15)) * 16), rd1 = lds + 49152 + row * 256 + (((c + 1) ^ (row & 15)) * 16);
#pragma unroll
    for (int p = 0; p < 4; ++p) {
      const int o = p == 3 ? KVA - 49152 : p * 16384;
      s0 += *(const LAS f32x4*)(rd0 + o); s1 += *(const LAS f32x4*)(rd1 + o);
    }
  }
  __builtin_amdgcn_raw_buffer_store_b128((u32x4){pk2(s0[0], s0[1]), pk2(s0[2], s0[3]), pk2(s1[0], s1[1]), pk2(s1[2], s1[3])}, mk_rsrc(out, S_ * D_ * 2), off, 0, OUT_AUX);
  asm volatile("Lk_tail:");
  ST(12); ST_OUT(ws); TR_OUT(ws);
}

extern "C" void kernel_launch(void* const* d_in, const int* in_sizes, int n_in, void* d_out, int out_size, void* d_ws,
                              size_t ws_size, hipStream_t stream) {
  (void)hipFuncSetAttribute((const void*)k_all, hipFuncAttributeMaxDynamicSharedMemorySize, LDS_BYTES);
  k_all<<<256, 512, LDS_BYTES, stream>>>((const char*)d_in[0], (const char*)d_in[1], (const char*)d_in[2], (const char*)d_in[4],
                                         (const char*)d_in[5], (const char*)d_in[6], (char*)d_ws, (char*)d_out);
}
```

```cpp
#include <hip/hip_runtime.h>
#include <hip/hip_bf16.h>
#include <stdint.h>
#include <type_traits>

typedef __attribute__((ext_vector_type(8))) short bf16x8;
typedef __attribute__((ext_vector_type(4))) short s16x4;
typedef __attribute__((ext_vector_type(16))) float f32x16;
typedef __attribute__((ext_vector_type(4))) float f32x4;
typedef __attribute__((ext_vector_type(2))) float f32x2;
typedef __attribute__((ext_vector_type(4))) unsigned u32x4;
typedef __attribute__((ext_vector_type(2))) unsigned u32x2;
typedef __attribute__((ext_vector_type(2))) __bf16 bf2;
typedef unsigned long long u64;
typedef __amdgpu_buffer_rsrc_t rsrc_t;
#define LAS __attribute__((address_space(3)))
#define GAS __attribute__((address_space(1)))
typedef LAS char* lptr;

#define MFMA(a, b, c) __builtin_amdgcn_mfma_f32_32x32x16_bf16((a), (b), (c), 0, 0, 0)
#define MM(c, a, b) do { __builtin_amdgcn_sched_barrier(0); c = MFMA(a, b, c); asm volatile("" : "+v"(c) :: "memory"); \
                         __builtin_amdgcn_sched_barrier(0); } while (0)
#define RFL(x) __builtin_amdgcn_readfirstlane(x)

constexpr int S_ = 2048, D_ = 512, F_ = 1792;
constexpr size_t WS_EPOCH = 0;
constexpr size_t WS_FH = 2048;
constexpr size_t WS_FQ = 16384;
constexpr size_t WS_FX = 49152;
constexpr size_t WS_FO = 24u << 20;
constexpr size_t WS_Q    = 65536;
constexpr size_t WS_KV   = WS_Q + 2097152;
constexpr size_t WS_O    = WS_KV + 4194304;
constexpr size_t WS_XN   = WS_O + 2097152;
constexpr size_t WS_H    = WS_XN + 4194304;
constexpr float QSCALE = 0.25f * 1.4426950408889634f;
constexpr float MBIAS = 80.0f;
constexpr int LDS_BYTES = 163840;
#define W2_AT 7
#define W3_AT 7
#define CPF_LANES 2
#define OUT_AUX 0
#define T_EARLY 0
#define SPREAD 0
#define ONE_POLL 1
#define FO_STRIDE 1
#define CSH 4
#define XS_ONE 1
#define IDLE_SLEEP 32
#define POLL_SLEEP 4
#define SPIN_CAP (1u << 16)

#define ST_DECL
#define ST(i)
#define STC(i)
#define TR(i)
#define TRD(i)
#define TR_OUT(wsp)
#define ST_OUT(wsp)
#define ST_ARG
#define ST_PASS

__device__ __forceinline__ rsrc_t mk_rsrc(const void* p, unsigned bytes) {
  return __builtin_amdgcn_make_buffer_rsrc((void*)p, (short)0, (int)bytes, 0x00020000);
}
__device__ __forceinline__ void dma16(rsrc_t r, unsigned voff, unsigned soff, unsigned lds) {
  unsigned keep;
  asm volatile("s_mov_b32 %0, m0\n\ts_mov_b32 m0, %1\n\ts_nop 0\n\tbuffer_load_dwordx4 %2, %3, %4 offen lds\n\ts_mov_b32 m0, %0"
               : "=&s"(keep) : "s"(lds), "v"(voff), "s"(r), "s"(soff) : "memory");
}
__device__ __forceinline__ void dma16_dev(rsrc_t r, unsigned voff, unsigned soff, unsigned lds) {
  unsigned keep;
  asm volatile("s_mov_b32 %0, m0\n\ts_mov_b32 m0, %1\n\ts_nop 0\n\tbuffer_load_dwordx4 %2, %3, %4 offen sc1 lds\n\ts_mov_b32 m0, %0"
               : "=&s"(keep) : "s"(lds), "v"(voff), "s"(r), "s"(soff) : "memory");
}
__device__ __forceinline__ void dma16x3(rsrc_t r, unsigned voff, unsigned soff, unsigned lds) {
  unsigned keep;
  asm volatile("s_mov_b32 %0, m0\n\ts_mov_b32 m0, %1\n\ts_nop 0\n\tbuffer_load_dwordx4 %2, %3, %4 offen lds\n\t"
               "buffer_load_dwordx4 %2, %3, %4 offen offset:1024 lds\n\tbuffer_load_dwordx4 %2, %3, %4 offen offset:2048 lds\n\ts_mov_b32 m0, %0"
               : "=&s"(keep) : "s"(lds), "v"(voff), "s"(r), "s"(soff) : "memory");
}
__device__ __forceinline__ float fma1(float a, float b, float c) {
  float d; asm("v_fma_f32 %0, %1, %2, %3" : "=v"(d) : "v"(a), "v"(b), "v"(c)); return d;
}
#define BLOAD(dst, voff, r, soff) asm volatile("buffer_load_dwordx4 %0, %1, %2, %3 offen" : "=v"(dst) : "v"(voff), "s"(r), "s"(soff) : "memory")
#define BLOAD_DEV(dst, voff, r, soff) asm volatile("buffer_load_dwordx4 %0, %1, %2, %3 offen sc1" : "=v"(dst) : "v"(voff), "s"(r), "s"(soff) : "memory")
#define BSTORE_DEV(val, r, voff) __builtin_amdgcn_raw_buffer_store_b128((val), (r), (voff), 0, 16)
#define WAIT_BAR(N) asm volatile("s_waitcnt vmcnt(" #N ") lgkmcnt(0)\n\ts_barrier" ::: "memory")
#define BAR() asm volatile("s_waitcnt lgkmcnt(0)\n\ts_barrier" ::: "memory")
template <int N> __device__ __forceinline__ void wait_bar() { asm volatile("s_waitcnt vmcnt(%0) lgkmcnt(0)\n\ts_barrier" :: "n"(N) : "memory"); }
template <int N> __device__ __forceinline__ void wait_vm2(u32x4& a, u32x4& b) {
  asm volatile("s_waitcnt vmcnt(%0)" :: "n"(N) : "memory"); __builtin_amdgcn_sched_barrier(0); asm volatile("" : "+v"(a), "+v"(b) :: "memory");
}
template <int N> __device__ __forceinline__ void wait_vm1(u32x4& a) {
  asm volatile("s_waitcnt vmcnt(%0)" :: "n"(N) : "memory"); __builtin_amdgcn_sched_barrier(0); asm volatile("" : "+v"(a) :: "memory");
}

__device__ __forceinline__ unsigned pk2(float lo, float hi) {
  f32x2 v = {lo, hi};
  return __builtin_bit_cast(unsigned, __builtin_convertvector(v, bf2));
}
__device__ __forceinline__ float bf_lo(unsigned u) { return __uint_as_float(u << 16); }
__device__ __forceinline__ float bf_hi(unsigned u) { return __uint_as_float(u & 0xffff0000u); }
__device__ __forceinline__ float xhalf_max(float v) {
  auto r = __builtin_amdgcn_permlane32_swap(__float_as_uint(v), __float_as_uint(v), false, false);
  return __builtin_fmaxf(__uint_as_float(r[0]), __uint_as_float(r[1]));
}
__device__ __forceinline__ bf16x8 lds_frag(lptr p) { return *(const LAS bf16x8*)p; }
typedef short v4i16_t __attribute__((ext_vector_type(4)));
__device__ __forceinline__ bf16x8 lds_frag_tr(lptr p0, lptr p1) {
  s16x4 a = __builtin_bit_cast(s16x4, __builtin_amdgcn_ds_read_tr16_b64_v4i16((LAS v4i16_t*)p0));
  s16x4 b = __builtin_bit_cast(s16x4, __builtin_amdgcn_ds_read_tr16_b64_v4i16((LAS v4i16_t*)p1));
  return (bf16x8){a[0], a[1], a[2], a[3], b[0], b[1], b[2], b[3]};
}
__device__ __forceinline__ float dot2(unsigned a, unsigned b, float c) {
  return __builtin_amdgcn_fdot2_f32_bf16(__builtin_bit_cast(bf2, a), __builtin_bit_cast(bf2, b), c, false);
}
__device__ __forceinline__ u32x4 pack8(const f32x16& a, int o) {
  return (u32x4){pk2(a[o], a[o + 1]), pk2(a[o + 2], a[o + 3]), pk2(a[o + 4], a[o + 5]), pk2(a[o + 6], a[o + 7])};
}
template <int CTRL> __device__ __forceinline__ float dpp_add(float x) {
  return x + __builtin_bit_cast(float, __builtin_amdgcn_mov_dpp(__builtin_bit_cast(int, x), CTRL, 0xf, 0xf, true));
}
__device__ __forceinline__ float sum8(float x) { return dpp_add<0x141>(dpp_add<0x4E>(dpp_add<0xB1>(x))); }

__device__ __forceinline__ u64 read_tag(const char* ws) {
  u64 e;
  asm volatile("s_load_dwordx2 %0, %1, 0x0\n\ts_waitcnt lgkmcnt(0)" : "=s"(e) : "s"(ws + WS_EPOCH) : "memory");
  e = (e ^ (e >> 31)) * 0x9E3779B97F4A7C15ull + 0x632BE59BD9B4E019ull;
  return e ^ (e >> 29);
}
__device__ __forceinline__ void set_flag(char* p, u64 tag) { __hip_atomic_store((GAS u64*)p, tag, __ATOMIC_RELAXED, __HIP_MEMORY_SCOPE_AGENT); }
__device__ __forceinline__ void wait_flags(const char* p, int n, u64 tag, int lane, int stride = 1) {
  GAS u64* f = (GAS u64*)p + (lane < n ? lane : 0) * stride;
#pragma unroll 1
  for (unsigned spins = 0; spins < SPIN_CAP; ++spins) {
    if (__all(__hip_atomic_load(f, __ATOMIC_RELAXED, __HIP_MEMORY_SCOPE_AGENT) == tag)) break;
    __builtin_amdgcn_s_sleep(POLL_SLEEP);
  }
  asm volatile("" ::: "memory");
}

__device__ __forceinline__ void dma16x2(rsrc_t r, unsigned voff, unsigned soff0, unsigned soff1, unsigned lds) {
  unsigned keep;
  asm volatile("s_mov_b32 %0, m0\n\ts_mov_b32 m0, %1\n\ts_nop 0\n\tbuffer_load_dwordx4 %2, %3, %4 offen lds\n\t"
               "buffer_load_dwordx4 %2, %3, %5 offen offset:1024 lds\n\ts_mov_b32 m0, %0"
               : "=&s"(keep) : "s"(lds), "v"(voff), "s"(r), "s"(soff0), "s"(soff1 - 1024u) : "memory");
}
template <int N> __device__ __forceinline__ void wait_vm() { asm volatile("s_waitcnt vmcnt(%0)" :: "n"(N) : "memory"); }
#define PIN_AFTER(W, ...) do { asm volatile(W ::: "memory"); __builtin_amdgcn_sched_barrier(0); asm volatile("" : __VA_ARGS__ :: "memory"); } while (0)
#define V4(c, g_) (f32x4){c[4 * (g_)], c[4 * (g_) + 1], c[4 * (g_) + 2], c[4 * (g_) + 3]}

constexpr int WR = 65536, QSLOT = 24576;
constexpr int GSLOT = 20480;
constexpr int REC = 2080, CHUNK = 4 * REC;
constexpr int KVA = WR + 3 * GSLOT, KVB = KVA - 4 * CHUNK;
#define NG 11

struct F4 { bf16x8 x0, x1, w0, w1; };
struct QFrag { bf16x8 x, q, k, v; };

template <int G>
__device__ __forceinline__ void qkv_steps(const lptr (&pa)[4], lptr pw, lptr pw2, QFrag& c, QFrag& n, f32x16& aq, f32x16& ak, f32x16& av,
                                          char* fx, u64 tag, int lane ST_ARG) {
  if constexpr (G < 32) {
    constexpr int s = G >> 2, N = G + 1, ns = N >> 2;
    if constexpr ((G & 3) == 3) {
      if constexpr (s == 0) wait_bar<9>(); else if constexpr (s == 1) wait_bar<4 - T_EARLY>(); else if constexpr (s == 2) wait_bar<1 - T_EARLY>(); else BAR();
      if constexpr (s == 1) { if (lane == 0) set_flag(fx, tag); }
    }
    const lptr w = ((ns & 2) ? pw2 : pw) + (ns & 1) * QSLOT + (N & 3) * 6144;
    MM(aq, c.q, c.x);
    if constexpr (N < 32) { n.x = lds_frag(pa[N & 3] + ns * 4096); n.q = lds_frag_tr(w, w + 256); }
    MM(ak, c.k, c.x);
    if constexpr (N < 32) { n.k = lds_frag_tr(w + 1024, w + 1024 + 256); n.v = lds_frag_tr(w + 2048, w + 2048 + 256); }
    MM(av, c.x, c.v);
    qkv_steps<G + 1>(pa, pw, pw2, n, c, aq, ak, av, fx, tag, lane ST_PASS);
  }
}

#define EXP(x) "v_exp_f32 v" #x ", v" #x "\n\t"
#define CVT(d, a, b) "v_cvt_pk_bf16_f32 v" #d ", v" #a ", v" #b "\n\t"
#define MF(d, a, b, c) "v_mfma_f32_32x32x16_bf16 " d ", " a ", " b ", " c "\n\t"
#define HALF(c0, c1, c2, c3, c4, c5, c6, c7, c8, c9, c10, c11, c12, c13, c14, c15, QK, PV1, PV0, X0, X1) \
  QK EXP(c0) EXP(c1) EXP(c2) EXP(c3) PV1 X0 \
  EXP(c4) EXP(c5) CVT(80, c0, c1) CVT(81, c2, c3) EXP(c6) EXP(c7) EXP(c8) EXP(c9) CVT(82, c4, c5) CVT(83, c6, c7) \
  EXP(c10) EXP(c11) PV0 X1 \
  EXP(c12) EXP(c13) CVT(84, c8, c9) CVT(85, c10, c11) EXP(c14) EXP(c15) CVT(86, c12, c13) CVT(87, c14, c15)
#define TILE(off, S, X0, X1, X2, X3) \
  S \
  HALF(16, 17, 18, 19, 20, 21, 22, 23, 24, 25, 26, 27, 28, 29, 30, 31, \
       MF("v[32:47]", "v[68:71]", "v[92:95]", "v[96:111]") "ds_read_b128 v[68:71], v88 offset:" off "\n\t", \
       MF("v[112:127]", "v[76:79]", "v[84:87]", "v[112:127]") "ds_read_b128 v[76:79], v90 offset:" off "\n\t", \
       "s_waitcnt lgkmcnt(2)\n\t" MF("v[48:63]", "v[72:75]", "v[80:83]", "v[48:63]"), X0, X1) \
  HALF(32, 33, 34, 35, 36, 37, 38, 39, 40, 41, 42, 43, 44, 45, 46, 47, \
       "s_waitcnt lgkmcnt(0)\n\t" MF("v[16:31]", "v[68:71]", "v[64:67]", "v[0:15]"), \
       MF("v[48:63]", "v[76:79]", "v[84:87]", "v[48:63]"), \
       MF("v[112:127]", "v[72:75]", "v[80:83]", "v[112:127]") "ds_read_b128 v[72:75], v89 offset:" off "\n\t", X2, X3)
#define T_NONE(off) TILE(off, "", "", "", "", "")
#define T_KV(off, m, so) TILE(off, "s_mov_b32 m0, %[" #m "]\n\t", "", "buffer_load_dwordx4 %[voff], %[rkv], %[" #so "] offen sc1 lds\n\t", \
    "buffer_load_dwordx4 %[voff], %[rkv], %[" #so "] offen offset:1024 sc1 lds\n\t", "")
#define T_WD(off, st) TILE(off, "s_add_u32 m0, %[dm0], 16384*" #st "\n\ts_add_u32 %[t0], %[ds0], 131072*" #st "\n\t", "", \
    "buffer_load_dwordx4 %[vw1], %[rwd], %[t0] offen lds\n\ts_add_u32 %[t0], %[t0], 7168\n\t", "buffer_load_dwordx4 %[vw1], %[rwd], %[t0] offen offset:1024 lds\n\t", "")
#define T_KVI(off, MO, SO) TILE(off, "s_add_u32 m0, %[kvm], " #MO "\n\ts_add_u32 %[t0], %[kvs], " #SO "\n\t", "", \
    "buffer_load_dwordx4 %[voff], %[rkv], %[t0] offen sc1 lds\n\t", "buffer_load_dwordx4 %[voff], %[rkv], %[t0] offen offset:1024 sc1 lds\n\t", "")
#define VB(n) "s_waitcnt vmcnt(" #n ")\n\ts_barrier\n\t"
#define SB_ "s_barrier\n\t"
#define AT(i)
#define AT_OP
#define AT_CL
#define HLA(d) "buffer_load_dwordx4 %[" #d "], %[voff], %[rh], %[hs] offen sc1\n\t"
#define HLB(d) "buffer_load_dwordx4 %[" #d "], %[voff], %[rh], %[hs] offen offset:1024 sc1\n\ts_add_u32 %[hs], %[hs], 4096\n\t"
#define T_H2(off, da, db, dc, dd) TILE(off, "", HLA(da), HLB(db), HLA(dc), HLB(dd))
#define ATT_BEGIN "s_mov_b32 %[keep], m0\n\ts_waitcnt lgkmcnt(0)\n\ts_nop 2\n\t"
#define ATT_END "s_mov_b32 m0, %[keep]\n\ts_waitcnt lgkmcnt(0)\n\ts_nop 3\n\t"
#define ATT_STATE "+{v[48:63]}"(OA), "+{v[112:127]}"(OB), "+{v[16:31]}"(SA), "+{v[68:71]}"(K), "+{v[72:75]}"(V0), "+{v[76:79]}"(V1), \
                  "+{v[84:87]}"(P1), [t0] "=&s"(t0), [keep] "=&s"(keep) AT_OP
#define ATT_CONST "{v[0:15]}"(CA), "{v[96:111]}"(CB), "{v[64:67]}"(QA), "{v[92:95]}"(QB), [voff] "v"(voff)
#define ATT_IN ATT_CONST, "{v88}"(aK), "{v89}"(aV0), "{v90}"(aV1)
#define ATT_CLOB "v32", "v33", "v34", "v35", "v36", "v37", "v38", "v39", "v40", "v41", "v42", "v43", "v44", "v45", "v46", "v47", \
                 "v80", "v81", "v82", "v83", "scc", "memory" AT_CL

struct GFrag { bf16x8 x[4], g, u; };
__host__ __device__ constexpr int gslot(int s) { return s < 8 ? (s & 3) : (s - 8) % 3; }
template <int S, int KG>
__device__ __forceinline__ void gu_read(const lptr (&pa)[4], const lptr (&pp)[4], lptr pg, lptr pg3, GFrag& n, int part) {
  const lptr x = pa[(S & 1) * 2 + KG] + (S >> 1) * 4096, y = pp[(S & 1) * 2 + KG] + gslot(S) * GSLOT, w = (gslot(S) == 3 ? pg3 : pg + gslot(S) * GSLOT) + KG * 4096;
  if (part == 0) { n.x[0] = lds_frag(x); n.g = lds_frag_tr(w, w + 256); }
  if (part == 1) { n.x[1] = lds_frag(x + 32768); n.u = lds_frag_tr(w + 8192, w + 8192 + 256); }
  if (part == 2) { n.x[2] = lds_frag(y); n.x[3] = lds_frag(y + 2048); }
}
template <int S, class FILL>
__device__ __forceinline__ void gu_stages(const lptr (&pa)[4], const lptr (&pp)[4], lptr pg, lptr pg3, GFrag& f0, GFrag& f1, f32x16 (&g)[4], f32x16 (&u)[4], FILL fill ST_ARG) {
  if constexpr (S < 16) {
#define GU8(c, R0, R1, R2, D) do { MM(g[0], c.g, c.x[0]); R0; MM(g[1], c.g, c.x[1]); R1; MM(u[0], c.u, c.x[0]); R2; MM(u[1], c.u, c.x[1]); \
      MM(g[2], c.g, c.x[2]); D; MM(u[2], c.u, c.x[2]); MM(g[3], c.g, c.x[3]); MM(u[3], c.u, c.x[3]); } while (0)
    GU8(f0, (gu_read<S, 1>(pa, pp, pg, pg3, f1, 0)), (gu_read<S, 1>(pa, pp, pg, pg3, f1, 1)), (gu_read<S, 1>(pa, pp, pg, pg3, f1, 2)), );
    if constexpr (S < 15) {
      if constexpr (S == 0) wait_bar<6>(); else if constexpr (S == 2 || S == 14) wait_bar<0>(); else wait_bar<5>();
      TR(17 + S);
      GU8(f1, (gu_read<S + 1, 0>(pa, pp, pg, pg3, f0, 0)), (gu_read<S + 1, 0>(pa, pp, pg, pg3, f0, 1)), (gu_read<S + 1, 0>(pa, pp, pg, pg3, f0, 2)),
          if (S >= 8 && S <= 12) fill(S + 3));
    } else {
      GU8(f1, , , , );
    }
    gu_stages<S + 1>(pa, pp, pg, pg3, f0, f1, g, u, fill ST_PASS);
  }
}

__global__ __launch_bounds__(512, 2) void k_all(const char* __restrict__ gx, const char* __restrict__ gwqkv, const char* __restrict__ gwo,
                                                const char* __restrict__ gwg, const char* __restrict__ gwu, const char* __restrict__ gwd,
                                                char* __restrict__ ws, char* __restrict__ out) {
  extern __shared__ __attribute__((aligned(16))) char smem[];
  const lptr lds = (lptr)smem;
  const unsigned lds0 = (unsigned)(uintptr_t)lds;
  const int lane = threadIdx.x & 63, wid = RFL(threadIdx.x >> 6), b = blockIdx.x;
  const int xcd = b & 7, bi = b >> 3;
  const int rb = (xcd >> 1) * 8 + (bi & 7);
  const int cb = (xcd & 1) * 4 + (bi >> 3);
  const int cq = (cb + CSH - (int)((uintptr_t)gwqkv >> 7)) & 7, co = (cb + CSH - (int)((uintptr_t)gwd >> 7)) & 7;
  const int xg = XS_ONE ? 0 : (cb & 4);
  const int rot = (rb + 2 * cb) & 7;
  const int head = xcd * 4 + (bi >> 3);
  const int r = lane & 31, h = lane >> 5;
  const unsigned trn = ((lane & 15) >> 2) * 64 + ((lane >> 4) & 1) * 32 + (lane & 3) * 8;
  const unsigned voff = lane * 16;
  ST_DECL;
  unsigned cpf;
  {
    u64 pc; unsigned len;
    asm volatile("s_getpc_b64 %0\nLk_here:\n\ts_mov_b32 %1, Lk_tail-Lk_here" : "=s"(pc), "=s"(len));
    const unsigned o = (unsigned)((bi * 8 + wid) * CPF_LANES + (lane < CPF_LANES ? lane : 0)) * 128;
    asm volatile("global_load_dword %0, %1, off" : "=v"(cpf) : "v"((const char*)pc + (o + 4 <= len ? o : 0)) : "memory");
  }
  __builtin_amdgcn_sched_barrier(0);
  ST(0);

  const rsrc_t rx = mk_rsrc(gx, S_ * D_ * 2), rw = mk_rsrc(gwqkv, D_ * 3 * D_ * 2), rxn = mk_rsrc(ws + WS_XN + (xg >> 2) * 2097152, 2097152);
  u32x4 xr[8];
  {
    const unsigned vx = (lane >> 3) * 1024 + (lane & 7) * 16, sx = (unsigned)(rb * 64 + wid * 8) * 1024;
#pragma unroll
    for (int i = 0; i < 8; ++i) BLOAD(xr[i], vx, rx, sx + ((i + rot) & 7) * 128);
  }
  BAR();
  const unsigned vw3 = ((lane >> 2) & 7) * 3072 + (lane >> 5) * 64 + (lane & 3) * 16;
  auto issue_wk = [&](int s, int kb) {
    const unsigned so = (unsigned)(kb * 8) * 3072 + ((s + rot) & 7) * (64 * 3072);
    dma16x3(rw, vw3, so + cq * 128, lds0 + WR + (s & 3) * QSLOT + kb * 3072);
  };
  auto issue_w = [&](int s) { issue_wk(s, wid); };
  issue_w(0); issue_w(1);
  unsigned pf;
#define TOUCH() asm volatile("global_load_dword %0, %1, off" : "=v"(pf) : "v"((h ? gwu : gwg) + (size_t)((b * 8 + wid) * 7 + (r < 7 ? r : 0)) * 128) : "memory")
  const u64 tag = read_tag(ws);
  float rs, mr;
  {
    const unsigned ONE2 = 0x3f803f80u;
    float s0 = 0.f, s1 = 0.f, q0 = 0.f, q1 = 0.f;
#define STAT(i) do { wait_vm1<13 + T_EARLY - (i)>(xr[i]); \
      s0 = dot2(xr[i][0], ONE2, s0); q0 = dot2(xr[i][0], xr[i][0], q0); s1 = dot2(xr[i][1], ONE2, s1); q1 = dot2(xr[i][1], xr[i][1], q1); \
      s0 = dot2(xr[i][2], ONE2, s0); q0 = dot2(xr[i][2], xr[i][2], q0); s1 = dot2(xr[i][3], ONE2, s1); q1 = dot2(xr[i][3], xr[i][3], q1); \
      asm volatile("" : "+v"(s0), "+v"(s1), "+v"(q0), "+v"(q1)); } while (0)
    STAT(0); STAT(1); STAT(2); STAT(3); STAT(4); STAT(5); STAT(6); STAT(7);
    ST(1);
    const float mean = sum8(s0 + s1) * (1.0f / 512.0f);
    rs = __builtin_amdgcn_rsqf(__builtin_fmaxf(sum8(q0 + q1) * (1.0f / 512.0f) - mean * mean, 0.0f) + 1e-5f);
    mr = -mean * rs;
  }
  const unsigned xo = (lane & 7) * 512 + ((((wid & 3) * 8 + (lane >> 3)) ^ (lane & 7)) * 16);
  const lptr pxw = lds + (wid >> 2) * 32768 + xo;
#pragma unroll
  for (int j = 0; j < 8; ++j) {
    u32x4 n;
#pragma unroll
    for (int i = 0; i < 4; ++i) n[i] = pk2(__builtin_fmaf(bf_lo(xr[j][i]), rs, mr), __builtin_fmaf(bf_hi(xr[j][i]), rs, mr));
    if (j < 2) BSTORE_DEV(n, rxn, ((cb & 4) == xg ? 0u : 0x7ff00000u) + (unsigned)(rb * 8 + ((rot + j) & 7)) * 8192 + (wid >> 2) * 4096 + xo);
    *(LAS u32x4*)(pxw + j * 4096) = n;
    if (j == W2_AT) issue_w(2);
    if (j == W3_AT) issue_w(3);
  }
  TOUCH();
  const bool has_ffn = cb < 7;
  const int fb = cb * 2 + (rb & 1), w4 = wid & 3;
  const rsrc_t rg = mk_rsrc(gwg - 1024, D_ * F_ * 2 + 1024), ru = mk_rsrc(gwu - 1024, D_ * F_ * 2 + 1024);
  const unsigned vwf = ((lane >> 2) & 7) * (F_ * 2) + (lane >> 5) * 64 + (lane & 3) * 16;
  auto gu_w = [&](int s) {
    const unsigned so = (has_ffn ? 0u : 0x7ff00000u) + (unsigned)(w4 * 8) * (F_ * 2) + fb * 256 + (unsigned)((s + 2 * rot) & 15) * (32 * F_ * 2) + 1024;
    const unsigned d = lds0 + WR + gslot(s) * GSLOT + w4 * 2048;
    dma16x2(rg, vwf, so, so + 128, d); dma16x2(ru, vwf, so, so + 128, d + 8192);
  };
  auto gu_x = [&](int s) {
    dma16_dev(rxn, voff, (has_ffn ? 0u : 0x7ff00000u) + (unsigned)((rb ^ 1) * 8 + (((s >> 1) + rot) & 7)) * 8192 + (w4 >> 1) * 4096 + (s & 1) * 2048 + (w4 & 1) * 1024,
              lds0 + WR + gslot(s) * GSLOT + 16384 + w4 * 1024);
  };
  auto gu_fill = [&](int s) { gu_w(s); gu_x(s); };
  u64 fl;
  lptr pa[4];
#pragma unroll
  for (int g = 0; g < 4; ++g) pa[g] = lds + (2 * g + h) * 512 + ((r ^ (2 * g + h)) * 16);

  const rsrc_t rkv = mk_rsrc(ws + WS_KV + (size_t)head * 131072, 131072), rh = mk_rsrc(ws + WS_H, 7340032);
  const unsigned vvo = h * 256 + (r & 15) * 16;
  const bf16x8 ONES = {0x3f80, 0x3f80, 0x3f80, 0x3f80, 0x3f80, 0x3f80, 0x3f80, 0x3f80};
  const bf16x8 PAD = ((r & 27) == 16) ? ONES : (bf16x8){};
  const int qt0 = (bi & 7) * 8 + w4 * 2;
  const unsigned ring = lds0 + (wid < 4 ? KVA : KVB), kt0 = wid < 4 ? 0 : 32;
  const unsigned kvm = ring + w4 * REC, kvs = (kt0 + w4) * 2048;
  u32x4 QAu, QBu, Ku, V0u;
  auto att_loads = [&] {
    const rsrc_t rq = mk_rsrc(ws + WS_Q, 2097152);
    BLOAD_DEV(QAu, voff, rq, (unsigned)(head * 64 + qt0) * 1024);
    BLOAD_DEV(QBu, voff, rq, (unsigned)(head * 64 + qt0 + 1) * 1024);
    BLOAD_DEV(Ku, voff, rkv, ((kt0 + 63) & 63) * 2048);
    BLOAD_DEV(V0u, vvo, rkv, ((kt0 + 63) & 63) * 2048 + 1024);
  };
  auto att_ring = [&] {
    dma16_dev(rkv, voff, kvs, kvm); dma16_dev(rkv, voff, kvs + 1024, kvm + 1024);
    dma16_dev(rkv, voff, kvs + 8192, kvm + CHUNK); dma16_dev(rkv, voff, kvs + 8192 + 1024, kvm + CHUNK + 1024);
    *(LAS bf16x8*)(lds + (ring - lds0) + (w4 * 4 + (r & 3)) * REC + 2048 + h * 16) = h ? (bf16x8){} : ONES;
  };
  LAS float* const mx = (LAS float*)(lds + KVA + 4 * CHUNK + w4 * 512) + lane;
  bf16x8 QA, QB, K, V0, V1, P1;
  f32x16 SA, CA, CB, OA, OB;
#define MV2(d, s) "v_mov_b64 v[" #d ":" #d "+1], " s "\n\t"
#define MV16(d, s) MV2(d, s) MV2(d + 2, s) MV2(d + 4, s) MV2(d + 6, s) MV2(d + 8, s) MV2(d + 10, s) MV2(d + 12, s) MV2(d + 14, s)
  auto att_init = [&](float ma, float mb) {
    QA = __builtin_bit_cast(bf16x8, QAu); QB = __builtin_bit_cast(bf16x8, QBu); K = __builtin_bit_cast(bf16x8, Ku);
    V0 = (r >= 16) ? PAD : __builtin_bit_cast(bf16x8, V0u);
    asm volatile(MV16(0, "%[ma]") MV2(76, "0") MV2(78, "0") MF("v[16:31]", "v[68:71]", "v[64:67]", "v[0:15]")
                 MV16(96, "%[mb]") MV16(48, "0") MV16(112, "0") MV2(84, "0") MV2(86, "0")
                 : "=&{v[0:15]}"(CA), "=&{v[96:111]}"(CB), "=&{v[16:31]}"(SA), "=&{v[48:63]}"(OA), "=&{v[112:127]}"(OB), "=&{v[76:79]}"(V1), "=&{v[84:87]}"(P1)
                 : "{v[68:71]}"(K), "{v[64:67]}"(QA), [ma] "v"((f32x2){ma, ma}), [mb] "v"((f32x2){mb, mb}));
  };
  const unsigned apad = ((r & 27) == 16) ? 2048 : 2064;
  const unsigned aK = ring + voff, aV0 = ring + (r < 16 ? 1024 + vvo : apad), aV1 = ring + (r < 16 ? 1536 + vvo : apad);
  unsigned t0, keep;
  const int rt = wid & 1, kq = wid >> 1;
  const rsrc_t rwd = mk_rsrc(gwd, F_ * D_ * 2), rwo = mk_rsrc(gwo, D_ * D_ * 2), ro = mk_rsrc(ws + WS_O, 2097152);
  const unsigned vw1 = ((lane >> 2) & 7) * 1024 + (lane >> 5) * 64 + (lane & 3) * 16;
#define DADDR(st) ((st) < 5 ? (st) * 16384 : (st) == 5 || (st) == 11 ? KVA : (st) < 11 ? ((st) - 6) * 16384 : (st) < 14 ? ((st) - 12) * 16384 : \
                   (st) == 14 ? 81920 : (st) == 15 ? 147456 : (st) == 16 ? 98304 : 32768)
  auto d_so = [&](int st, int pc) { return (unsigned)((st < 14 ? st : st - 14) * 128 + pc * 16) * 1024 + co * 128; };
  auto d_m = [&](int st, int pc) { return lds0 + DADDR(st) + pc * 2048; };
  auto issue_dp = [&](int st, int pc) { dma16x2(st < 14 ? rwd : rwo, vw1, d_so(st, pc), d_so(st, pc) + 8192, d_m(st, pc)); };
  auto h_so = [&](int e) { return (unsigned)((rb * 2 + rt) * 112 + e * 4 + kq * 2) * 1024; };
  const unsigned kvd = kvs + (wid < 4 ? 4 * 8192 : 0);
  u32x4 ha[14], hb[14], xv;
  const unsigned kve = wid < 4 ? 0x7ff00000u : kvd;
  LAS f32x4* ex = (LAS f32x4*)(lds + 147456 + w4 * 4096) + lane;
  LAS float* el = (LAS float*)(lds + 145408 + w4 * 512) + lane;
  WAIT_BAR(12);
  ST(2);

  if (wid < 4) {
    {
      const int ch = wid >> 1;
      lptr par[4];
#pragma unroll
      for (int g = 0; g < 4; ++g) par[g] = pa[g] + rt * 32768;
      lptr pw = lds + WR + h * 3072 + ch * 512 + trn, pw2 = pw + 2 * QSLOT;
      asm volatile("" : "+v"(pw2));
      const int pair = cq * 2 + ch, tile = rb * 2 + rt;
      f32x16 aq = {}, ak = {}, av = {};
      QFrag f0, f1;
      f0.x = lds_frag(par[0]); f0.q = lds_frag_tr(pw, pw + 256); f0.k = lds_frag_tr(pw + 1024, pw + 1024 + 256); f0.v = lds_frag_tr(pw + 2048, pw + 2048 + 256);
      qkv_steps<0>(par, pw, pw2, f0, f1, aq, ak, av, ws + WS_FX + (rb * 8 + cb) * 8, tag, threadIdx.x ST_PASS);
      ST(3);
#pragma unroll
      for (int i = 0; i < 16; ++i) aq[i] *= QSCALE;
      u32x4 qs[6] = {pack8(ak, 0), pack8(ak, 8), pack8(av, 0), pack8(av, 8), pack8(aq, 0), pack8(aq, 8)};
      asm volatile("" : "+v"(qs[0]), "+v"(qs[1]), "+v"(qs[2]), "+v"(qs[3]), "+v"(qs[4]), "+v"(qs[5]));
      const rsrc_t rq = mk_rsrc(ws + WS_Q, 2097152 + 4194304);
      const unsigned ko = 2097152 + (unsigned)((2 * pair) * 64 + ((tile + 63) & 63)) * 2048 + voff;
      const unsigned vo = 2097152 + (unsigned)(2 * pair + (r >> 4)) * 131072 + vvo;
      const unsigned qo = (unsigned)((2 * pair) * 64 + tile) * 1024 + voff;
      auto qst = [&](int i) {
        BSTORE_DEV(qs[i], rq, i == 0 ? ko : i == 1 ? ko + 131072 : i == 2 ? vo + ((tile + 63) & 63) * 2048 + 1024 : i == 3 ? vo + tile * 2048 + 1536 : i == 4 ? qo : qo + 65536);
      };
      auto fq_set = [&] {
        __builtin_amdgcn_raw_buffer_store_b64(__builtin_bit_cast(u32x2, tag), mk_rsrc(ws + WS_FQ, 8192),
                                              lane == 0 ? (unsigned)((cq * 2 + (wid >> 1)) * 64 + rb * 2 + (wid & 1)) * 8 : 0x7ff00000u, 0, 16);
      };
      if (__builtin_expect(has_ffn, 1)) {
        BAR();
        qst(0); qst(1); qst(2);
        BAR(); qst(3); qst(4); qst(5); gu_fill(4);
        BAR(); gu_fill(5);
        BAR(); gu_fill(6);
        WAIT_BAR(10); fq_set(); gu_fill(7);
        WAIT_BAR(10); gu_fill(8);
        WAIT_BAR(10); gu_fill(9);
        WAIT_BAR(10); gu_fill(10);
      } else {
        qst(0); qst(1); qst(2); qst(3); qst(4); qst(5);
        wait_vm<0>();
        fq_set();
#pragma unroll 1
        for (int i = 0; i < 8; ++i) BAR();
      }
    }
    if (NG >= 11) WAIT_BAR(10);
    if (NG >= 12) WAIT_BAR(5);
    if (!has_ffn) __builtin_amdgcn_s_sleep(IDLE_SLEEP);
    asm volatile("global_load_dwordx2 %0, %1, off sc1" : "=v"(fl) : "v"(ws + WS_FQ + ((head >> 1) * 64 + lane) * 8) : "memory");
    wait_bar<11 - 5 * (NG - 10)>();
    wait_vm<0>(); __builtin_amdgcn_sched_barrier(0); asm volatile("" : "+v"(fl) :: "memory");
    if (__builtin_expect(!__all(fl == tag), 0)) wait_flags(ws + WS_FQ + (head >> 1) * 512, 64, tag, lane);
    ST(4);
    u32x4 K1u;
    att_loads();
    BLOAD_DEV(K1u, voff, rkv, 0u);
    BAR();
    att_ring();
    PIN_AFTER("s_waitcnt vmcnt(4)", "+v"(QAu), "+v"(QBu), "+v"(Ku), "+v"(V0u), "+v"(K1u));
    float ma, mb;
    {
      const f32x16 z = {};
      const bf16x8 k0 = __builtin_bit_cast(bf16x8, Ku), k1 = __builtin_bit_cast(bf16x8, K1u), qa = __builtin_bit_cast(bf16x8, QAu), qb = __builtin_bit_cast(bf16x8, QBu);
      const f32x16 a0 = MFMA(k0, qa, z), a1 = MFMA(k1, qa, z), b0 = MFMA(k0, qb, z), b1 = MFMA(k1, qb, z);
      ma = a0[0]; mb = b0[0];
#pragma unroll
      for (int i = 0; i < 16; ++i) { ma = __builtin_fmaxf(__builtin_fmaxf(ma, a0[i]), a1[i]); mb = __builtin_fmaxf(__builtin_fmaxf(mb, b0[i]), b1[i]); }
      asm volatile("" : "+v"(ma), "+v"(mb));
    }
    BAR();
    ma = -(xhalf_max(ma) + MBIAS); mb = -(xhalf_max(mb) + MBIAS);
    mx[0] = ma; mx[64] = mb;
    att_init(ma, mb);
    ST(5);
    asm volatile(ATT_BEGIN "s_setprio 2\n\t"
                 VB(2) T_KVI("0", 16640, 16384) T_NONE("2080") SB_ T_NONE("4160") T_NONE("6240")
                 VB(2) T_KVI("8320", 24960, 24576) T_NONE("10400") SB_ T_NONE("12480") T_NONE("14560")
                 VB(2) T_KVI("16640", 0, 32768) T_NONE("18720") SB_ T_NONE("20800") T_NONE("22880")
                 VB(2) T_KVI("24960", 8320, 40960) T_NONE("27040") SB_ T_NONE("29120") T_NONE("31200")
                 ATT_END
                 : ATT_STATE : ATT_IN, [rkv] "s"(rkv), [kvm] "s"(kvm), [kvs] "s"(kvs) : ATT_CLOB);
    __builtin_amdgcn_s_setprio(0);
    wait_vm<2>();
  } else {
    auto w2 = [&](int s) { issue_wk(s, w4 * 2); issue_wk(s, w4 * 2 + 1); };
    WAIT_BAR(9);  w2(4);
    WAIT_BAR(10); w2(5);
    WAIT_BAR(13); w2(6);
    WAIT_BAR(12); w2(7);
    WAIT_BAR(12);
    asm volatile("global_load_dwordx2 %0, %1, off sc1" : "=v"(fl) : "v"(ws + WS_FX + ((rb ^ 1) * 8 + xg + (lane & 3)) * 8) : "memory");
    gu_w(0);
    WAIT_BAR(11); gu_w(1);
    do { asm volatile("s_waitcnt vmcnt(8) lgkmcnt(0)\n\ts_barrier" ::: "memory"); __builtin_amdgcn_sched_barrier(0); asm volatile("" : "+v"(fl) :: "memory"); } while (0);
    const bool xok = __all(fl == tag) || !has_ffn;
    if (xok) gu_x(0);
    gu_w(2);
    if (xok) { gu_x(1); gu_x(2); }
    BAR();
    if (__builtin_expect(xok, 1)) { gu_fill(3); wait_vm<11>(); }
    else { wait_flags(ws + WS_FX + ((rb ^ 1) * 8 + xg) * 8, 4, tag, lane); gu_x(0); gu_x(1); gu_x(2); gu_fill(3); wait_vm<7>(); }
    ST(3);
    BAR();
    f32x16 g[4], u[4];
    if (has_ffn) {
      lptr pp[4];
#pragma unroll
      for (int i = 0; i < 4; ++i) { pp[i] = pa[i] + (WR + 16384 - (i >> 1) * 2048); asm volatile("" : "+v"(pp[i])); g[i] = (f32x16){}; u[i] = (f32x16){}; }
      const lptr pg = lds + WR + h * 2048 + (w4 >> 1) * 1024 + (w4 & 1) * 512 + trn;
      lptr pg3 = pg + 3 * GSLOT;
      asm volatile("" : "+v"(pg3));
      GFrag f0, f1;
      gu_read<0, 0>(pa, pp, pg, pg3, f0, 0); gu_read<0, 0>(pa, pp, pg, pg3, f0, 1); gu_read<0, 0>(pa, pp, pg, pg3, f0, 2);
      TR(16);
      gu_stages<0>(pa, pp, pg, pg3, f0, f1, g, u, gu_fill ST_PASS);
      TR(32);
    } else {
      unsigned d[5];
#pragma unroll 1
      for (int i = 0; i < NG + 1; ++i) BAR();
#pragma unroll
      for (int i = 0; i < 5; ++i) {
        const int t = (rb * 4 + w4) * 144 + i * 32 + (r + i * 32 < 144 ? r : 0);
        const char* p = (t < 14336) ? gwd + (size_t)t * 128 : gwo + (size_t)(t - 14336) * 128;
        asm volatile("global_load_dword %0, %1, off" : "=v"(d[i]) : "v"(p) : "memory");
      }
#pragma unroll 1
      for (int i = NG + 1; i < 15; ++i) BAR();
      do { asm volatile("s_waitcnt vmcnt(0)" ::: "memory"); __builtin_amdgcn_sched_barrier(0); asm volatile("" : "+v"(d[0]), "+v"(d[1]), "+v"(d[2]), "+v"(d[3]), "+v"(d[4]) :: "memory"); } while (0);
    }
    ST(4);
    BAR();
    __builtin_amdgcn_s_setprio(3);
    TR(33);
    att_loads(); att_ring();
    TR(34);
    auto swiglu = [&](int i) {
#pragma unroll
      for (int j = 0; j < 16; ++j) g[i][j] = g[i][j] * u[i][j] * __builtin_amdgcn_rcpf(1.0f + __builtin_amdgcn_exp2f(g[i][j] * -1.4426950408889634f));
      const unsigned ho = (unsigned)(((rb ^ (i >> 1)) * 2 + (i & 1)) * 112 + fb * 8 + w4 * 2) * 1024 + voff;
      BSTORE_DEV(pack8(g[i], 0), rh, ho);
      BSTORE_DEV(pack8(g[i], 8), rh, ho + 1024);
    };
    static_assert(NG >= 10 && NG <= 12, "barrier count below");
    if (NG >= 12) BAR();
    if (has_ffn) swiglu(0);
    TR(35);
    BAR();
    if (has_ffn) swiglu(1);
    if (NG >= 11) BAR();
    if (has_ffn) swiglu(2);
    BAR();
    TR(36);
    const float ma = mx[0], mb = mx[64];
    if (has_ffn) swiglu(3); else wait_vm<0>();
    TR(37);
    PIN_AFTER("s_waitcnt vmcnt(8)", "+v"(QAu), "+v"(QBu), "+v"(Ku), "+v"(V0u));
    att_init(ma, mb);
    TR(38);
    ST(5);
  }
  asm volatile("" :: "v"(pf), "v"(cpf));
  TR(39);
  ST(7); STC(14);
  asm volatile(ATT_BEGIN "s_setprio 0\n\t" SB_ AT(40)
               T_KVI("0", 16640, 16384) T_WD("2080", 0) T_WD("4160", 1) T_WD("6240", 2)
               AT(41) VB(8) AT(42)
               "buffer_store_dwordx2 %[tag], %[fho], %[rfl], 0 offen sc1\n\t"
               T_KVI("8320", 24960, 24576) T_WD("10400", 3) T_WD("12480", 4) T_NONE("14560")
               AT(43) VB(7) AT(44)
               T_KV("16640", kvm, s5_) T_NONE("18720")
               TILE("20800", "", "buffer_load_dwordx2 %[fl], %[fhi], %[rfl], 0 offen sc1\n\t", "", "", "") T_NONE("22880") AT(45)
               ATT_END
               : ATT_STATE, [fl] "=&v"(fl)
               : ATT_IN, [rkv] "s"(rkv), [rwd] "s"(rwd), [vw1] "v"(vw1), [kvm] "s"(kvm), [kvs] "s"(kvd),
                 [dm0] "s"(RFL(d_m(0, wid))), [ds0] "s"(RFL(d_so(0, wid))), [s5_] "s"(kve + 4 * 8192), [rfl] "s"(mk_rsrc(ws + WS_FH, 2048)), [tag] "v"(tag),
                 [fho] "v"(threadIdx.x == 0 ? (unsigned)(rb * 8 + cb) * 8 : 0x7ff00000u), [fhi] "v"((unsigned)((rb >> 1) * 16 + (lane & 15)) * 8) : ATT_CLOB);
  PIN_AFTER("s_waitcnt vmcnt(0)", "+v"(fl));
  if (__builtin_expect(!__all(fl == tag), 0)) wait_flags(ws + WS_FH + (rb >> 1) * 128, 16, tag, lane);
#define H_OUT(n, st) [a##n] "=&v"(ha[st]), [b##n] "=&v"(hb[st])
  unsigned hs = h_so(0) + (wid >> 2) * 0x7ff00000u;
  f32x4 p0, p1, p2, p3;
  float pl0, pl1;
  asm volatile(ATT_BEGIN SB_ AT(46)
               TILE("24960", "s_add_u32 m0, %[kvm], 8320\n\ts_add_u32 %[t0], %[kvs], 40960\n\t", "buffer_load_dwordx4 %[voff], %[rkv], %[t0] offen sc1 lds\n\t",
                    "buffer_load_dwordx4 %[voff], %[rkv], %[t0] offen offset:1024 sc1 lds\n\t", HLA(a0), HLB(b0))
               T_H2("27040", a1, b1, a2, b2) T_H2("29120", a3, b3, a4, b4) T_H2("31200", a5, b5, a6, b6)
               AT(47) "s_cmp_lt_u32 %[wid], 4\n\ts_cbranch_scc1 L_skip_%=\n\t"
               "s_setprio 2\n\t"
               SB_ AT(48) T_KVI("0", 16640, 49152) T_NONE("2080") SB_ AT(49) T_NONE("4160") T_NONE("6240")
               VB(2) AT(50) T_KVI("8320", 24960, 57344)
               TILE("10400", "", "", "", "ds_read_b128 %[p0], %[ex]\n\tds_read_b128 %[p1], %[ex] offset:1024\n\tds_read_b32 %[pl0], %[el]\n\t", "")
               SB_ AT(51)
               TILE("12480", "", "", "", "ds_read_b128 %[p2], %[ex] offset:2048\n\tds_read_b128 %[p3], %[ex] offset:3072\n\tds_read_b32 %[pl1], %[el] offset:256\n\t", "")
               T_NONE("14560")
               VB(2) AT(52) T_NONE("16640") T_NONE("18720") SB_ AT(53) T_NONE("20800") T_NONE("22880")
               VB(0) AT(54) T_NONE("24960") T_NONE("27040") SB_ AT(55) T_NONE("29120") T_NONE("31200") AT(56)
               "L_skip_%=:\n\t"
               ATT_END
               : ATT_STATE, H_OUT(0, 0), H_OUT(1, 1), H_OUT(2, 2), H_OUT(3, 3), H_OUT(4, 4), H_OUT(5, 5), H_OUT(6, 6),
                 [p0] "=&v"(p0), [p1] "=&v"(p1), [p2] "=&v"(p2), [p3] "=&v"(p3), [pl0] "=&v"(pl0), [pl1] "=&v"(pl1), [hs] "+s"(hs)
               : ATT_IN, [rkv] "s"(rkv), [kvm] "s"(kvm), [kvs] "s"(kve), [wid] "s"(wid), [ex] "v"(ex), [el] "v"(el),
                 [rh] "s"(rh) : ATT_CLOB);
  ST(6);

  const int t = threadIdx.x;
  const unsigned off = (unsigned)(rb * 64 + (t >> 3)) * 1024 + co * 128 + (t & 7) * 16;
  f32x16 d0, d1;
  F4 e0, e1;
#define D_PTR(pd, a) ((a) < 65536 ? pd[0] + (a) : (a) < KVA ? pd[1] + ((a) - 65536) : pd[2] + ((a) - KVA))
#define D_READ_A(n, w_) do { n.w0 = lds_frag_tr(w_, w_ + 1024); n.w1 = lds_frag_tr(w_ + 512, w_ + 1536); } while (0)
#define D_READ_B(n, w_) do { n.x0 = lds_frag_tr(w_ + 2048, w_ + 3072); n.x1 = lds_frag_tr(w_ + 2560, w_ + 3584); } while (0)
#define D_STEPX(c, n, nxt, A, B, RD, X, Y) do { \
    MM(d0, c.w0, __builtin_bit_cast(bf16x8, A)); if (RD) D_READ_A(n, (nxt)); MM(d1, c.w1, __builtin_bit_cast(bf16x8, A)); if (RD) D_READ_B(n, (nxt)); \
    MM(d0, c.x0, __builtin_bit_cast(bf16x8, B)); X; MM(d1, c.x1, __builtin_bit_cast(bf16x8, B)); Y; } while (0)
#define H_LOAD(e, so) do { BLOAD_DEV(ha[e], voff, rh, so); BLOAD_DEV(hb[e], voff, rh, (so) + 1024); } while (0)
  PIN_AFTER("s_waitcnt vmcnt(0)", "+v"(ha[0]), "+v"(ha[1]), "+v"(ha[2]), "+v"(ha[3]), "+v"(ha[4]), "+v"(ha[5]), "+v"(ha[6]),
            "+v"(hb[0]), "+v"(hb[1]), "+v"(hb[2]), "+v"(hb[3]), "+v"(hb[4]), "+v"(hb[5]), "+v"(hb[6]));
  OB = MFMA(V1, P1, OB);
  lptr pe[3];
  pe[0] = lds + (kq & 1) * 4096 + h * 256 + trn; pe[1] = pe[0] + 65536; pe[2] = pe[0] + KVA;
  asm volatile("" : "+v"(pe[0]), "+v"(pe[1]), "+v"(pe[2]));
#define EP(e) D_PTR(pe, DADDR((e) >> 1) + ((e) & 1) * 8192)
#define RADDR(q) ((q) == 0 ? 49152 : (q) == 1 ? 65536 : (q) == 2 ? KVA : 0)
  auto put_partial = [&](int base) {
    const lptr red = lds + base + (rt * 32 + r) * 256;
#pragma unroll
    for (int i = 0; i < 4; ++i) {
      *(LAS f32x4*)(red + (((2 * i + h) ^ (r & 15)) * 16)) = V4(d0, i);
      *(LAS f32x4*)(red + (((8 + 2 * i + h) ^ (r & 15)) * 16)) = V4(d1, i);
    }
  };
  if (wid < 4) {
    d0 = d1 = (f32x16){};
#define E_DMA(st, i) issue_dp(st, wid * 2 + (i))
#define E_STAGE(k, X0, Y0, X1, Y1) do { D_STEPX(e0, e1, EP(2 * (k) + 1), ha[(2 * (k)) % 14], hb[(2 * (k)) % 14], true, X0, Y0); \
      D_STEPX(e1, e0, EP(2 * (k) + 2), ha[(2 * (k) + 1) % 14], hb[(2 * (k) + 1) % 14], (k) < 13, X1, Y1); } while (0)
#define HE(e) H_LOAD((e) % 14, h_so(e))
#define HP(e) "+v"(ha[(e) % 14]), "+v"(hb[(e) % 14])
#define W2(k, sa, sb, e) do { E_STAGE(k, E_DMA(sa, 0), E_DMA(sa, 1), E_DMA(sb, 0), E_DMA(sb, 1)); HE(e); } while (0)
#define W1(k, sa, e) do { E_STAGE(k, E_DMA(sa, 0), E_DMA(sa, 1), HE(e), HE((e) + 1)); HE((e) + 2); } while (0)
#define FB(...) PIN_AFTER("s_waitcnt vmcnt(10) lgkmcnt(0)\n\ts_barrier", __VA_ARGS__)
    D_READ_A(e0, EP(0)); D_READ_B(e0, EP(0));
    E_STAGE(0, HE(7), HE(8), HE(9), ); E_STAGE(1, HE(10), , HE(11), );
    BAR();
    ex[0] = V4(OA, 0); ex[64] = V4(OA, 1); ex[128] = V4(OB, 0); ex[192] = V4(OB, 1); el[0] = OA[8]; el[64] = OB[8];
    W2(2, 5, 6, 12);   FB(HP(7), HP(8), HP(9), HP(10), HP(11));
    W2(3, 7, 8, 13);   FB(HP(12));
    W1(4, 9, 14);      FB(HP(13));
    W2(5, 10, 14, 17); FB(HP(14), HP(15), HP(16));
    W2(6, 11, 15, 18); FB(HP(17));
    W1(7, 12, 19);     FB(HP(18));
    W1(8, 13, 22);     FB(HP(19), HP(20), HP(21));
    W2(9, 16, 17, 25);
    E_STAGE(10, HE(26), HE(27), BLOAD(xv, off, rx, 0u), );
    if (b == 193 && wid == 0) {
#pragma unroll 1
      for (int i = 0; i < 4; ++i) wait_flags(ws + WS_FH + i * 512, 64, tag, lane);
      if (lane == 0) set_flag(ws + WS_EPOCH, tag);
    }
    ST(8);
    PIN_AFTER("s_waitcnt vmcnt(5) lgkmcnt(0)\n\ts_barrier", HP(22), HP(23), HP(24), HP(25));
    ST(9);
    E_STAGE(11, , , , ); E_STAGE(12, , , , );
    PIN_AFTER("s_waitcnt vmcnt(0)", HP(26), HP(27), "+v"(xv));
    E_STAGE(13, , , , );
    put_partial(kq ? RADDR(1) : RADDR(0));
    ST(10);
    BAR();
    ST(11);
  } else {
    ST(8); STC(15);
    auto finish_o = [&](f32x16 O, const bf16x8& Q, int i) {
      float l = O[8], mag = __builtin_fabsf(O[0]);
#pragma unroll
      for (int j = 1; j < 8; ++j) mag += __builtin_fabsf(O[j]);
      const bool bad = !(l < 0x1p120f) || !(mag < 0x1p126f) || !(xhalf_max(mag) > 0x1p-100f);
      if (__builtin_expect(__any(bad), 0)) {
        const f32x16 z = {};
        float mt = -__builtin_inff();
#pragma unroll 1
        for (int tt = 0; tt < 64; ++tt) {
          const f32x16 s = MFMA(__builtin_bit_cast(bf16x8, __builtin_amdgcn_raw_buffer_load_b128(rkv, voff, tt * 2048, 16)), Q, z);
#pragma unroll
          for (int j = 0; j < 16; ++j) mt = __builtin_fmaxf(mt, s[j]);
        }
        mt = xhalf_max(mt);
        O = z;
#pragma unroll 1
        for (int tt = 0; tt < 64; ++tt) {
          f32x16 s = MFMA(__builtin_bit_cast(bf16x8, __builtin_amdgcn_raw_buffer_load_b128(rkv, voff, ((tt + 63) & 63) * 2048, 16)), Q, z);
          bf16x8 v0 = __builtin_bit_cast(bf16x8, __builtin_amdgcn_raw_buffer_load_b128(rkv, vvo, ((tt + 63) & 63) * 2048 + 1024, 16));
          bf16x8 v1 = __builtin_bit_cast(bf16x8, __builtin_amdgcn_raw_buffer_load_b128(rkv, vvo, tt * 2048 + 1536, 16));
          if (r >= 16) { v0 = PAD; v1 = PAD; }
#pragma unroll
          for (int j = 0; j < 16; ++j) s[j] = __builtin_amdgcn_exp2f(s[j] - mt);
          O = MFMA(v0, __builtin_bit_cast(bf16x8, pack8(s, 0)), O);
          O = MFMA(v1, __builtin_bit_cast(bf16x8, pack8(s, 8)), O);
        }
        l = O[8];
      }
      const float il = __builtin_amdgcn_rcpf(l);
#pragma unroll
      for (int j = 0; j < 8; ++j) O[j] *= il;
      BSTORE_DEV(pack8(O, 0), ro, (unsigned)((qt0 + i) * 32 + head) * 1024 + voff);
    };
#pragma unroll
    for (int j = 0; j < 4; ++j) { OA[j] += p0[j]; OA[4 + j] += p1[j]; OB[j] += p2[j]; OB[4 + j] += p3[j]; }
    OA[8] += pl0; OB[8] += pl1;
    finish_o(OA, QA, 0); finish_o(OB, QB, 1);
    BAR();
    ST(9);
    TR(58);
    wait_vm<0>();
    TR(59);
    __builtin_amdgcn_raw_buffer_store_b64(__builtin_bit_cast(u32x2, tag), mk_rsrc(ws + WS_FO, 16384 * FO_STRIDE),
                                          lane < 2 ? (unsigned)((qt0 + lane) * 32 + head) * 8 * FO_STRIDE : 0x7ff00000u, 0, 16);
    BLOAD(xv, off, rx, 0u);
    D_READ_A(e0, EP(28)); D_READ_B(e0, EP(28));
    d0 = d1 = (f32x16){};
    if (wid == 4) wait_flags(ws + WS_FO + rb * 512 * FO_STRIDE, 64, tag, lane, FO_STRIDE);
    PIN_AFTER("s_waitcnt vmcnt(0) lgkmcnt(0)\n\ts_barrier", "+v"(xv));
    ST(10);
    u32x4 oa[8], ob[8];
    const unsigned obase = (unsigned)((rb * 2 + rt) * 32 + (kq & 1) * 2) * 1024;
#pragma unroll
    for (int j = 0; j < 8; ++j) { BLOAD_DEV(oa[j], voff, ro, obase + j * 4096); BLOAD_DEV(ob[j], voff, ro, obase + j * 4096 + 1024); }
    ST(11);
#define O_STEP(c, n, j) do { wait_vm2<14 - 2 * (j)>(oa[j], ob[j]); D_STEPX(c, n, EP(29 + (j)), oa[j], ob[j], (j) < 7, , ); } while (0)
    O_STEP(e0, e1, 0); O_STEP(e1, e0, 1); O_STEP(e0, e1, 2); O_STEP(e1, e0, 3); O_STEP(e0, e1, 4); O_STEP(e1, e0, 5); O_STEP(e0, e1, 6); O_STEP(e1, e0, 7);
    put_partial((kq & 1) ? RADDR(3) : RADDR(2));
  }
  BAR();
  f32x4 s0 = {bf_lo(xv[0]), bf_hi(xv[0]), bf_lo(xv[1]), bf_hi(xv[1])}, s1 = {bf_lo(xv[2]), bf_hi(xv[2]), bf_lo(xv[3]), bf_hi(xv[3])};
  {
    const int row = t >> 3, c = (t & 7) * 2;
    const lptr rd0 = lds + row * 256 + ((c ^ (row & 15)) * 16), rd1 = lds + row * 256 + (((c + 1) ^ (row & 15)) * 16);
#pragma unroll
    for (int p = 0; p < 4; ++p) {
      const int o = RADDR(p);
      s0 += *(const LAS f32x4*)(rd0 + o); s1 += *(const LAS f32x4*)(rd1 + o);
    }
  }
  __builtin_amdgcn_raw_buffer_store_b128((u32x4){pk2(s0[0], s0[1]), pk2(s0[2], s0[3]), pk2(s1[0], s1[1]), pk2(s1[2], s1[3])}, mk_rsrc(out, S_ * D_ * 2), off, 0, OUT_AUX);
  asm volatile("Lk_tail:");
  ST(12); ST_OUT(ws); TR_OUT(ws);
}

extern "C" void kernel_launch(void* const* d_in, const int* in_sizes, int n_in, void* d_out, int out_size, void* d_ws,
                              size_t ws_size, hipStream_t stream) {
  (void)hipFuncSetAttribute((const void*)k_all, hipFuncAttributeMaxDynamicSharedMemorySize, LDS_BYTES);
  k_all<<<256, 512, LDS_BYTES, stream>>>((const char*)d_in[0], (const char*)d_in[1], (const char*)d_in[2], (const char*)d_in[4],
                                         (const char*)d_in[5], (const char*)d_in[6], (char*)d_ws, (char*)d_out);
}
```

```cpp
#include <hip/hip_runtime.h>
#include <hip/hip_bf16.h>
#include <stdint.h>

typedef __attribute__((ext_vector_type(8))) short bf16x8;
typedef __attribute__((ext_vector_type(4))) short s16x4;
typedef __attribute__((ext_vector_type(16))) float f32x16;
typedef __attribute__((ext_vector_type(4))) float f32x4;
typedef __attribute__((ext_vector_type(2))) float f32x2;
typedef __attribute__((ext_vector_type(4))) unsigned u32x4;
typedef __attribute__((ext_vector_type(2))) unsigned u32x2;
typedef __attribute__((ext_vector_type(2))) __bf16 bf2;
typedef unsigned long long u64;
typedef __amdgpu_buffer_rsrc_t rsrc_t;
#define LAS __attribute__((address_space(3)))
#define GAS __attribute__((address_space(1)))
typedef LAS char* lptr;

#define MFMA(a, b, c) __builtin_amdgcn_mfma_f32_32x32x16_bf16((a), (b), (c), 0, 0, 0)
#define MM(c, a, b) do { __builtin_amdgcn_sched_barrier(0); c = MFMA(a, b, c); asm volatile("" : "+v"(c) :: "memory"); \
                         __builtin_amdgcn_sched_barrier(0); } while (0)
#define RFL(x) __builtin_amdgcn_readfirstlane(x)

constexpr int S_ = 2048, D_ = 512, F_ = 1792;
constexpr size_t WS_EPOCH = 0;
constexpr size_t WS_FH = 2048;
constexpr size_t WS_FQ = 16384;
constexpr size_t WS_FX = 49152;
constexpr size_t WS_FO = 32768;
constexpr size_t WS_Q    = 65536;
constexpr size_t WS_KV   = WS_Q + 2097152;
constexpr size_t WS_O    = WS_KV + 4194304;
constexpr size_t WS_XN   = WS_O + 2097152;
constexpr size_t WS_H    = WS_XN + 2097152;
constexpr float QSCALE = 0.25f * 1.4426950408889634f;
constexpr float MBIAS = 80.0f;
constexpr int LDS_BYTES = 163840;
constexpr unsigned SPIN_CAP = 1u << 22;
constexpr unsigned OOB = 0x7ff00000u;

__device__ __forceinline__ rsrc_t mk_rsrc(const void* p, unsigned bytes) {
  return __builtin_amdgcn_make_buffer_rsrc((void*)p, (short)0, (int)bytes, 0x00020000);
}
__device__ __forceinline__ void dma16(rsrc_t r, unsigned voff, unsigned soff, unsigned lds) {
  unsigned keep;
  asm volatile("s_mov_b32 %0, m0\n\ts_mov_b32 m0, %1\n\ts_nop 0\n\tbuffer_load_dwordx4 %2, %3, %4 offen lds\n\ts_mov_b32 m0, %0"
               : "=&s"(keep) : "s"(lds), "v"(voff), "s"(r), "s"(soff) : "memory");
}
__device__ __forceinline__ void dma16_dev(rsrc_t r, unsigned voff, unsigned soff, unsigned lds) {
  unsigned keep;
  asm volatile("s_mov_b32 %0, m0\n\ts_mov_b32 m0, %1\n\ts_nop 0\n\tbuffer_load_dwordx4 %2, %3, %4 offen sc1 lds\n\ts_mov_b32 m0, %0"
               : "=&s"(keep) : "s"(lds), "v"(voff), "s"(r), "s"(soff) : "memory");
}
__device__ __forceinline__ void dma16x3(rsrc_t r, unsigned voff, unsigned soff, unsigned lds) {
  unsigned keep;
  asm volatile("s_mov_b32 %0, m0\n\ts_mov_b32 m0, %1\n\ts_nop 0\n\tbuffer_load_dwordx4 %2, %3, %4 offen lds\n\t"
               "buffer_load_dwordx4 %2, %3, %4 offen offset:1024 lds\n\tbuffer_load_dwordx4 %2, %3, %4 offen offset:2048 lds\n\ts_mov_b32 m0, %0"
               : "=&s"(keep) : "s"(lds), "v"(voff), "s"(r), "s"(soff) : "memory");
}
__device__ __forceinline__ float fma1(float a, float b, float c) {
  float d; asm("v_fma_f32 %0, %1, %2, %3" : "=v"(d) : "v"(a), "v"(b), "v"(c)); return d;
}
__device__ __forceinline__ float add1(float a, float b) { float d; asm("v_add_f32 %0, %1, %2" : "=v"(d) : "v"(a), "v"(b)); return d; }
__device__ __forceinline__ float mul1(float a, float b) { float d; asm("v_mul_f32 %0, %1, %2" : "=v"(d) : "v"(a), "v"(b)); return d; }
#define BLOAD(dst, voff, r, soff) asm volatile("buffer_load_dwordx4 %0, %1, %2, %3 offen" : "=v"(dst) : "v"(voff), "s"(r), "s"(soff) : "memory")
#define BLOAD_DEV(dst, voff, r, soff) asm volatile("buffer_load_dwordx4 %0, %1, %2, %3 offen sc1" : "=v"(dst) : "v"(voff), "s"(r), "s"(soff) : "memory")
#define BSTORE_DEV(val, r, voff) __builtin_amdgcn_raw_buffer_store_b128((val), (r), (voff), 0, 16)
#define WAIT_BAR(N) asm volatile("s_waitcnt vmcnt(" #N ") lgkmcnt(0)\n\ts_barrier" ::: "memory")
#define BAR() asm volatile("s_waitcnt lgkmcnt(0)\n\ts_barrier" ::: "memory")
template <int N> __device__ __forceinline__ void wait_bar() { asm volatile("s_waitcnt vmcnt(%0) lgkmcnt(0)\n\ts_barrier" :: "n"(N) : "memory"); }
template <int N> __device__ __forceinline__ void wait_vm2(u32x4& a, u32x4& b) {
  asm volatile("s_waitcnt vmcnt(%0)" :: "n"(N) : "memory"); __builtin_amdgcn_sched_barrier(0); asm volatile("" : "+v"(a), "+v"(b) :: "memory");
}
template <int N> __device__ __forceinline__ void wait_vm1(u32x4& a) {
  asm volatile("s_waitcnt vmcnt(%0)" :: "n"(N) : "memory"); __builtin_amdgcn_sched_barrier(0); asm volatile("" : "+v"(a) :: "memory");
}

__device__ __forceinline__ unsigned pk2(float lo, float hi) {
  f32x2 v = {lo, hi};
  return __builtin_bit_cast(unsigned, __builtin_convertvector(v, bf2));
}
__device__ __forceinline__ float bf_lo(unsigned u) { return __uint_as_float(u << 16); }
__device__ __forceinline__ float bf_hi(unsigned u) { return __uint_as_float(u & 0xffff0000u); }
__device__ __forceinline__ float xhalf_max(float v) {
  auto r = __builtin_amdgcn_permlane32_swap(__float_as_uint(v), __float_as_uint(v), false, false);
  return __builtin_fmaxf(__uint_as_float(r[0]), __uint_as_float(r[1]));
}
__device__ __forceinline__ bf16x8 lds_frag(lptr p) { return *(const LAS bf16x8*)p; }
typedef short v4i16_t __attribute__((ext_vector_type(4)));
__device__ __forceinline__ bf16x8 lds_frag_tr(lptr p0, lptr p1) {
  s16x4 a = __builtin_bit_cast(s16x4, __builtin_amdgcn_ds_read_tr16_b64_v4i16((LAS v4i16_t*)p0));
  s16x4 b = __builtin_bit_cast(s16x4, __builtin_amdgcn_ds_read_tr16_b64_v4i16((LAS v4i16_t*)p1));
  return (bf16x8){a[0], a[1], a[2], a[3], b[0], b[1], b[2], b[3]};
}
__device__ __forceinline__ float dot2(unsigned a, unsigned b, float c) {
  return __builtin_amdgcn_fdot2_f32_bf16(__builtin_bit_cast(bf2, a), __builtin_bit_cast(bf2, b), c, false);
}
__device__ __forceinline__ u32x4 pack8(const f32x16& a, int o) {
  return (u32x4){pk2(a[o], a[o + 1]), pk2(a[o + 2], a[o + 3]), pk2(a[o + 4], a[o + 5]), pk2(a[o + 6], a[o + 7])};
}
template <int CTRL> __device__ __forceinline__ float dpp_add(float x) {
  return x + __builtin_bit_cast(float, __builtin_amdgcn_mov_dpp(__builtin_bit_cast(int, x), CTRL, 0xf, 0xf, true));
}
__device__ __forceinline__ float sum8(float x) { return dpp_add<0x141>(dpp_add<0x4E>(dpp_add<0xB1>(x))); }

__device__ __forceinline__ u64 read_tag(const char* ws) {
  u64 e;
  asm volatile("s_load_dwordx2 %0, %1, 0x0\n\ts_waitcnt lgkmcnt(0)" : "=s"(e) : "s"(ws + WS_EPOCH) : "memory");
  e = (e ^ (e >> 31)) * 0x9E3779B97F4A7C15ull + 0x632BE59BD9B4E019ull;
  return e ^ (e >> 29);
}
__device__ __forceinline__ void set_flag(char* p, u64 tag) { __hip_atomic_store((GAS u64*)p, tag, __ATOMIC_RELAXED, __HIP_MEMORY_SCOPE_AGENT); }
__device__ __forceinline__ void wait_flags(const char* p, int n, u64 tag, int lane) {
  GAS u64* f = (GAS u64*)p + (lane < n ? lane : 0);
#pragma unroll 1
  for (unsigned spins = 0; spins < SPIN_CAP; ++spins) {
    if (__all(__hip_atomic_load(f, __ATOMIC_RELAXED, __HIP_MEMORY_SCOPE_AGENT) == tag)) break;
    __builtin_amdgcn_s_sleep(4);
  }
  asm volatile("" ::: "memory");
}


__device__ __forceinline__ void dma16x2(rsrc_t r, unsigned voff, unsigned soff0, unsigned soff1, unsigned lds) {
  unsigned keep;
  asm volatile("s_mov_b32 %0, m0\n\ts_mov_b32 m0, %1\n\ts_nop 0\n\tbuffer_load_dwordx4 %2, %3, %4 offen lds\n\t"
               "buffer_load_dwordx4 %2, %3, %5 offen offset:1024 lds\n\ts_mov_b32 m0, %0"
               : "=&s"(keep) : "s"(lds), "v"(voff), "s"(r), "s"(soff0), "s"(soff1 - 1024u) : "memory");
}
template <int N> __device__ __forceinline__ void wait_vm() { asm volatile("s_waitcnt vmcnt(%0)" :: "n"(N) : "memory"); }
#define PIN_AFTER(W, ...) do { asm volatile(W ::: "memory"); __builtin_amdgcn_sched_barrier(0); asm volatile("" : __VA_ARGS__ :: "memory"); } while (0)
#define V4(c, g_) (f32x4){c[4 * (g_)], c[4 * (g_) + 1], c[4 * (g_) + 2], c[4 * (g_) + 3]}

constexpr int WR = 65536, QSLOT = 24576;
constexpr int GSLOT = 20480;
constexpr int REC = 2080, CHUNK = 4 * REC;
constexpr int KVA = WR + 3 * GSLOT, KVB = KVA - 4 * CHUNK;

struct F4 { bf16x8 x0, x1, w0, w1; };
struct QFrag { bf16x8 x, q, k, v; };

template <int G>
__device__ __forceinline__ void qkv_steps(const lptr (&pa)[4], lptr pw, lptr pw2, QFrag& c, QFrag& n, f32x16& aq, f32x16& ak, f32x16& av,
                                          char* fx, u64 tag, int lane, bool shares) {
  if constexpr (G < 32) {
    constexpr int s = G >> 2, N = G + 1, ns = N >> 2;
    if constexpr ((G & 3) == 3) {
      if constexpr (s == 0) { if (!shares) wait_vm<7>(); }
      if constexpr (s == 0) wait_bar<9>(); else if constexpr (s == 1) wait_bar<4>(); else if constexpr (s == 2) wait_bar<1>(); else BAR();
      if constexpr (s == 1) { if (lane == 0) set_flag(fx, tag); }
    }
    const lptr w = ((ns & 2) ? pw2 : pw) + (ns & 1) * QSLOT + (N & 3) * 6144;
    MM(aq, c.q, c.x);
    if constexpr (N < 32) { n.x = lds_frag(pa[N & 3] + ns * 4096); n.q = lds_frag_tr(w, w + 256); }
    MM(ak, c.k, c.x);
    if constexpr (N < 32) { n.k = lds_frag_tr(w + 1024, w + 1024 + 256); n.v = lds_frag_tr(w + 2048, w + 2048 + 256); }
    MM(av, c.x, c.v);
    qkv_steps<G + 1>(pa, pw, pw2, n, c, aq, ak, av, fx, tag, lane, shares);
  }
}

#define EXP(x) "v_exp_f32 v" #x ", v" #x "\n\t"
#define CVT(d, a, b) "v_cvt_pk_bf16_f32 v" #d ", v" #a ", v" #b "\n\t"
#define MF(d, a, b, c) "v_mfma_f32_32x32x16_bf16 " d ", " a ", " b ", " c "\n\t"
#define HALF(c0, c1, c2, c3, c4, c5, c6, c7, c8, c9, c10, c11, c12, c13, c14, c15, QK, PV1, PV0, X0, X1) \
  QK EXP(c0) EXP(c1) EXP(c2) EXP(c3) PV1 X0 \
  EXP(c4) EXP(c5) CVT(80, c0, c1) CVT(81, c2, c3) EXP(c6) EXP(c7) EXP(c8) EXP(c9) CVT(82, c4, c5) CVT(83, c6, c7) \
  EXP(c10) EXP(c11) PV0 X1 \
  EXP(c12) EXP(c13) CVT(84, c8, c9) CVT(85, c10, c11) EXP(c14) EXP(c15) CVT(86, c12, c13) CVT(87, c14, c15)
#define TILE(off, S, X0, X1, X2, X3) \
  S \
  HALF(16, 17, 18, 19, 20, 21, 22, 23, 24, 25, 26, 27, 28, 29, 30, 31, \
       MF("v[32:47]", "v[68:71]", "v[92:95]", "v[96:111]") "ds_read_b128 v[68:71], v88 offset:" off "\n\t", \
       MF("v[112:127]", "v[76:79]", "v[84:87]", "v[112:127]") "ds_read_b128 v[76:79], v90 offset:" off "\n\t", \
       "s_waitcnt lgkmcnt(2)\n\t" MF("v[48:63]", "v[72:75]", "v[80:83]", "v[48:63]"), X0, X1) \
  HALF(32, 33, 34, 35, 36, 37, 38, 39, 40, 41, 42, 43, 44, 45, 46, 47, \
       "s_waitcnt lgkmcnt(0)\n\t" MF("v[16:31]", "v[68:71]", "v[64:67]", "v[0:15]"), \
       MF("v[48:63]", "v[76:79]", "v[84:87]", "v[48:63]"), \
       MF("v[112:127]", "v[72:75]", "v[80:83]", "v[112:127]") "ds_read_b128 v[72:75], v89 offset:" off "\n\t", X2, X3)
#define T_NONE(off) TILE(off, "", "", "", "", "")
#define T_KV(off, m, so) TILE(off, "s_mov_b32 m0, %[" #m "]\n\t", "", "buffer_load_dwordx4 %[voff], %[rkv], %[" #so "] offen sc1 lds\n\t", \
    "buffer_load_dwordx4 %[voff], %[rkv], %[" #so "] offen offset:1024 sc1 lds\n\t", "")
#define T_WD(off, st) TILE(off, "s_add_u32 m0, %[dm0], 16384*" #st "\n\ts_add_u32 %[t0], %[ds0], 131072*" #st "\n\t", "", \
    "buffer_load_dwordx4 %[vw1], %[rwd], %[t0] offen lds\n\ts_add_u32 %[t0], %[t0], 7168\n\t", "buffer_load_dwordx4 %[vw1], %[rwd], %[t0] offen offset:1024 lds\n\t", "")
#define T_KVI(off, MO, SO) TILE(off, "s_add_u32 m0, %[kvm], " #MO "\n\ts_add_u32 %[t0], %[kvs], " #SO "\n\t", "", \
    "buffer_load_dwordx4 %[voff], %[rkv], %[t0] offen sc1 lds\n\t", "buffer_load_dwordx4 %[voff], %[rkv], %[t0] offen offset:1024 sc1 lds\n\t", "")
#define VB(n) "s_waitcnt vmcnt(" #n ")\n\ts_barrier\n\t"
#define SB_ "s_barrier\n\t"
#define HLA(d) "buffer_load_dwordx4 %[" #d "], %[voff], %[rh], %[hs] offen sc1\n\t"
#define HLB(d) "buffer_load_dwordx4 %[" #d "], %[voff], %[rh], %[hs] offen offset:1024 sc1\n\ts_add_u32 %[hs], %[hs], 4096\n\t"
#define T_H2(off, da, db, dc, dd) TILE(off, "", HLA(da), HLB(db), HLA(dc), HLB(dd))
#define ATT_BEGIN "s_mov_b32 %[keep], m0\n\ts_waitcnt lgkmcnt(0)\n\ts_nop 2\n\t"
#define ATT_END "s_mov_b32 m0, %[keep]\n\ts_waitcnt lgkmcnt(0)\n\ts_nop 3\n\t"
#define ATT_STATE "+{v[48:63]}"(OA), "+{v[112:127]}"(OB), "+{v[16:31]}"(SA), "+{v[68:71]}"(K), "+{v[72:75]}"(V0), "+{v[76:79]}"(V1), \
                  "+{v[84:87]}"(P1), [t0] "=&s"(t0), [keep] "=&s"(keep)
#define ATT_CONST "{v[0:15]}"(CA), "{v[96:111]}"(CB), "{v[64:67]}"(QA), "{v[92:95]}"(QB), [voff] "v"(voff)
#define ATT_IN ATT_CONST, "{v88}"(aK), "{v89}"(aV0), "{v90}"(aV1)
#define ATT_CLOB "v32", "v33", "v34", "v35", "v36", "v37", "v38", "v39", "v40", "v41", "v42", "v43", "v44", "v45", "v46", "v47", \
                 "v80", "v81", "v82", "v83", "scc", "memory"

struct GFrag { bf16x8 x[4], g, u; };
__host__ __device__ constexpr int gslot(int s) { return s < 8 ? (s & 3) : (s - 8) % 3; }
template <int S, int KG>
__device__ __forceinline__ void gu_read(const lptr (&pa)[4], const lptr (&pp)[4], lptr pg, lptr pg3, GFrag& n, int part) {
  const lptr x = pa[(S & 1) * 2 + KG] + (S >> 1) * 4096, y = pp[(S & 1) * 2 + KG] + gslot(S) * GSLOT, w = (gslot(S) == 3 ? pg3 : pg + gslot(S) * GSLOT) + KG * 4096;
  if (part == 0) { n.x[0] = lds_frag(x); n.g = lds_frag_tr(w, w + 256); }
  if (part == 1) { n.x[1] = lds_frag(x + 32768); n.u = lds_frag_tr(w + 8192, w + 8192 + 256); }
  if (part == 2) { n.x[2] = lds_frag(y); n.x[3] = lds_frag(y + 2048); }
}
template <int S, class FILL>
__device__ __forceinline__ void gu_stages(const lptr (&pa)[4], const lptr (&pp)[4], lptr pg, lptr pg3, GFrag& f0, GFrag& f1, f32x16 (&g)[4], f32x16 (&u)[4], FILL fill) {
  if constexpr (S < 16) {
#define GU8(c, R0, R1, R2, D) do { MM(g[0], c.g, c.x[0]); R0; MM(g[1], c.g, c.x[1]); R1; MM(u[0], c.u, c.x[0]); R2; MM(u[1], c.u, c.x[1]); \
      MM(g[2], c.g, c.x[2]); D; MM(u[2], c.u, c.x[2]); MM(g[3], c.g, c.x[3]); MM(u[3], c.u, c.x[3]); } while (0)
    GU8(f0, (gu_read<S, 1>(pa, pp, pg, pg3, f1, 0)), (gu_read<S, 1>(pa, pp, pg, pg3, f1, 1)), (gu_read<S, 1>(pa, pp, pg, pg3, f1, 2)), );
    if constexpr (S < 15) {
      if constexpr (S == 0) wait_bar<6>(); else if constexpr (S == 2 || S == 14) wait_bar<0>(); else wait_bar<5>();
      GU8(f1, (gu_read<S + 1, 0>(pa, pp, pg, pg3, f0, 0)), (gu_read<S + 1, 0>(pa, pp, pg, pg3, f0, 1)), (gu_read<S + 1, 0>(pa, pp, pg, pg3, f0, 2)),
          if (S >= 8 && S <= 12) fill(S + 3));
    } else {
      GU8(f1, , , , );
    }
    gu_stages<S + 1>(pa, pp, pg, pg3, f0, f1, g, u, fill);
  }
}

__global__ __launch_bounds__(512, 2) void k_all(const char* __restrict__ gx, const char* __restrict__ gwqkv, const char* __restrict__ gwo,
                                                const char* __restrict__ gwg, const char* __restrict__ gwu, const char* __restrict__ gwd,
                                                char* __restrict__ ws, char* __restrict__ out) {
  extern __shared__ __attribute__((aligned(16))) char smem[];
  const lptr lds = (lptr)smem;
  const unsigned lds0 = (unsigned)(uintptr_t)lds;
  const int lane = threadIdx.x & 63, wid = RFL(threadIdx.x >> 6), b = blockIdx.x;
  const int xcd = b & 7, bi = b >> 3;
  const int rb = (xcd >> 1) * 8 + (bi & 7);
  const int cb = (xcd & 1) * 4 + (bi >> 3);
  const int cq = (cb + 4 - (int)((uintptr_t)gwqkv >> 7)) & 7, co = (cb + 4 - (int)((uintptr_t)gwd >> 7)) & 7;
  const int rot = (rb + 2 * cb) & 7;
  const int head = xcd * 4 + (bi >> 3);
  const int r = lane & 31, h = lane >> 5;
  const unsigned trn = ((lane & 15) >> 2) * 64 + ((lane >> 4) & 1) * 32 + (lane & 3) * 8;
  const unsigned voff = lane * 16;
  unsigned cpf;
  {
    u64 pc; unsigned len;
    asm volatile("s_getpc_b64 %0\nLk_here:\n\ts_mov_b32 %1, Lk_tail-Lk_here" : "=s"(pc), "=s"(len));
    const unsigned o = (unsigned)((bi * 8 + wid) * 2 + (lane < 2 ? lane : 0)) * 128;
    asm volatile("global_load_dword %0, %1, off" : "=v"(cpf) : "v"((const char*)pc + (o + 4 <= len ? o : 0)) : "memory");
  }
  __builtin_amdgcn_sched_barrier(0);

  const rsrc_t rx = mk_rsrc(gx, S_ * D_ * 2), rw = mk_rsrc(gwqkv, D_ * 3 * D_ * 2), rxn = mk_rsrc(ws + WS_XN, 2097152);
  u32x4 xr[8];
  {
    const unsigned vx = (lane >> 3) * 1024 + (lane & 7) * 16, sx = (unsigned)(rb * 64 + wid * 8) * 1024;
#pragma unroll
    for (int i = 0; i < 8; ++i) BLOAD(xr[i], vx, rx, sx + ((i + rot) & 7) * 128);
  }
  BAR();
  const unsigned vw3 = ((lane >> 2) & 7) * 3072 + (lane >> 5) * 64 + (lane & 3) * 16;
  auto issue_wk = [&](int s, int kb) {
    const unsigned so = (unsigned)(kb * 8) * 3072 + ((s + rot) & 7) * (64 * 3072);
    dma16x3(rw, vw3, so + cq * 128, lds0 + WR + (s & 3) * QSLOT + kb * 3072);
  };
  auto issue_w = [&](int s) { issue_wk(s, wid); };
  issue_w(0); issue_w(1);
  unsigned pf;
#define TOUCH() asm volatile("global_load_dword %0, %1, off" : "=v"(pf) : "v"((h ? gwu : gwg) + (size_t)((b * 8 + wid) * 7 + (r < 7 ? r : 0)) * 128) : "memory")
  const u64 tag = read_tag(ws);
  float rs, mr;
  {
    const unsigned ONE2 = 0x3f803f80u;
    float s0 = 0.f, s1 = 0.f, q0 = 0.f, q1 = 0.f;
#define STAT(i) do { wait_vm1<13 - (i)>(xr[i]); \
      s0 = dot2(xr[i][0], ONE2, s0); q0 = dot2(xr[i][0], xr[i][0], q0); s1 = dot2(xr[i][1], ONE2, s1); q1 = dot2(xr[i][1], xr[i][1], q1); \
      s0 = dot2(xr[i][2], ONE2, s0); q0 = dot2(xr[i][2], xr[i][2], q0); s1 = dot2(xr[i][3], ONE2, s1); q1 = dot2(xr[i][3], xr[i][3], q1); \
      asm volatile("" : "+v"(s0), "+v"(s1), "+v"(q0), "+v"(q1)); } while (0)
    STAT(0); STAT(1); STAT(2); STAT(3); STAT(4); STAT(5); STAT(6); STAT(7);
    const float mean = sum8(s0 + s1) * (1.0f / 512.0f);
    rs = __builtin_amdgcn_rsqf(__builtin_fmaxf(sum8(q0 + q1) * (1.0f / 512.0f) - mean * mean, 0.0f) + 1e-5f);
    mr = -mean * rs;
  }
  const unsigned xo = (lane & 7) * 512 + ((((wid & 3) * 8 + (lane >> 3)) ^ (lane & 7)) * 16);
  const lptr pxw = lds + (wid >> 2) * 32768 + xo;
#pragma unroll
  for (int j = 0; j < 8; ++j) {
    u32x4 n;
#pragma unroll
    for (int i = 0; i < 4; ++i) n[i] = pk2(__builtin_fmaf(bf_lo(xr[j][i]), rs, mr), __builtin_fmaf(bf_hi(xr[j][i]), rs, mr));
    if (j < 2) BSTORE_DEV(n, rxn, (cb < 4 ? 0u : OOB) + (unsigned)(rb * 8 + ((rot + j) & 7)) * 8192 + (wid >> 2) * 4096 + xo);
    *(LAS u32x4*)(pxw + j * 4096) = n;
  }
  issue_w(2); issue_w(3);
  TOUCH();
  const bool has_ffn = cb < 7;
  const int fb = cb * 2 + (rb & 1), w4 = wid & 3;
  const rsrc_t rg = mk_rsrc(gwg - 1024, D_ * F_ * 2 + 1024), ru = mk_rsrc(gwu - 1024, D_ * F_ * 2 + 1024);
  const unsigned vwf = ((lane >> 2) & 7) * (F_ * 2) + (lane >> 5) * 64 + (lane & 3) * 16;
  auto gu_w = [&](int s) {
    const unsigned so = (has_ffn ? 0u : OOB) + (unsigned)(w4 * 8) * (F_ * 2) + fb * 256 + (unsigned)((s + 2 * rot) & 15) * (32 * F_ * 2) + 1024;
    const unsigned d = lds0 + WR + gslot(s) * GSLOT + w4 * 2048;
    dma16x2(rg, vwf, so, so + 128, d); dma16x2(ru, vwf, so, so + 128, d + 8192);
  };
  auto gu_x = [&](int s) {
    dma16_dev(rxn, voff, (has_ffn ? 0u : OOB) + (unsigned)((rb ^ 1) * 8 + (((s >> 1) + rot) & 7)) * 8192 + (w4 >> 1) * 4096 + (s & 1) * 2048 + (w4 & 1) * 1024,
              lds0 + WR + gslot(s) * GSLOT + 16384 + w4 * 1024);
  };
  auto gu_fill = [&](int s) { gu_w(s); gu_x(s); };
  u64 fl;
  lptr pa[4];
#pragma unroll
  for (int g = 0; g < 4; ++g) pa[g] = lds + (2 * g + h) * 512 + ((r ^ (2 * g + h)) * 16);

  const rsrc_t rkv = mk_rsrc(ws + WS_KV + (size_t)head * 131072, 131072), rh = mk_rsrc(ws + WS_H, 7340032);
  const unsigned vvo = h * 256 + (r & 15) * 16;
  const bf16x8 ONES = {0x3f80, 0x3f80, 0x3f80, 0x3f80, 0x3f80, 0x3f80, 0x3f80, 0x3f80};
  const bf16x8 PAD = ((r & 27) == 16) ? ONES : (bf16x8){};
  const int qt0 = (bi & 7) * 8 + w4 * 2;
  const unsigned ring = lds0 + (wid < 4 ? KVA : KVB), kt0 = wid < 4 ? 0 : 32;
  const unsigned kvm = ring + w4 * REC, kvs = (kt0 + w4) * 2048;
  u32x4 QAu, QBu, Ku, V0u;
  auto att_loads = [&] {
    const rsrc_t rq = mk_rsrc(ws + WS_Q, 2097152);
    BLOAD_DEV(QAu, voff, rq, (unsigned)(head * 64 + qt0) * 1024);
    BLOAD_DEV(QBu, voff, rq, (unsigned)(head * 64 + qt0 + 1) * 1024);
    BLOAD_DEV(Ku, voff, rkv, ((kt0 + 63) & 63) * 2048);
    BLOAD_DEV(V0u, vvo, rkv, ((kt0 + 63) & 63) * 2048 + 1024);
  };
  auto att_ring = [&] {
    dma16_dev(rkv, voff, kvs, kvm); dma16_dev(rkv, voff, kvs + 1024, kvm + 1024);
    dma16_dev(rkv, voff, kvs + 8192, kvm + CHUNK); dma16_dev(rkv, voff, kvs + 8192 + 1024, kvm + CHUNK + 1024);
    *(LAS bf16x8*)(lds + (ring - lds0) + (w4 * 4 + (r & 3)) * REC + 2048 + h * 16) = h ? (bf16x8){} : ONES;
  };
  LAS float* const mx = (LAS float*)(lds + KVA + 4 * CHUNK + w4 * 512) + lane;
  bf16x8 QA, QB, K, V0, V1, P1;
  f32x16 SA, CA, CB, OA, OB;
#define MV2(d, s) "v_mov_b64 v[" #d ":" #d "+1], " s "\n\t"
#define MV16(d, s) MV2(d, s) MV2(d + 2, s) MV2(d + 4, s) MV2(d + 6, s) MV2(d + 8, s) MV2(d + 10, s) MV2(d + 12, s) MV2(d + 14, s)
  auto att_init = [&](float ma, float mb) {
    QA = __builtin_bit_cast(bf16x8, QAu); QB = __builtin_bit_cast(bf16x8, QBu); K = __builtin_bit_cast(bf16x8, Ku);
    V0 = (r >= 16) ? PAD : __builtin_bit_cast(bf16x8, V0u);
    asm volatile(MV16(0, "%[ma]") MV2(76, "0") MV2(78, "0") MF("v[16:31]", "v[68:71]", "v[64:67]", "v[0:15]")
                 MV16(96, "%[mb]") MV16(48, "0") MV16(112, "0") MV2(84, "0") MV2(86, "0")
                 : "=&{v[0:15]}"(CA), "=&{v[96:111]}"(CB), "=&{v[16:31]}"(SA), "=&{v[48:63]}"(OA), "=&{v[112:127]}"(OB), "=&{v[76:79]}"(V1), "=&{v[84:87]}"(P1)
                 : "{v[68:71]}"(K), "{v[64:67]}"(QA), [ma] "v"((f32x2){ma, ma}), [mb] "v"((f32x2){mb, mb}));
  };
  const unsigned apad = ((r & 27) == 16) ? 2048 : 2064;
  const unsigned aK = ring + voff, aV0 = ring + (r < 16 ? 1024 + vvo : apad), aV1 = ring + (r < 16 ? 1536 + vvo : apad);
  unsigned t0, keep;
  const int rt = wid & 1, kq = wid >> 1;
  const rsrc_t rwd = mk_rsrc(gwd, F_ * D_ * 2), rwo = mk_rsrc(gwo, D_ * D_ * 2), ro = mk_rsrc(ws + WS_O, 2097152);
  const unsigned vw1 = ((lane >> 2) & 7) * 1024 + (lane >> 5) * 64 + (lane & 3) * 16;
#define DADDR(st) ((st) < 5 ? (st) * 16384 : (st) == 5 || (st) == 11 ? KVA : (st) < 11 ? ((st) - 6) * 16384 : (st) < 14 ? ((st) - 12) * 16384 : \
                   (st) == 14 ? 81920 : (st) == 15 ? 147456 : (st) == 16 ? 98304 : 32768)
  auto d_so = [&](int st, int pc) { return (unsigned)((st < 14 ? st : st - 14) * 128 + pc * 16) * 1024 + co * 128; };
  auto d_m = [&](int st, int pc) { return lds0 + DADDR(st) + pc * 2048; };
  auto issue_dp = [&](int st, int pc) { dma16x2(st < 14 ? rwd : rwo, vw1, d_so(st, pc), d_so(st, pc) + 8192, d_m(st, pc)); };
  auto h_so = [&](int e) { return (unsigned)((rb * 2 + rt) * 112 + e * 4 + kq * 2) * 1024; };
  const unsigned kvd = kvs + (wid < 4 ? 4 * 8192 : 0);
  u32x4 ha[14], hb[14], xv;
  const unsigned kve = wid < 4 ? OOB : kvd;
  LAS f32x4* ex = (LAS f32x4*)(lds + 147456 + w4 * 4096) + lane;
  LAS float* el = (LAS float*)(lds + 145408 + w4 * 512) + lane;
  if (cb >= 4) wait_vm<10>();
  WAIT_BAR(12);

  if (wid < 4) {
    {
      const int ch = wid >> 1;
      lptr par[4];
#pragma unroll
      for (int g = 0; g < 4; ++g) par[g] = pa[g] + rt * 32768;
      lptr pw = lds + WR + h * 3072 + ch * 512 + trn, pw2 = pw + 2 * QSLOT;
      asm volatile("" : "+v"(pw2));
      const int pair = cq * 2 + ch, tile = rb * 2 + rt;
      f32x16 aq = {}, ak = {}, av = {};
      QFrag f0, f1;
      f0.x = lds_frag(par[0]); f0.q = lds_frag_tr(pw, pw + 256); f0.k = lds_frag_tr(pw + 1024, pw + 1024 + 256); f0.v = lds_frag_tr(pw + 2048, pw + 2048 + 256);
      qkv_steps<0>(par, pw, pw2, f0, f1, aq, ak, av, ws + WS_FX + (rb * 8 + cb) * 8, tag, threadIdx.x, cb < 4);
#pragma unroll
      for (int i = 0; i < 16; ++i) aq[i] *= QSCALE;
      u32x4 qs[6] = {pack8(ak, 0), pack8(ak, 8), pack8(av, 0), pack8(av, 8), pack8(aq, 0), pack8(aq, 8)};
      asm volatile("" : "+v"(qs[0]), "+v"(qs[1]), "+v"(qs[2]), "+v"(qs[3]), "+v"(qs[4]), "+v"(qs[5]));
      const rsrc_t rq = mk_rsrc(ws + WS_Q, 2097152 + 4194304);
      const unsigned ko = 2097152 + (unsigned)((2 * pair) * 64 + ((tile + 63) & 63)) * 2048 + voff;
      const unsigned vo = 2097152 + (unsigned)(2 * pair + (r >> 4)) * 131072 + vvo;
      const unsigned qo = (unsigned)((2 * pair) * 64 + tile) * 1024 + voff;
      auto qst = [&](int i) {
        BSTORE_DEV(qs[i], rq, i == 0 ? ko : i == 1 ? ko + 131072 : i == 2 ? vo + ((tile + 63) & 63) * 2048 + 1024 : i == 3 ? vo + tile * 2048 + 1536 : i == 4 ? qo : qo + 65536);
      };
      auto fq_set = [&] {
        __builtin_amdgcn_raw_buffer_store_b64(__builtin_bit_cast(u32x2, tag), mk_rsrc(ws + WS_FQ, 8192),
                                              lane == 0 ? (unsigned)((cq * 2 + (wid >> 1)) * 64 + rb * 2 + (wid & 1)) * 8 : OOB, 0, 16);
      };
      if (__builtin_expect(has_ffn, 1)) {
        BAR();
        qst(0); qst(1); qst(2);
        BAR(); qst(3); qst(4); qst(5); gu_fill(4);
        BAR(); gu_fill(5);
        BAR(); gu_fill(6);
        WAIT_BAR(10); fq_set(); gu_fill(7);
        WAIT_BAR(10); gu_fill(8);
        WAIT_BAR(10); gu_fill(9);
        WAIT_BAR(10); gu_fill(10);
      } else {
        qst(0); qst(1); qst(2); qst(3); qst(4); qst(5);
        wait_vm<0>();
        fq_set();
#pragma unroll 1
        for (int i = 0; i < 8; ++i) BAR();
      }
    }
    if (!has_ffn) __builtin_amdgcn_s_sleep(32);
    asm volatile("global_load_dwordx2 %0, %1, off sc1" : "=v"(fl) : "v"(ws + WS_FQ + ((head >> 1) * 64 + lane) * 8) : "memory");
    WAIT_BAR(11);
    PIN_AFTER("s_waitcnt vmcnt(0)", "+v"(fl));
    if (__builtin_expect(!__all(fl == tag), 0)) wait_flags(ws + WS_FQ + (head >> 1) * 512, 64, tag, lane);
    u32x4 K1u;
    att_loads();
    BLOAD_DEV(K1u, voff, rkv, 0u);
    BAR();
    att_ring();
    PIN_AFTER("s_waitcnt vmcnt(4)", "+v"(QAu), "+v"(QBu), "+v"(Ku), "+v"(V0u), "+v"(K1u));
    float ma, mb;
    {
      const f32x16 z = {};
      const bf16x8 k0 = __builtin_bit_cast(bf16x8, Ku), k1 = __builtin_bit_cast(bf16x8, K1u), qa = __builtin_bit_cast(bf16x8, QAu), qb = __builtin_bit_cast(bf16x8, QBu);
      const f32x16 a0 = MFMA(k0, qa, z), a1 = MFMA(k1, qa, z), b0 = MFMA(k0, qb, z), b1 = MFMA(k1, qb, z);
      ma = a0[0]; mb = b0[0];
#pragma unroll
      for (int i = 0; i < 16; ++i) { ma = __builtin_fmaxf(__builtin_fmaxf(ma, a0[i]), a1[i]); mb = __builtin_fmaxf(__builtin_fmaxf(mb, b0[i]), b1[i]); }
      asm volatile("" : "+v"(ma), "+v"(mb));
    }
    BAR();
    ma = -(xhalf_max(ma) + MBIAS); mb = -(xhalf_max(mb) + MBIAS);
    mx[0] = ma; mx[64] = mb;
    att_init(ma, mb);
    asm volatile(ATT_BEGIN "s_setprio 2\n\t"
                 VB(2) T_KVI("0", 16640, 16384) T_NONE("2080") SB_ T_NONE("4160") T_NONE("6240")
                 VB(2) T_KVI("8320", 24960, 24576) T_NONE("10400") SB_ T_NONE("12480") T_NONE("14560")
                 VB(2) T_KVI("16640", 0, 32768) T_NONE("18720") SB_ T_NONE("20800") T_NONE("22880")
                 VB(2) T_KVI("24960", 8320, 40960) T_NONE("27040") SB_ T_NONE("29120") T_NONE("31200")
                 ATT_END
                 : ATT_STATE : ATT_IN, [rkv] "s"(rkv), [kvm] "s"(kvm), [kvs] "s"(kvs) : ATT_CLOB);
    __builtin_amdgcn_s_setprio(0);
    wait_vm<2>();
  } else {
    auto w2 = [&](int s) { issue_wk(s, w4 * 2); issue_wk(s, w4 * 2 + 1); };
    if (cb >= 4) wait_vm<7>();
    WAIT_BAR(9);  w2(4);
    WAIT_BAR(10); w2(5);
    WAIT_BAR(13); w2(6);
    WAIT_BAR(12); w2(7);
    WAIT_BAR(12);
    asm volatile("global_load_dwordx2 %0, %1, off sc1" : "=v"(fl) : "v"(ws + WS_FX + ((rb ^ 1) * 8 + (lane & 3)) * 8) : "memory");
    gu_w(0);
    if (!has_ffn) wait_vm<7>();
    WAIT_BAR(11); gu_w(1);
    if (!has_ffn) wait_vm<0>();
    PIN_AFTER("s_waitcnt vmcnt(8) lgkmcnt(0)\n\ts_barrier", "+v"(fl));
    const bool xok = __all(fl == tag) || !has_ffn;
    if (xok) gu_x(0);
    gu_w(2);
    if (xok) { gu_x(1); gu_x(2); }
    BAR();
    if (__builtin_expect(xok, 1)) { gu_fill(3); wait_vm<11>(); }
    else { wait_flags(ws + WS_FX + (rb ^ 1) * 64, 4, tag, lane); gu_x(0); gu_x(1); gu_x(2); gu_fill(3); wait_vm<7>(); }
    BAR();
    f32x16 g[4], u[4];
    if (has_ffn) {
      lptr pp[4];
#pragma unroll
      for (int i = 0; i < 4; ++i) { pp[i] = pa[i] + (WR + 16384 - (i >> 1) * 2048); asm volatile("" : "+v"(pp[i])); g[i] = (f32x16){}; u[i] = (f32x16){}; }
      const lptr pg = lds + WR + h * 2048 + (w4 >> 1) * 1024 + (w4 & 1) * 512 + trn;
      lptr pg3 = pg + 3 * GSLOT;
      asm volatile("" : "+v"(pg3));
      GFrag f0, f1;
      gu_read<0, 0>(pa, pp, pg, pg3, f0, 0); gu_read<0, 0>(pa, pp, pg, pg3, f0, 1); gu_read<0, 0>(pa, pp, pg, pg3, f0, 2);
      gu_stages<0>(pa, pp, pg, pg3, f0, f1, g, u, gu_fill);
    } else {
      unsigned d[5];
      wait_vm<0>();
#pragma unroll 1
      for (int i = 0; i < 11; ++i) BAR();
#pragma unroll
      for (int i = 0; i < 5; ++i) {
        const int t = (rb * 4 + w4) * 144 + i * 32 + (r + i * 32 < 144 ? r : 0);
        const char* p = (t < 14336) ? gwd + (size_t)t * 128 : gwo + (size_t)(t - 14336) * 128;
        asm volatile("global_load_dword %0, %1, off" : "=v"(d[i]) : "v"(p) : "memory");
      }
#pragma unroll 1
      for (int i = 11; i < 15; ++i) BAR();
      PIN_AFTER("s_waitcnt vmcnt(0)", "+v"(d[0]), "+v"(d[1]), "+v"(d[2]), "+v"(d[3]), "+v"(d[4]));
    }
    BAR();
    __builtin_amdgcn_s_setprio(3);
    att_loads(); att_ring();
    auto swiglu = [&](int i) {
#pragma unroll
      for (int j = 0; j < 16; ++j) g[i][j] = g[i][j] * u[i][j] * __builtin_amdgcn_rcpf(1.0f + __builtin_amdgcn_exp2f(g[i][j] * -1.4426950408889634f));
      const unsigned ho = (unsigned)(((rb ^ (i >> 1)) * 2 + (i & 1)) * 112 + fb * 8 + w4 * 2) * 1024 + voff;
      BSTORE_DEV(pack8(g[i], 0), rh, ho);
      BSTORE_DEV(pack8(g[i], 8), rh, ho + 1024);
    };
    if (has_ffn) swiglu(0);
    BAR();
    if (has_ffn) { swiglu(1); swiglu(2); }
    BAR();
    const float ma = mx[0], mb = mx[64];
    if (has_ffn) swiglu(3); else wait_vm<0>();
    PIN_AFTER("s_waitcnt vmcnt(8)", "+v"(QAu), "+v"(QBu), "+v"(Ku), "+v"(V0u));
    att_init(ma, mb);
  }
  asm volatile("" :: "v"(pf), "v"(cpf));
  asm volatile(ATT_BEGIN "s_setprio 0\n\t" SB_
               T_KVI("0", 16640, 16384) T_WD("2080", 0) T_WD("4160", 1) T_WD("6240", 2)
               VB(8)
               "buffer_store_dwordx2 %[tag], %[fho], %[rfl], 0 offen sc1\n\t"
               T_KVI("8320", 24960, 24576) T_WD("10400", 3) T_WD("12480", 4) T_NONE("14560")
               VB(7)
               T_KV("16640", kvm, s5_) T_NONE("18720")
               TILE("20800", "", "buffer_load_dwordx2 %[fl], %[fhi], %[rfl], 0 offen sc1\n\t", "", "", "") T_NONE("22880")
               ATT_END
               : ATT_STATE, [fl] "=&v"(fl)
               : ATT_IN, [rkv] "s"(rkv), [rwd] "s"(rwd), [vw1] "v"(vw1), [kvm] "s"(kvm), [kvs] "s"(kvd),
                 [dm0] "s"(RFL(d_m(0, wid))), [ds0] "s"(RFL(d_so(0, wid))), [s5_] "s"(kve + 4 * 8192), [rfl] "s"(mk_rsrc(ws + WS_FH, 2048)), [tag] "v"(tag),
                 [fho] "v"(threadIdx.x == 0 ? (unsigned)(rb * 8 + cb) * 8 : OOB), [fhi] "v"((unsigned)((rb >> 1) * 16 + (lane & 15)) * 8) : ATT_CLOB);
  PIN_AFTER("s_waitcnt vmcnt(0)", "+v"(fl));
  if (__builtin_expect(!__all(fl == tag), 0)) wait_flags(ws + WS_FH + (rb >> 1) * 128, 16, tag, lane);
#define H_OUT(n, st) [a##n] "=&v"(ha[st]), [b##n] "=&v"(hb[st])
  unsigned hs = h_so(0) + (wid >> 2) * OOB;
  f32x4 p0, p1, p2, p3;
  float pl0, pl1;
  asm volatile(ATT_BEGIN SB_
               TILE("24960", "s_add_u32 m0, %[kvm], 8320\n\ts_add_u32 %[t0], %[kvs], 40960\n\t", "buffer_load_dwordx4 %[voff], %[rkv], %[t0] offen sc1 lds\n\t",
                    "buffer_load_dwordx4 %[voff], %[rkv], %[t0] offen offset:1024 sc1 lds\n\t", HLA(a0), HLB(b0))
               T_H2("27040", a1, b1, a2, b2) T_H2("29120", a3, b3, a4, b4) T_H2("31200", a5, b5, a6, b6)
               "s_cmp_lt_u32 %[wid], 4\n\ts_cbranch_scc1 L_skip_%=\n\t"
               "s_setprio 2\n\t"
               SB_ T_KVI("0", 16640, 49152) T_NONE("2080") SB_ T_NONE("4160") T_NONE("6240")
               VB(2) T_KVI("8320", 24960, 57344)
               TILE("10400", "", "", "", "ds_read_b128 %[p0], %[ex]\n\tds_read_b128 %[p1], %[ex] offset:1024\n\tds_read_b32 %[pl0], %[el]\n\t", "")
               SB_
               TILE("12480", "", "", "", "ds_read_b128 %[p2], %[ex] offset:2048\n\tds_read_b128 %[p3], %[ex] offset:3072\n\tds_read_b32 %[pl1], %[el] offset:256\n\t", "")
               T_NONE("14560")
               VB(2) T_NONE("16640") T_NONE("18720") SB_ T_NONE("20800") T_NONE("22880")
               VB(0) T_NONE("24960") T_NONE("27040") SB_ T_NONE("29120") T_NONE("31200")
               "L_skip_%=:\n\t"
               ATT_END
               : ATT_STATE, H_OUT(0, 0), H_OUT(1, 1), H_OUT(2, 2), H_OUT(3, 3), H_OUT(4, 4), H_OUT(5, 5), H_OUT(6, 6),
                 [p0] "=&v"(p0), [p1] "=&v"(p1), [p2] "=&v"(p2), [p3] "=&v"(p3), [pl0] "=&v"(pl0), [pl1] "=&v"(pl1), [hs] "+s"(hs)
               : ATT_IN, [rkv] "s"(rkv), [kvm] "s"(kvm), [kvs] "s"(kve), [wid] "s"(wid), [ex] "v"(ex), [el] "v"(el),
                 [rh] "s"(rh) : ATT_CLOB);

  const int t = threadIdx.x;
  const unsigned off = (unsigned)(rb * 64 + (t >> 3)) * 1024 + co * 128 + (t & 7) * 16;
  f32x16 d0, d1;
  F4 e0, e1;
#define D_PTR(pd, a) ((a) < 65536 ? pd[0] + (a) : (a) < KVA ? pd[1] + ((a) - 65536) : pd[2] + ((a) - KVA))
#define D_READ_A(n, w_) do { n.w0 = lds_frag_tr(w_, w_ + 1024); n.w1 = lds_frag_tr(w_ + 512, w_ + 1536); } while (0)
#define D_READ_B(n, w_) do { n.x0 = lds_frag_tr(w_ + 2048, w_ + 3072); n.x1 = lds_frag_tr(w_ + 2560, w_ + 3584); } while (0)
#define D_STEPX(c, n, nxt, A, B, RD, X, Y) do { \
    MM(d0, c.w0, __builtin_bit_cast(bf16x8, A)); if (RD) D_READ_A(n, (nxt)); MM(d1, c.w1, __builtin_bit_cast(bf16x8, A)); if (RD) D_READ_B(n, (nxt)); \
    MM(d0, c.x0, __builtin_bit_cast(bf16x8, B)); X; MM(d1, c.x1, __builtin_bit_cast(bf16x8, B)); Y; } while (0)
#define H_LOAD(e, so) do { BLOAD_DEV(ha[e], voff, rh, so); BLOAD_DEV(hb[e], voff, rh, (so) + 1024); } while (0)
  PIN_AFTER("s_waitcnt vmcnt(0)", "+v"(ha[0]), "+v"(ha[1]), "+v"(ha[2]), "+v"(ha[3]), "+v"(ha[4]), "+v"(ha[5]), "+v"(ha[6]),
            "+v"(hb[0]), "+v"(hb[1]), "+v"(hb[2]), "+v"(hb[3]), "+v"(hb[4]), "+v"(hb[5]), "+v"(hb[6]));
  OB = MFMA(V1, P1, OB);
  lptr pe[3];
  pe[0] = lds + (kq & 1) * 4096 + h * 256 + trn; pe[1] = pe[0] + 65536; pe[2] = pe[0] + KVA;
  asm volatile("" : "+v"(pe[0]), "+v"(pe[1]), "+v"(pe[2]));
#define EP(e) D_PTR(pe, DADDR((e) >> 1) + ((e) & 1) * 8192)
#define RADDR(q) ((q) == 0 ? 49152 : (q) == 1 ? 65536 : (q) == 2 ? KVA : 0)
  auto put_partial = [&](int base) {
    const lptr red = lds + base + (rt * 32 + r) * 256;
#pragma unroll
    for (int i = 0; i < 4; ++i) {
      *(LAS f32x4*)(red + (((2 * i + h) ^ (r & 15)) * 16)) = V4(d0, i);
      *(LAS f32x4*)(red + (((8 + 2 * i + h) ^ (r & 15)) * 16)) = V4(d1, i);
    }
  };
  if (wid < 4) {
    d0 = d1 = (f32x16){};
#define E_DMA(st, i) issue_dp(st, wid * 2 + (i))
#define E_STAGE(k, X0, Y0, X1, Y1) do { D_STEPX(e0, e1, EP(2 * (k) + 1), ha[(2 * (k)) % 14], hb[(2 * (k)) % 14], true, X0, Y0); \
      D_STEPX(e1, e0, EP(2 * (k) + 2), ha[(2 * (k) + 1) % 14], hb[(2 * (k) + 1) % 14], (k) < 13, X1, Y1); } while (0)
#define HE(e) H_LOAD((e) % 14, h_so(e))
#define HP(e) "+v"(ha[(e) % 14]), "+v"(hb[(e) % 14])
#define W2(k, sa, sb, e) do { E_STAGE(k, E_DMA(sa, 0), E_DMA(sa, 1), E_DMA(sb, 0), E_DMA(sb, 1)); HE(e); } while (0)
#define W1(k, sa, e) do { E_STAGE(k, E_DMA(sa, 0), E_DMA(sa, 1), HE(e), HE((e) + 1)); HE((e) + 2); } while (0)
#define FB(...) PIN_AFTER("s_waitcnt vmcnt(10) lgkmcnt(0)\n\ts_barrier", __VA_ARGS__)
    D_READ_A(e0, EP(0)); D_READ_B(e0, EP(0));
    E_STAGE(0, HE(7), HE(8), HE(9), ); E_STAGE(1, HE(10), , HE(11), );
    BAR();
    ex[0] = V4(OA, 0); ex[64] = V4(OA, 1); ex[128] = V4(OB, 0); ex[192] = V4(OB, 1); el[0] = OA[8]; el[64] = OB[8];
    W2(2, 5, 6, 12);   FB(HP(7), HP(8), HP(9), HP(10), HP(11));
    W2(3, 7, 8, 13);   FB(HP(12));
    W1(4, 9, 14);      FB(HP(13));
    W2(5, 10, 14, 17); FB(HP(14), HP(15), HP(16));
    W2(6, 11, 15, 18); FB(HP(17));
    W1(7, 12, 19);     FB(HP(18));
    W1(8, 13, 22);
    PIN_AFTER("s_waitcnt vmcnt(6) lgkmcnt(0)\n\ts_barrier", HP(19), HP(20), HP(21));
    E_STAGE(9, HE(25), HE(26), HE(27), E_DMA(16, 0));
    E_STAGE(10, E_DMA(16, 1), E_DMA(17, 0), E_DMA(17, 1), BLOAD(xv, off, rx, 0u));
    PIN_AFTER("s_waitcnt vmcnt(9)", HP(22), HP(23), HP(24), HP(25), HP(26), HP(27));
    E_STAGE(11, , , , ); E_STAGE(12, , , , ); E_STAGE(13, , , , );
    if (b == 193 && wid == 0) {
#pragma unroll 1
      for (int i = 0; i < 4; ++i) wait_flags(ws + WS_FH + i * 512, 64, tag, lane);
      if (lane == 0) set_flag(ws + WS_EPOCH, tag);
    }
    PIN_AFTER("s_waitcnt vmcnt(0) lgkmcnt(0)\n\ts_barrier", "+v"(xv));
    put_partial(kq ? RADDR(1) : RADDR(0));
  } else {
    auto store_o = [&](const f32x16& O, int i) {
      float il = __builtin_amdgcn_rcpf(O[8]);
      asm volatile("s_nop 0" : "+v"(il));
      const u32x4 v = {pk2(mul1(O[0], il), mul1(O[1], il)), pk2(mul1(O[2], il), mul1(O[3], il)), pk2(mul1(O[4], il), mul1(O[5], il)), pk2(mul1(O[6], il), mul1(O[7], il))};
      BSTORE_DEV(v, ro, (unsigned)((qt0 + i) * 32 + head) * 1024 + voff);
    };
    auto check_o = [&](f32x16 O, const bf16x8& Q, int i) {
      float mag = __builtin_fabsf(O[0]);
#pragma unroll
      for (int j = 1; j < 8; ++j) mag += __builtin_fabsf(O[j]);
      const bool bad = (int)!(O[8] < 0x1p120f) | (int)!(mag < 0x1p126f) | (int)!(xhalf_max(mag) > 0x1p-100f);
      if (__builtin_expect(__any(bad), 0)) {
        const f32x16 z = {};
        float mt = -__builtin_inff();
#pragma unroll 1
        for (int tt = 0; tt < 64; ++tt) {
          const f32x16 s = MFMA(__builtin_bit_cast(bf16x8, __builtin_amdgcn_raw_buffer_load_b128(rkv, voff, tt * 2048, 16)), Q, z);
#pragma unroll
          for (int j = 0; j < 16; ++j) mt = __builtin_fmaxf(mt, s[j]);
        }
        mt = xhalf_max(mt);
        O = z;
#pragma unroll 1
        for (int tt = 0; tt < 64; ++tt) {
          f32x16 s = MFMA(__builtin_bit_cast(bf16x8, __builtin_amdgcn_raw_buffer_load_b128(rkv, voff, ((tt + 63) & 63) * 2048, 16)), Q, z);
          bf16x8 v0 = __builtin_bit_cast(bf16x8, __builtin_amdgcn_raw_buffer_load_b128(rkv, vvo, ((tt + 63) & 63) * 2048 + 1024, 16));
          bf16x8 v1 = __builtin_bit_cast(bf16x8, __builtin_amdgcn_raw_buffer_load_b128(rkv, vvo, tt * 2048 + 1536, 16));
          if (r >= 16) { v0 = PAD; v1 = PAD; }
#pragma unroll
          for (int j = 0; j < 16; ++j) s[j] = __builtin_amdgcn_exp2f(s[j] - mt);
          O = MFMA(v0, __builtin_bit_cast(bf16x8, pack8(s, 0)), O);
          O = MFMA(v1, __builtin_bit_cast(bf16x8, pack8(s, 8)), O);
        }
        asm volatile("s_nop 15" : "+v"(O));
        wait_vm<0>();
        store_o(O, i);
      }
    };
#pragma unroll
    for (int j = 0; j < 4; ++j) { OA[j] = add1(OA[j], p0[j]); OA[4 + j] = add1(OA[4 + j], p1[j]); }
    OA[8] += pl0;
    store_o(OA, 0);
    asm volatile("s_nop 3" : "+v"(OB) :: "memory");
#pragma unroll
    for (int j = 0; j < 4; ++j) { OB[j] = add1(OB[j], p2[j]); OB[4 + j] = add1(OB[4 + j], p3[j]); }
    OB[8] += pl1;
    store_o(OB, 1);
    check_o(OA, QA, 0); check_o(OB, QB, 1);
    wait_vm<0>();
    __builtin_amdgcn_raw_buffer_store_b64(__builtin_bit_cast(u32x2, tag), mk_rsrc(ws + WS_FO, 16384),
                                          lane < 2 ? (unsigned)((qt0 + lane) * 32 + head) * 8 : OOB, 0, 16);
    BLOAD(xv, off, rx, 0u);
    D_READ_A(e0, EP(28)); D_READ_B(e0, EP(28));
    d0 = d1 = (f32x16){};
    if (wid == 4) wait_flags(ws + WS_FO + rb * 512, 64, tag, lane);
    PIN_AFTER("s_waitcnt vmcnt(0) lgkmcnt(0)\n\ts_barrier", "+v"(xv));
    u32x4 oa[8], ob[8];
    const unsigned obase = (unsigned)((rb * 2 + rt) * 32 + (kq & 1) * 2) * 1024;
#pragma unroll
    for (int j = 0; j < 8; ++j) { BLOAD_DEV(oa[j], voff, ro, obase + j * 4096); BLOAD_DEV(ob[j], voff, ro, obase + j * 4096 + 1024); }
#define O_STEP(c, n, j) do { wait_vm2<14 - 2 * (j)>(oa[j], ob[j]); D_STEPX(c, n, EP(29 + (j)), oa[j], ob[j], (j) < 7, , ); } while (0)
    O_STEP(e0, e1, 0); O_STEP(e1, e0, 1); O_STEP(e0, e1, 2); O_STEP(e1, e0, 3); O_STEP(e0, e1, 4); O_STEP(e1, e0, 5); O_STEP(e0, e1, 6); O_STEP(e1, e0, 7);
    put_partial((kq & 1) ? RADDR(3) : RADDR(2));
  }
  BAR();
  f32x4 s0 = {bf_lo(xv[0]), bf_hi(xv[0]), bf_lo(xv[1]), bf_hi(xv[1])}, s1 = {bf_lo(xv[2]), bf_hi(xv[2]), bf_lo(xv[3]), bf_hi(xv[3])};
  {
    const int row = t >> 3, c = (t & 7) * 2;
    const lptr rd0 = lds + row * 256 + ((c ^ (row & 15)) * 16), rd1 = lds + row * 256 + (((c + 1) ^ (row & 15)) * 16);
#pragma unroll
    for (int p = 0; p < 4; ++p) {
      const int o = RADDR(p);
      s0 += *(const LAS f32x4*)(rd0 + o); s1 += *(const LAS f32x4*)(rd1 + o);
    }
  }
  __builtin_amdgcn_raw_buffer_store_b128((u32x4){pk2(s0[0], s0[1]), pk2(s0[2], s0[3]), pk2(s1[0], s1[1]), pk2(s1[2], s1[3])}, mk_rsrc(out, S_ * D_ * 2), off, 0, 0);
  asm volatile("Lk_tail:");
}

extern "C" void kernel_launch(void* const* d_in, const int* in_sizes, int n_in, void* d_out, int out_size, void* d_ws,
                              size_t ws_size, hipStream_t stream) {
  (void)hipFuncSetAttribute((const void*)k_all, hipFuncAttributeMaxDynamicSharedMemorySize, LDS_BYTES);
  k_all<<<256, 512, LDS_BYTES, stream>>>((const char*)d_in[0], (const char*)d_in[1], (const char*)d_in[2], (const char*)d_in[4],
                                         (const char*)d_in[5], (const char*)d_in[6], (char*)d_ws, (char*)d_out);
}
```

```cpp
#include <hip/hip_runtime.h>
#include <hip/hip_bf16.h>
#include <stdint.h>

typedef __attribute__((ext_vector_type(8))) short bf16x8;
typedef __attribute__((ext_vector_type(4))) short s16x4;
typedef __attribute__((ext_vector_type(16))) float f32x16;
typedef __attribute__((ext_vector_type(4))) float f32x4;
typedef __attribute__((ext_vector_type(2))) float f32x2;
typedef __attribute__((ext_vector_type(4))) unsigned u32x4;
typedef __attribute__((ext_vector_type(2))) unsigned u32x2;
typedef __attribute__((ext_vector_type(2))) __bf16 bf2;
typedef unsigned long long u64;
typedef __amdgpu_buffer_rsrc_t rsrc_t;
#define LAS __attribute__((address_space(3)))
#define GAS __attribute__((address_space(1)))
typedef LAS char* lptr;

#define MFMA(a, b, c) __builtin_amdgcn_mfma_f32_32x32x16_bf16((a), (b), (c), 0, 0, 0)
#define MM(c, a, b) do { __builtin_amdgcn_sched_barrier(0); c = MFMA(a, b, c); asm volatile("" : "+v"(c) :: "memory"); \
                         __builtin_amdgcn_sched_barrier(0); } while (0)
#define RFL(x) __builtin_amdgcn_readfirstlane(x)

constexpr int S_ = 2048, D_ = 512, F_ = 1792;
constexpr size_t WS_EPOCH = 0;
constexpr size_t WS_FH = 2048;
constexpr size_t WS_FQ = 16384;
constexpr size_t WS_FX = 49152;
constexpr size_t WS_FO = 32768;
constexpr size_t WS_Q    = 65536;
constexpr size_t WS_KV   = WS_Q + 2097152;
constexpr size_t WS_O    = WS_KV + 4194304;
constexpr size_t WS_XN   = WS_O + 2097152;
constexpr size_t WS_H    = WS_XN + 2097152;
constexpr float QSCALE = 0.25f * 1.4426950408889634f;
constexpr float MBIAS = 80.0f;
constexpr int LDS_BYTES = 163840;
constexpr unsigned SPIN_CAP = 1u << 22;
constexpr unsigned OOB = 0x7ff00000u;

__device__ __forceinline__ rsrc_t mk_rsrc(const void* p, unsigned bytes) {
  return __builtin_amdgcn_make_buffer_rsrc((void*)p, (short)0, (int)bytes, 0x00020000);
}
__device__ __forceinline__ void dma16(rsrc_t r, unsigned voff, unsigned soff, unsigned lds) {
  unsigned keep;
  asm volatile("s_mov_b32 %0, m0\n\ts_mov_b32 m0, %1\n\ts_nop 0\n\tbuffer_load_dwordx4 %2, %3, %4 offen lds\n\ts_mov_b32 m0, %0"
               : "=&s"(keep) : "s"(lds), "v"(voff), "s"(r), "s"(soff) : "memory");
}
__device__ __forceinline__ void dma16_dev(rsrc_t r, unsigned voff, unsigned soff, unsigned lds) {
  unsigned keep;
  asm volatile("s_mov_b32 %0, m0\n\ts_mov_b32 m0, %1\n\ts_nop 0\n\tbuffer_load_dwordx4 %2, %3, %4 offen sc1 lds\n\ts_mov_b32 m0, %0"
               : "=&s"(keep) : "s"(lds), "v"(voff), "s"(r), "s"(soff) : "memory");
}
__device__ __forceinline__ void dma16x3(rsrc_t r, unsigned voff, unsigned soff, unsigned lds) {
  unsigned keep;
  asm volatile("s_mov_b32 %0, m0\n\ts_mov_b32 m0, %1\n\ts_nop 0\n\tbuffer_load_dwordx4 %2, %3, %4 offen lds\n\t"
               "buffer_load_dwordx4 %2, %3, %4 offen offset:1024 lds\n\tbuffer_load_dwordx4 %2, %3, %4 offen offset:2048 lds\n\ts_mov_b32 m0, %0"
               : "=&s"(keep) : "s"(lds), "v"(voff), "s"(r), "s"(soff) : "memory");
}
__device__ __forceinline__ float fma1(float a, float b, float c) {
  float d; asm("v_fma_f32 %0, %1, %2, %3" : "=v"(d) : "v"(a), "v"(b), "v"(c)); return d;
}
__device__ __forceinline__ float add1(float a, float b) { float d; asm("v_add_f32 %0, %1, %2" : "=v"(d) : "v"(a), "v"(b)); return d; }
__device__ __forceinline__ float mul1(float a, float b) { float d; asm("v_mul_f32 %0, %1, %2" : "=v"(d) : "v"(a), "v"(b)); return d; }
#define BLOAD(dst, voff, r, soff) asm volatile("buffer_load_dwordx4 %0, %1, %2, %3 offen" : "=v"(dst) : "v"(voff), "s"(r), "s"(soff) : "memory")
#define BLOAD_DEV(dst, voff, r, soff) asm volatile("buffer_load_dwordx4 %0, %1, %2, %3 offen sc1" : "=v"(dst) : "v"(voff), "s"(r), "s"(soff) : "memory")
#define BSTORE_DEV(val, r, voff) __builtin_amdgcn_raw_buffer_store_b128((val), (r), (voff), 0, 16)
#define WAIT_BAR(N) asm volatile("s_waitcnt vmcnt(" #N ") lgkmcnt(0)\n\ts_barrier" ::: "memory")
#define BAR() asm volatile("s_waitcnt lgkmcnt(0)\n\ts_barrier" ::: "memory")
template <int N> __device__ __forceinline__ void wait_bar() { asm volatile("s_waitcnt vmcnt(%0) lgkmcnt(0)\n\ts_barrier" :: "n"(N) : "memory"); }
template <int N> __device__ __forceinline__ void wait_vm2(u32x4& a, u32x4& b) {
  asm volatile("s_waitcnt vmcnt(%0)" :: "n"(N) : "memory"); __builtin_amdgcn_sched_barrier(0); asm volatile("" : "+v"(a), "+v"(b) :: "memory");
}
template <int N> __device__ __forceinline__ void wait_vm1(u32x4& a) {
  asm volatile("s_waitcnt vmcnt(%0)" :: "n"(N) : "memory"); __builtin_amdgcn_sched_barrier(0); asm volatile("" : "+v"(a) :: "memory");
}

__device__ __forceinline__ unsigned pk2(float lo, float hi) {
  f32x2 v = {lo, hi};
  return __builtin_bit_cast(unsigned, __builtin_convertvector(v, bf2));
}
__device__ __forceinline__ float bf_lo(unsigned u) { return __uint_as_float(u << 16); }
__device__ __forceinline__ float bf_hi(unsigned u) { return __uint_as_float(u & 0xffff0000u); }
__device__ __forceinline__ float xhalf_max(float v) {
  auto r = __builtin_amdgcn_permlane32_swap(__float_as_uint(v), __float_as_uint(v), false, false);
  return __builtin_fmaxf(__uint_as_float(r[0]), __uint_as_float(r[1]));
}
__device__ __forceinline__ bf16x8 lds_frag(lptr p) { return *(const LAS bf16x8*)p; }
typedef short v4i16_t __attribute__((ext_vector_type(4)));
__device__ __forceinline__ bf16x8 lds_frag_tr(lptr p0, lptr p1) {
  s16x4 a = __builtin_bit_cast(s16x4, __builtin_amdgcn_ds_read_tr16_b64_v4i16((LAS v4i16_t*)p0));
  s16x4 b = __builtin_bit_cast(s16x4, __builtin_amdgcn_ds_read_tr16_b64_v4i16((LAS v4i16_t*)p1));
  return (bf16x8){a[0], a[1], a[2], a[3], b[0], b[1], b[2], b[3]};
}
__device__ __forceinline__ float dot2(unsigned a, unsigned b, float c) {
  return __builtin_amdgcn_fdot2_f32_bf16(__builtin_bit_cast(bf2, a), __builtin_bit_cast(bf2, b), c, false);
}
__device__ __forceinline__ u32x4 pack8(const f32x16& a, int o) {
  return (u32x4){pk2(a[o], a[o + 1]), pk2(a[o + 2], a[o + 3]), pk2(a[o + 4], a[o + 5]), pk2(a[o + 6], a[o + 7])};
}
template <int CTRL> __device__ __forceinline__ float dpp_add(float x) {
  return x + __builtin_bit_cast(float, __builtin_amdgcn_mov_dpp(__builtin_bit_cast(int, x), CTRL, 0xf, 0xf, true));
}
__device__ __forceinline__ float sum8(float x) { return dpp_add<0x141>(dpp_add<0x4E>(dpp_add<0xB1>(x))); }

__device__ __forceinline__ u64 read_tag(const char* ws) {
  u64 e;
  asm volatile("s_load_dwordx2 %0, %1, 0x0\n\ts_waitcnt lgkmcnt(0)" : "=s"(e) : "s"(ws + WS_EPOCH) : "memory");
  e = (e ^ (e >> 31)) * 0x9E3779B97F4A7C15ull + 0x632BE59BD9B4E019ull;
  return e ^ (e >> 29);
}
__device__ __forceinline__ void set_flag(char* p, u64 tag) { __hip_atomic_store((GAS u64*)p, tag, __ATOMIC_RELAXED, __HIP_MEMORY_SCOPE_AGENT); }
__device__ __forceinline__ void wait_flags(const char* p, int n, u64 tag, int lane) {
  GAS u64* f = (GAS u64*)p + (lane < n ? lane : 0);
#pragma unroll 1
  for (unsigned spins = 0; spins < SPIN_CAP; ++spins) {
    if (__all(__hip_atomic_load(f, __ATOMIC_RELAXED, __HIP_MEMORY_SCOPE_AGENT) == tag)) break;
    __builtin_amdgcn_s_sleep(4);
  }
  asm volatile("" ::: "memory");
}


__device__ __forceinline__ void dma16x2(rsrc_t r, unsigned voff, unsigned soff0, unsigned soff1, unsigned lds) {
  unsigned keep;
  asm volatile("s_mov_b32 %0, m0\n\ts_mov_b32 m0, %1\n\ts_nop 0\n\tbuffer_load_dwordx4 %2, %3, %4 offen lds\n\t"
               "buffer_load_dwordx4 %2, %3, %5 offen offset:1024 lds\n\ts_mov_b32 m0, %0"
               : "=&s"(keep) : "s"(lds), "v"(voff), "s"(r), "s"(soff0), "s"(soff1 - 1024u) : "memory");
}
template <int N> __device__ __forceinline__ void wait_vm() { asm volatile("s_waitcnt vmcnt(%0)" :: "n"(N) : "memory"); }
#define PIN_AFTER(W, ...) do { asm volatile(W ::: "memory"); __builtin_amdgcn_sched_barrier(0); asm volatile("" : __VA_ARGS__ :: "memory"); } while (0)
#define V4(c, g_) (f32x4){c[4 * (g_)], c[4 * (g_) + 1], c[4 * (g_) + 2], c[4 * (g_) + 3]}

constexpr int WR = 65536, QSLOT = 24576;
constexpr int GSLOT = 20480;
constexpr int REC = 2080, CHUNK = 4 * REC;
constexpr int KVA = WR + 3 * GSLOT, KVB = KVA - 4 * CHUNK;

struct F4 { bf16x8 x0, x1, w0, w1; };
struct QFrag { bf16x8 x, q, k, v; };

template <int G>
__device__ __forceinline__ void qkv_steps(const lptr (&pa)[4], lptr pw, lptr pw2, QFrag& c, QFrag& n, f32x16& aq, f32x16& ak, f32x16& av,
                                          char* fx, u64 tag, int lane, bool shares) {
  if constexpr (G < 32) {
    constexpr int s = G >> 2, N = G + 1, ns = N >> 2;
    if constexpr ((G & 3) == 3) {
      if constexpr (s == 0) { if (!shares) wait_vm<7>(); }
      if constexpr (s == 0) wait_bar<9>(); else if constexpr (s == 1) wait_bar<4>(); else if constexpr (s == 2) wait_bar<1>(); else BAR();
      if constexpr (s == 1) { if (lane == 0) set_flag(fx, tag); }
    }
    const lptr w = ((ns & 2) ? pw2 : pw) + (ns & 1) * QSLOT + (N & 3) * 6144;
    MM(aq, c.q, c.x);
    if constexpr (N < 32) { n.x = lds_frag(pa[N & 3] + ns * 4096); n.q = lds_frag_tr(w, w + 256); }
    MM(ak, c.k, c.x);
    if constexpr (N < 32) { n.k = lds_frag_tr(w + 1024, w + 1024 + 256); n.v = lds_frag_tr(w + 2048, w + 2048 + 256); }
    MM(av, c.x, c.v);
    qkv_steps<G + 1>(pa, pw, pw2, n, c, aq, ak, av, fx, tag, lane, shares);
  }
}

#define EXP(x) "v_exp_f32 v" #x ", v" #x "\n\t"
#define CVT(d, a, b) "v_cvt_pk_bf16_f32 v" #d ", v" #a ", v" #b "\n\t"
#define MF(d, a, b, c) "v_mfma_f32_32x32x16_bf16 " d ", " a ", " b ", " c "\n\t"
#define HALF(c0, c1, c2, c3, c4, c5, c6, c7, c8, c9, c10, c11, c12, c13, c14, c15, QK, PV1, PV0, X0, X1) \
  QK EXP(c0) EXP(c1) EXP(c2) EXP(c3) PV1 X0 \
  EXP(c4) EXP(c5) CVT(80, c0, c1) CVT(81, c2, c3) EXP(c6) EXP(c7) EXP(c8) EXP(c9) CVT(82, c4, c5) CVT(83, c6, c7) \
  EXP(c10) EXP(c11) PV0 X1 \
  EXP(c12) EXP(c13) CVT(84, c8, c9) CVT(85, c10, c11) EXP(c14) EXP(c15) CVT(86, c12, c13) CVT(87, c14, c15)
#define TILE(off, S, X0, X1, X2, X3) \
  S \
  HALF(16, 17, 18, 19, 20, 21, 22, 23, 24, 25, 26, 27, 28, 29, 30, 31, \
       MF("v[32:47]", "v[68:71]", "v[92:95]", "v[96:111]") "ds_read_b128 v[68:71], v88 offset:" off "\n\t", \
       MF("v[112:127]", "v[76:79]", "v[84:87]", "v[112:127]") "ds_read_b128 v[76:79], v90 offset:" off "\n\t", \
       "s_waitcnt lgkmcnt(2)\n\t" MF("v[48:63]", "v[72:75]", "v[80:83]", "v[48:63]"), X0, X1) \
  HALF(32, 33, 34, 35, 36, 37, 38, 39, 40, 41, 42, 43, 44, 45, 46, 47, \
       "s_waitcnt lgkmcnt(0)\n\t" MF("v[16:31]", "v[68:71]", "v[64:67]", "v[0:15]"), \
       MF("v[48:63]", "v[76:79]", "v[84:87]", "v[48:63]"), \
       MF("v[112:127]", "v[72:75]", "v[80:83]", "v[112:127]") "ds_read_b128 v[72:75], v89 offset:" off "\n\t", X2, X3)
#define T_NONE(off) TILE(off, "", "", "", "", "")
#define T_KV(off, m, so) TILE(off, "s_mov_b32 m0, %[" #m "]\n\t", "", "buffer_load_dwordx4 %[voff], %[rkv], %[" #so "] offen sc1 lds\n\t", \
    "buffer_load_dwordx4 %[voff], %[rkv], %[" #so "] offen offset:1024 sc1 lds\n\t", "")
#define T_WD(off, st) TILE(off, "s_add_u32 m0, %[dm0], 16384*" #st "\n\ts_add_u32 %[t0], %[ds0], 131072*" #st "\n\t", "", \
    "buffer_load_dwordx4 %[vw1], %[rwd], %[t0] offen lds\n\ts_add_u32 %[t0], %[t0], 7168\n\t", "buffer_load_dwordx4 %[vw1], %[rwd], %[t0] offen offset:1024 lds\n\t", "")
#define T_KVI(off, MO, SO) TILE(off, "s_add_u32 m0, %[kvm], " #MO "\n\ts_add_u32 %[t0], %[kvs], " #SO "\n\t", "", \
    "buffer_load_dwordx4 %[voff], %[rkv], %[t0] offen sc1 lds\n\t", "buffer_load_dwordx4 %[voff], %[rkv], %[t0] offen offset:1024 sc1 lds\n\t", "")
#define VB(n) "s_waitcnt vmcnt(" #n ")\n\ts_barrier\n\t"
#define SB_ "s_barrier\n\t"
#define HLA(d) "buffer_load_dwordx4 %[" #d "], %[voff], %[rh], %[hs] offen sc1\n\t"
#define HLB(d) "buffer_load_dwordx4 %[" #d "], %[voff], %[rh], %[hs] offen offset:1024 sc1\n\ts_add_u32 %[hs], %[hs], 4096\n\t"
#define T_H2(off, da, db, dc, dd) TILE(off, "", HLA(da), HLB(db), HLA(dc), HLB(dd))
#define ATT_BEGIN "s_mov_b32 %[keep], m0\n\ts_waitcnt lgkmcnt(0)\n\ts_nop 2\n\t"
#define ATT_END "s_mov_b32 m0, %[keep]\n\ts_waitcnt lgkmcnt(0)\n\ts_nop 3\n\t"
#define ATT_STATE "+{v[48:63]}"(OA), "+{v[112:127]}"(OB), "+{v[16:31]}"(SA), "+{v[68:71]}"(K), "+{v[72:75]}"(V0), "+{v[76:79]}"(V1), \
                  "+{v[84:87]}"(P1), [t0] "=&s"(t0), [keep] "=&s"(keep)
#define ATT_CONST "{v[0:15]}"(CA), "{v[96:111]}"(CB), "{v[64:67]}"(QA), "{v[92:95]}"(QB), [voff] "v"(voff)
#define ATT_IN ATT_CONST, "{v88}"(aK), "{v89}"(aV0), "{v90}"(aV1)
#define ATT_CLOB "v32", "v33", "v34", "v35", "v36", "v37", "v38", "v39", "v40", "v41", "v42", "v43", "v44", "v45", "v46", "v47", \
                 "v80", "v81", "v82", "v83", "scc", "memory"

struct GFrag { bf16x8 x[4], g, u; };
__host__ __device__ constexpr int gslot(int s) { return s < 8 ? (s & 3) : (s - 8) % 3; }
template <int S, int KG>
__device__ __forceinline__ void gu_read(const lptr (&pa)[4], const lptr (&pp)[4], lptr pg, lptr pg3, GFrag& n, int part) {
  const lptr x = pa[(S & 1) * 2 + KG] + (S >> 1) * 4096, y = pp[(S & 1) * 2 + KG] + gslot(S) * GSLOT, w = (gslot(S) == 3 ? pg3 : pg + gslot(S) * GSLOT) + KG * 4096;
  if (part == 0) { n.x[0] = lds_frag(x); n.g = lds_frag_tr(w, w + 256); }
  if (part == 1) { n.x[1] = lds_frag(x + 32768); n.u = lds_frag_tr(w + 8192, w + 8192 + 256); }
  if (part == 2) { n.x[2] = lds_frag(y); n.x[3] = lds_frag(y + 2048); }
}
template <int S, class FILL>
__device__ __forceinline__ void gu_stages(const lptr (&pa)[4], const lptr (&pp)[4], lptr pg, lptr pg3, GFrag& f0, GFrag& f1, f32x16 (&g)[4], f32x16 (&u)[4], FILL fill) {
  if constexpr (S < 16) {
#define GU8(c, R0, R1, R2, D) do { MM(g[0], c.g, c.x[0]); R0; MM(g[1], c.g, c.x[1]); R1; MM(u[0], c.u, c.x[0]); R2; MM(u[1], c.u, c.x[1]); \
      MM(g[2], c.g, c.x[2]); D; MM(u[2], c.u, c.x[2]); MM(g[3], c.g, c.x[3]); MM(u[3], c.u, c.x[3]); } while (0)
    GU8(f0, (gu_read<S, 1>(pa, pp, pg, pg3, f1, 0)), (gu_read<S, 1>(pa, pp, pg, pg3, f1, 1)), (gu_read<S, 1>(pa, pp, pg, pg3, f1, 2)), );
    if constexpr (S < 15) {
      if constexpr (S == 0) wait_bar<6>(); else if constexpr (S == 2 || S == 14) wait_bar<0>(); else wait_bar<5>();
      GU8(f1, (gu_read<S + 1, 0>(pa, pp, pg, pg3, f0, 0)), (gu_read<S + 1, 0>(pa, pp, pg, pg3, f0, 1)), (gu_read<S + 1, 0>(pa, pp, pg, pg3, f0, 2)),
          if (S >= 8 && S <= 12) fill(S + 3));
    } else {
      GU8(f1, , , , );
    }
    gu_stages<S + 1>(pa, pp, pg, pg3, f0, f1, g, u, fill);
  }
}

__global__ __launch_bounds__(512, 2) void k_all(const char* __restrict__ gx, const char* __restrict__ gwqkv, const char* __restrict__ gwo,
                                                const char* __restrict__ gwg, const char* __restrict__ gwu, const char* __restrict__ gwd,
                                                char* __restrict__ ws, char* __restrict__ out) {
  extern __shared__ __attribute__((aligned(16))) char smem[];
  const lptr lds = (lptr)smem;
  const unsigned lds0 = (unsigned)(uintptr_t)lds;
  const int b = blockIdx.x;
  unsigned cpf;
  {
    u64 pc; unsigned o = ((b >> 3) * 16 + (threadIdx.x & 15)) * 128;
    asm volatile("s_getpc_b64 %1\nLk_here:\n\tv_min_u32 %2, Lk_tail-Lk_here-4, %2\n\tglobal_load_dword %0, %2, %1" : "=v"(cpf), "=&s"(pc), "+v"(o) :: "memory");
  }
  __builtin_amdgcn_sched_barrier(0);
  const int lane = threadIdx.x & 63, wid = RFL(threadIdx.x >> 6);
  const int xcd = b & 7, bi = b >> 3;
  const int rb = (xcd >> 1) * 8 + (bi & 7);
  const int rot = (bi + 2 * (bi >> 3)) & 7;
  const rsrc_t rx = mk_rsrc(gx, S_ * D_ * 2);
  u32x4 xr[8];
  {
    const unsigned vx = (threadIdx.x >> 3) * 1024 + (threadIdx.x & 7) * 16, sx = (unsigned)rb * 65536 + rot * 128;
#pragma unroll
    for (int i = 0; i < 8; ++i) BLOAD(xr[i], vx, rx, sx ^ (i * 128));
  }
  __builtin_amdgcn_sched_barrier(0);
  const int cb = (xcd & 1) * 4 + (bi >> 3);
  const int cq = (cb + 4 - (int)((uintptr_t)gwqkv >> 7)) & 7, co = (cb + 4 - (int)((uintptr_t)gwd >> 7)) & 7;
  const int head = xcd * 4 + (bi >> 3);
  const int r = lane & 31, h = lane >> 5;
  const unsigned trn = ((lane & 15) >> 2) * 64 + ((lane >> 4) & 1) * 32 + (lane & 3) * 8;
  const unsigned voff = lane * 16;

  const rsrc_t rw = mk_rsrc(gwqkv, D_ * 3 * D_ * 2), rxn = mk_rsrc(ws + WS_XN, 2097152);
  BAR();
  const unsigned vw3 = ((lane >> 2) & 7) * 3072 + (lane >> 5) * 64 + (lane & 3) * 16;
  auto issue_wk = [&](int s, int kb) {
    const unsigned so = (unsigned)(kb * 8) * 3072 + (s ^ rot) * (64 * 3072);
    dma16x3(rw, vw3, so + cq * 128, lds0 + WR + (s & 3) * QSLOT + kb * 3072);
  };
  auto issue_w = [&](int s) { issue_wk(s, wid); };
  issue_w(0); issue_w(1);
  unsigned pf;
#define TOUCH() asm volatile("global_load_dword %0, %1, off" : "=v"(pf) : "v"((h ? gwu : gwg) + (size_t)((b * 8 + wid) * 7 + (r < 7 ? r : 0)) * 128) : "memory")
  const u64 tag = read_tag(ws);
  float rs, mr;
  {
    const unsigned ONE2 = 0x3f803f80u;
    float s0 = 0.f, s1 = 0.f, q0 = 0.f, q1 = 0.f;
#define STAT(i) do { wait_vm1<13 - (i)>(xr[i]); \
      s0 = dot2(xr[i][0], ONE2, s0); q0 = dot2(xr[i][0], xr[i][0], q0); s1 = dot2(xr[i][1], ONE2, s1); q1 = dot2(xr[i][1], xr[i][1], q1); \
      s0 = dot2(xr[i][2], ONE2, s0); q0 = dot2(xr[i][2], xr[i][2], q0); s1 = dot2(xr[i][3], ONE2, s1); q1 = dot2(xr[i][3], xr[i][3], q1); \
      asm volatile("" : "+v"(s0), "+v"(s1), "+v"(q0), "+v"(q1)); } while (0)
    STAT(0); STAT(1); STAT(2); STAT(3); STAT(4); STAT(5); STAT(6); STAT(7);
    const float mean = sum8(s0 + s1) * (1.0f / 512.0f);
    rs = __builtin_amdgcn_rsqf(__builtin_fmaxf(sum8(q0 + q1) * (1.0f / 512.0f) - mean * mean, 0.0f) + 1e-5f);
    mr = -mean * rs;
  }
  const unsigned xo = (lane & 7) * 512 + ((((wid & 3) * 8 + (lane >> 3)) ^ (lane & 7)) * 16);
  const lptr pxw = lds + (wid >> 2) * 32768 + xo;
#pragma unroll
  for (int j = 0; j < 8; ++j) {
    u32x4 n;
#pragma unroll
    for (int i = 0; i < 4; ++i) n[i] = pk2(__builtin_fmaf(bf_lo(xr[j][i]), rs, mr), __builtin_fmaf(bf_hi(xr[j][i]), rs, mr));
    if (j < 2) BSTORE_DEV(n, rxn, (cb < 4 ? 0u : OOB) + (unsigned)(rb * 8 + (rot ^ j)) * 8192 + (wid >> 2) * 4096 + xo);
    *(LAS u32x4*)(pxw + j * 4096) = n;
  }
  issue_w(2); issue_w(3);
  TOUCH();
  const bool has_ffn = cb < 7;
  const int fb = cb * 2 + (rb & 1), w4 = wid & 3;
  const rsrc_t rg = mk_rsrc(gwg - 1024, D_ * F_ * 2 + 1024), ru = mk_rsrc(gwu - 1024, D_ * F_ * 2 + 1024);
  const unsigned vwf = ((lane >> 2) & 7) * (F_ * 2) + (lane >> 5) * 64 + (lane & 3) * 16;
  auto gu_w = [&](int s) {
    const unsigned so = (has_ffn ? 0u : OOB) + (unsigned)(w4 * 8) * (F_ * 2) + fb * 256 + (unsigned)(s ^ (2 * rot)) * (32 * F_ * 2) + 1024;
    const unsigned d = lds0 + WR + gslot(s) * GSLOT + w4 * 2048;
    dma16x2(rg, vwf, so, so + 128, d); dma16x2(ru, vwf, so, so + 128, d + 8192);
  };
  auto gu_x = [&](int s) {
    dma16_dev(rxn, voff, (has_ffn ? 0u : OOB) + (unsigned)((rb ^ 1) * 8 + ((s >> 1) ^ rot)) * 8192 + (w4 >> 1) * 4096 + (s & 1) * 2048 + (w4 & 1) * 1024,
              lds0 + WR + gslot(s) * GSLOT + 16384 + w4 * 1024);
  };
  auto gu_fill = [&](int s) { gu_w(s); gu_x(s); };
  u64 fl;
  lptr pa[4];
#pragma unroll
  for (int g = 0; g < 4; ++g) pa[g] = lds + (2 * g + h) * 512 + ((r ^ (2 * g + h)) * 16);

  const rsrc_t rkv = mk_rsrc(ws + WS_KV + (size_t)head * 131072, 131072), rh = mk_rsrc(ws + WS_H, 7340032);
  const unsigned vvo = h * 256 + (r & 15) * 16;
  const bf16x8 ONES = {0x3f80, 0x3f80, 0x3f80, 0x3f80, 0x3f80, 0x3f80, 0x3f80, 0x3f80};
  const bf16x8 PAD = ((r & 27) == 16) ? ONES : (bf16x8){};
  const int qt0 = (bi & 7) * 8 + w4 * 2;
  const unsigned ring = lds0 + (wid < 4 ? KVA : KVB), kt0 = wid < 4 ? 0 : 32;
  const unsigned kvm = ring + w4 * REC, kvs = (kt0 + w4) * 2048;
  u32x4 QAu, QBu, Ku, V0u;
  auto att_loads = [&] {
    const rsrc_t rq = mk_rsrc(ws + WS_Q, 2097152);
    BLOAD_DEV(QAu, voff, rq, (unsigned)(head * 64 + qt0) * 1024);
    BLOAD_DEV(QBu, voff, rq, (unsigned)(head * 64 + qt0 + 1) * 1024);
    BLOAD_DEV(Ku, voff, rkv, ((kt0 + 63) & 63) * 2048);
    BLOAD_DEV(V0u, vvo, rkv, ((kt0 + 63) & 63) * 2048 + 1024);
  };
  auto att_ring = [&] {
    dma16_dev(rkv, voff, kvs, kvm); dma16_dev(rkv, voff, kvs + 1024, kvm + 1024);
    dma16_dev(rkv, voff, kvs + 8192, kvm + CHUNK); dma16_dev(rkv, voff, kvs + 8192 + 1024, kvm + CHUNK + 1024);
    *(LAS bf16x8*)(lds + (ring - lds0) + (w4 * 4 + (r & 3)) * REC + 2048 + h * 16) = h ? (bf16x8){} : ONES;
  };
  LAS float* const mx = (LAS float*)(lds + KVA + 4 * CHUNK + w4 * 512) + lane;
  bf16x8 QA, QB, K, V0, V1, P1;
  f32x16 SA, CA, CB, OA, OB;
#define MV2(d, s) "v_mov_b64 v[" #d ":" #d "+1], " s "\n\t"
#define MV16(d, s) MV2(d, s) MV2(d + 2, s) MV2(d + 4, s) MV2(d + 6, s) MV2(d + 8, s) MV2(d + 10, s) MV2(d + 12, s) MV2(d + 14, s)
  auto att_init = [&](float ma, float mb) {
    QA = __builtin_bit_cast(bf16x8, QAu); QB = __builtin_bit_cast(bf16x8, QBu); K = __builtin_bit_cast(bf16x8, Ku);
    V0 = (r >= 16) ? PAD : __builtin_bit_cast(bf16x8, V0u);
    asm volatile(MV16(0, "%[ma]") MV2(76, "0") MV2(78, "0") MF("v[16:31]", "v[68:71]", "v[64:67]", "v[0:15]")
                 MV16(96, "%[mb]") MV16(48, "0") MV16(112, "0") MV2(84, "0") MV2(86, "0")
                 : "=&{v[0:15]}"(CA), "=&{v[96:111]}"(CB), "=&{v[16:31]}"(SA), "=&{v[48:63]}"(OA), "=&{v[112:127]}"(OB), "=&{v[76:79]}"(V1), "=&{v[84:87]}"(P1)
                 : "{v[68:71]}"(K), "{v[64:67]}"(QA), [ma] "v"((f32x2){ma, ma}), [mb] "v"((f32x2){mb, mb}));
  };
  const unsigned apad = ((r & 27) == 16) ? 2048 : 2064;
  const unsigned aK = ring + voff, aV0 = ring + (r < 16 ? 1024 + vvo : apad), aV1 = ring + (r < 16 ? 1536 + vvo : apad);
  unsigned t0, keep;
  const int rt = wid & 1, kq = wid >> 1;
  const rsrc_t rwd = mk_rsrc(gwd, F_ * D_ * 2), rwo = mk_rsrc(gwo, D_ * D_ * 2), ro = mk_rsrc(ws + WS_O, 2097152);
  const unsigned vw1 = ((lane >> 2) & 7) * 1024 + (lane >> 5) * 64 + (lane & 3) * 16;
#define DADDR(st) ((st) < 5 ? (st) * 16384 : (st) == 5 || (st) == 11 ? KVA : (st) < 11 ? ((st) - 6) * 16384 : (st) < 14 ? ((st) - 12) * 16384 : \
                   (st) == 14 ? 81920 : (st) == 15 ? 147456 : (st) == 16 ? 98304 : 32768)
  auto d_so = [&](int st, int pc) { return (unsigned)((st < 14 ? st : st - 14) * 128 + pc * 16) * 1024 + co * 128; };
  auto d_m = [&](int st, int pc) { return lds0 + DADDR(st) + pc * 2048; };
  auto issue_dp = [&](int st, int pc) { dma16x2(st < 14 ? rwd : rwo, vw1, d_so(st, pc), d_so(st, pc) + 8192, d_m(st, pc)); };
  auto h_so = [&](int e) { return (unsigned)((rb * 2 + rt) * 112 + e * 4 + kq * 2) * 1024; };
  const unsigned kvd = kvs + (wid < 4 ? 4 * 8192 : 0);
  u32x4 ha[14], hb[14], xv;
  const unsigned kve = wid < 4 ? OOB : kvd;
  LAS f32x4* ex = (LAS f32x4*)(lds + 147456 + w4 * 4096) + lane;
  LAS float* el = (LAS float*)(lds + 145408 + w4 * 512) + lane;
  if (cb >= 4) wait_vm<10>();
  WAIT_BAR(12);

  if (wid < 4) {
    {
      const int ch = wid >> 1;
      lptr par[4];
#pragma unroll
      for (int g = 0; g < 4; ++g) par[g] = pa[g] + rt * 32768;
      lptr pw = lds + WR + h * 3072 + ch * 512 + trn, pw2 = pw + 2 * QSLOT;
      asm volatile("" : "+v"(pw2));
      const int pair = cq * 2 + ch, tile = rb * 2 + rt;
      f32x16 aq = {}, ak = {}, av = {};
      QFrag f0, f1;
      f0.x = lds_frag(par[0]); f0.q = lds_frag_tr(pw, pw + 256); f0.k = lds_frag_tr(pw + 1024, pw + 1024 + 256); f0.v = lds_frag_tr(pw + 2048, pw + 2048 + 256);
      qkv_steps<0>(par, pw, pw2, f0, f1, aq, ak, av, ws + WS_FX + (rb * 8 + cb) * 8, tag, threadIdx.x, cb < 4);
#pragma unroll
      for (int i = 0; i < 16; ++i) aq[i] *= QSCALE;
      u32x4 qs[6] = {pack8(ak, 0), pack8(ak, 8), pack8(av, 0), pack8(av, 8), pack8(aq, 0), pack8(aq, 8)};
      asm volatile("" : "+v"(qs[0]), "+v"(qs[1]), "+v"(qs[2]), "+v"(qs[3]), "+v"(qs[4]), "+v"(qs[5]));
      const rsrc_t rq = mk_rsrc(ws + WS_Q, 2097152 + 4194304);
      const unsigned ko = 2097152 + (unsigned)((2 * pair) * 64 + ((tile + 63) & 63)) * 2048 + voff;
      const unsigned vo = 2097152 + (unsigned)(2 * pair + (r >> 4)) * 131072 + vvo;
      const unsigned qo = (unsigned)((2 * pair) * 64 + tile) * 1024 + voff;
      auto qst = [&](int i) {
        BSTORE_DEV(qs[i], rq, i == 0 ? ko : i == 1 ? ko + 131072 : i == 2 ? vo + ((tile + 63) & 63) * 2048 + 1024 : i == 3 ? vo + tile * 2048 + 1536 : i == 4 ? qo : qo + 65536);
      };
      auto fq_set = [&] {
        __builtin_amdgcn_raw_buffer_store_b64(__builtin_bit_cast(u32x2, tag), mk_rsrc(ws + WS_FQ, 8192),
                                              lane == 0 ? (unsigned)((cq * 2 + (wid >> 1)) * 64 + rb * 2 + (wid & 1)) * 8 : OOB, 0, 16);
      };
      if (__builtin_expect(has_ffn, 1)) {
        BAR();
        qst(0); qst(1); qst(2);
        BAR(); qst(3); qst(4); qst(5); gu_fill(4);
        BAR(); gu_fill(5);
        BAR(); gu_fill(6);
        WAIT_BAR(10); fq_set(); gu_fill(7);
        WAIT_BAR(10); gu_fill(8);
        WAIT_BAR(10); gu_fill(9);
        WAIT_BAR(10); gu_fill(10);
      } else {
        qst(0); qst(1); qst(2); qst(3); qst(4); qst(5);
        wait_vm<0>();
        fq_set();
#pragma unroll 1
        for (int i = 0; i < 8; ++i) BAR();
      }
    }
    if (!has_ffn) __builtin_amdgcn_s_sleep(32);
    asm volatile("global_load_dwordx2 %0, %1, off sc1" : "=v"(fl) : "v"(ws + WS_FQ + ((head >> 1) * 64 + lane) * 8) : "memory");
    WAIT_BAR(11);
    PIN_AFTER("s_waitcnt vmcnt(0)", "+v"(fl));
    if (__builtin_expect(!__all(fl == tag), 0)) wait_flags(ws + WS_FQ + (head >> 1) * 512, 64, tag, lane);
    u32x4 K1u;
    att_loads();
    BLOAD_DEV(K1u, voff, rkv, 0u);
    BAR();
    att_ring();
    PIN_AFTER("s_waitcnt vmcnt(4)", "+v"(QAu), "+v"(QBu), "+v"(Ku), "+v"(V0u), "+v"(K1u));
    float ma, mb;
    {
      const f32x16 z = {};
      const bf16x8 k0 = __builtin_bit_cast(bf16x8, Ku), k1 = __builtin_bit_cast(bf16x8, K1u), qa = __builtin_bit_cast(bf16x8, QAu), qb = __builtin_bit_cast(bf16x8, QBu);
      const f32x16 a0 = MFMA(k0, qa, z), a1 = MFMA(k1, qa, z), b0 = MFMA(k0, qb, z), b1 = MFMA(k1, qb, z);
      ma = a0[0]; mb = b0[0];
#pragma unroll
      for (int i = 0; i < 16; ++i) { ma = __builtin_fmaxf(__builtin_fmaxf(ma, a0[i]), a1[i]); mb = __builtin_fmaxf(__builtin_fmaxf(mb, b0[i]), b1[i]); }
      asm volatile("" : "+v"(ma), "+v"(mb));
    }
    BAR();
    ma = -(xhalf_max(ma) + MBIAS); mb = -(xhalf_max(mb) + MBIAS);
    mx[0] = ma; mx[64] = mb;
    att_init(ma, mb);
    asm volatile(ATT_BEGIN "s_setprio 2\n\t"
                 VB(2) T_KVI("0", 16640, 16384) T_NONE("2080") SB_ T_NONE("4160") T_NONE("6240")
                 VB(2) T_KVI("8320", 24960, 24576) T_NONE("10400") SB_ T_NONE("12480") T_NONE("14560")
                 VB(2) T_KVI("16640", 0, 32768) T_NONE("18720") SB_ T_NONE("20800") T_NONE("22880")
                 VB(2) T_KVI("24960", 8320, 40960) T_NONE("27040") SB_ T_NONE("29120") T_NONE("31200")
                 ATT_END
                 : ATT_STATE : ATT_IN, [rkv] "s"(rkv), [kvm] "s"(kvm), [kvs] "s"(kvs) : ATT_CLOB);
    __builtin_amdgcn_s_setprio(0);
    wait_vm<2>();
  } else {
    auto w2 = [&](int s) { issue_wk(s, w4 * 2); issue_wk(s, w4 * 2 + 1); };
    if (cb >= 4) wait_vm<7>();
    WAIT_BAR(9);  w2(4);
    WAIT_BAR(10); w2(5);
    WAIT_BAR(13); w2(6);
    WAIT_BAR(12); w2(7);
    WAIT_BAR(12);
    asm volatile("global_load_dwordx2 %0, %1, off sc1" : "=v"(fl) : "v"(ws + WS_FX + ((rb ^ 1) * 8 + (lane & 3)) * 8) : "memory");
    gu_w(0);
    if (!has_ffn) wait_vm<7>();
    WAIT_BAR(11); gu_w(1);
    if (!has_ffn) wait_vm<0>();
    PIN_AFTER("s_waitcnt vmcnt(8) lgkmcnt(0)\n\ts_barrier", "+v"(fl));
    const bool xok = __all(fl == tag) || !has_ffn;
    if (xok) gu_x(0);
    gu_w(2);
    if (xok) { gu_x(1); gu_x(2); }
    BAR();
    if (__builtin_expect(xok, 1)) { gu_fill(3); wait_vm<11>(); }
    else { wait_flags(ws + WS_FX + (rb ^ 1) * 64, 4, tag, lane); gu_x(0); gu_x(1); gu_x(2); gu_fill(3); wait_vm<7>(); }
    BAR();
    f32x16 g[4], u[4];
    if (has_ffn) {
      lptr pp[4];
#pragma unroll
      for (int i = 0; i < 4; ++i) { pp[i] = pa[i] + (WR + 16384 - (i >> 1) * 2048); asm volatile("" : "+v"(pp[i])); g[i] = (f32x16){}; u[i] = (f32x16){}; }
      const lptr pg = lds + WR + h * 2048 + (w4 >> 1) * 1024 + (w4 & 1) * 512 + trn;
      lptr pg3 = pg + 3 * GSLOT;
      asm volatile("" : "+v"(pg3));
      GFrag f0, f1;
      gu_read<0, 0>(pa, pp, pg, pg3, f0, 0); gu_read<0, 0>(pa, pp, pg, pg3, f0, 1); gu_read<0, 0>(pa, pp, pg, pg3, f0, 2);
      gu_stages<0>(pa, pp, pg, pg3, f0, f1, g, u, gu_fill);
    } else {
      unsigned d[5];
      wait_vm<0>();
#pragma unroll 1
      for (int i = 0; i < 11; ++i) BAR();
#pragma unroll
      for (int i = 0; i < 5; ++i) {
        const int t = (rb * 4 + w4) * 144 + i * 32 + (r + i * 32 < 144 ? r : 0);
        const char* p = (t < 14336) ? gwd + (size_t)t * 128 : gwo + (size_t)(t - 14336) * 128;
        asm volatile("global_load_dword %0, %1, off" : "=v"(d[i]) : "v"(p) : "memory");
      }
#pragma unroll 1
      for (int i = 11; i < 15; ++i) BAR();
      PIN_AFTER("s_waitcnt vmcnt(0)", "+v"(d[0]), "+v"(d[1]), "+v"(d[2]), "+v"(d[3]), "+v"(d[4]));
    }
    BAR();
    __builtin_amdgcn_s_setprio(3);
    att_loads(); att_ring();
    auto swiglu = [&](int i) {
#pragma unroll
      for (int j = 0; j < 16; ++j) g[i][j] = g[i][j] * u[i][j] * __builtin_amdgcn_rcpf(1.0f + __builtin_amdgcn_exp2f(g[i][j] * -1.4426950408889634f));
      const unsigned ho = (unsigned)(((rb ^ (i >> 1)) * 2 + (i & 1)) * 112 + fb * 8 + w4 * 2) * 1024 + voff;
      BSTORE_DEV(pack8(g[i], 0), rh, ho);
      BSTORE_DEV(pack8(g[i], 8), rh, ho + 1024);
    };
    if (has_ffn) swiglu(0);
    BAR();
    if (has_ffn) { swiglu(1); swiglu(2); }
    BAR();
    const float ma = mx[0], mb = mx[64];
    if (has_ffn) swiglu(3); else wait_vm<0>();
    PIN_AFTER("s_waitcnt vmcnt(8)", "+v"(QAu), "+v"(QBu), "+v"(Ku), "+v"(V0u));
    att_init(ma, mb);
  }
  asm volatile("" :: "v"(pf), "v"(cpf));
  asm volatile(ATT_BEGIN "s_setprio 0\n\t" SB_
               T_KVI("0", 16640, 16384) T_WD("2080", 0) T_WD("4160", 1) T_WD("6240", 2)
               VB(8)
               "buffer_store_dwordx2 %[tag], %[fho], %[rfl], 0 offen sc1\n\t"
               T_KVI("8320", 24960, 24576) T_WD("10400", 3) T_WD("12480", 4) T_NONE("14560")
               VB(7)
               T_KV("16640", kvm, s5_) T_NONE("18720")
               TILE("20800", "", "buffer_load_dwordx2 %[fl], %[fhi], %[rfl], 0 offen sc1\n\t", "", "", "") T_NONE("22880")
               ATT_END
               : ATT_STATE, [fl] "=&v"(fl)
               : ATT_IN, [rkv] "s"(rkv), [rwd] "s"(rwd), [vw1] "v"(vw1), [kvm] "s"(kvm), [kvs] "s"(kvd),
                 [dm0] "s"(RFL(d_m(0, wid))), [ds0] "s"(RFL(d_so(0, wid))), [s5_] "s"(kve + 4 * 8192), [rfl] "s"(mk_rsrc(ws + WS_FH, 2048)), [tag] "v"(tag),
                 [fho] "v"(threadIdx.x == 0 ? (unsigned)(rb * 8 + cb) * 8 : OOB), [fhi] "v"((unsigned)((rb >> 1) * 16 + (lane & 15)) * 8) : ATT_CLOB);
  PIN_AFTER("s_waitcnt vmcnt(0)", "+v"(fl));
  if (__builtin_expect(!__all(fl == tag), 0)) wait_flags(ws + WS_FH + (rb >> 1) * 128, 16, tag, lane);
#define H_OUT(n, st) [a##n] "=&v"(ha[st]), [b##n] "=&v"(hb[st])
  unsigned hs = h_so(0) + (wid >> 2) * OOB;
  f32x4 p0, p1, p2, p3;
  float pl0, pl1;
  asm volatile(ATT_BEGIN SB_
               TILE("24960", "s_add_u32 m0, %[kvm], 8320\n\ts_add_u32 %[t0], %[kvs], 40960\n\t", "buffer_load_dwordx4 %[voff], %[rkv], %[t0] offen sc1 lds\n\t",
                    "buffer_load_dwordx4 %[voff], %[rkv], %[t0] offen offset:1024 sc1 lds\n\t", HLA(a0), HLB(b0))
               T_H2("27040", a1, b1, a2, b2) T_H2("29120", a3, b3, a4, b4) T_H2("31200", a5, b5, a6, b6)
               "s_cmp_lt_u32 %[wid], 4\n\ts_cbranch_scc1 L_skip_%=\n\t"
               "s_setprio 2\n\t"
               SB_ T_KVI("0", 16640, 49152) T_NONE("2080") SB_ T_NONE("4160") T_NONE("6240")
               VB(2) T_KVI("8320", 24960, 57344)
               TILE("10400", "", "", "", "ds_read_b128 %[p0], %[ex]\n\tds_read_b128 %[p1], %[ex] offset:1024\n\tds_read_b32 %[pl0], %[el]\n\t", "")
               SB_
               TILE("12480", "", "", "", "ds_read_b128 %[p2], %[ex] offset:2048\n\tds_read_b128 %[p3], %[ex] offset:3072\n\tds_read_b32 %[pl1], %[el] offset:256\n\t", "")
               T_NONE("14560")
               VB(2) T_NONE("16640") T_NONE("18720") SB_ T_NONE("20800") T_NONE("22880")
               VB(0) T_NONE("24960") T_NONE("27040") SB_ T_NONE("29120") T_NONE("31200")
               "L_skip_%=:\n\t"
               ATT_END
               : ATT_STATE, H_OUT(0, 0), H_OUT(1, 1), H_OUT(2, 2), H_OUT(3, 3), H_OUT(4, 4), H_OUT(5, 5), H_OUT(6, 6),
                 [p0] "=&v"(p0), [p1] "=&v"(p1), [p2] "=&v"(p2), [p3] "=&v"(p3), [pl0] "=&v"(pl0), [pl1] "=&v"(pl1), [hs] "+s"(hs)
               : ATT_IN, [rkv] "s"(rkv), [kvm] "s"(kvm), [kvs] "s"(kve), [wid] "s"(wid), [ex] "v"(ex), [el] "v"(el),
                 [rh] "s"(rh) : ATT_CLOB);

  const int t = threadIdx.x;
  const unsigned off = (unsigned)(rb * 64 + (t >> 3)) * 1024 + co * 128 + (t & 7) * 16;
  f32x16 d0, d1;
  F4 e0, e1;
#define D_PTR(pd, a) ((a) < 65536 ? pd[0] + (a) : (a) < KVA ? pd[1] + ((a) - 65536) : pd[2] + ((a) - KVA))
#define D_READ_A(n, w_) do { n.w0 = lds_frag_tr(w_, w_ + 1024); n.w1 = lds_frag_tr(w_ + 512, w_ + 1536); } while (0)
#define D_READ_B(n, w_) do { n.x0 = lds_frag_tr(w_ + 2048, w_ + 3072); n.x1 = lds_frag_tr(w_ + 2560, w_ + 3584); } while (0)
#define D_STEPX(c, n, nxt, A, B, RD, X, Y) do { \
    MM(d0, c.w0, __builtin_bit_cast(bf16x8, A)); if (RD) D_READ_A(n, (nxt)); MM(d1, c.w1, __builtin_bit_cast(bf16x8, A)); if (RD) D_READ_B(n, (nxt)); \
    MM(d0, c.x0, __builtin_bit_cast(bf16x8, B)); X; MM(d1, c.x1, __builtin_bit_cast(bf16x8, B)); Y; } while (0)
#define H_LOAD(e, so) do { BLOAD_DEV(ha[e], voff, rh, so); BLOAD_DEV(hb[e], voff, rh, (so) + 1024); } while (0)
  PIN_AFTER("s_waitcnt vmcnt(0)", "+v"(ha[0]), "+v"(ha[1]), "+v"(ha[2]), "+v"(ha[3]), "+v"(ha[4]), "+v"(ha[5]), "+v"(ha[6]),
            "+v"(hb[0]), "+v"(hb[1]), "+v"(hb[2]), "+v"(hb[3]), "+v"(hb[4]), "+v"(hb[5]), "+v"(hb[6]));
  OB = MFMA(V1, P1, OB);
  lptr pe[3];
  pe[0] = lds + (kq & 1) * 4096 + h * 256 + trn; pe[1] = pe[0] + 65536; pe[2] = pe[0] + KVA;
  asm volatile("" : "+v"(pe[0]), "+v"(pe[1]), "+v"(pe[2]));
#define EP(e) D_PTR(pe, DADDR((e) >> 1) + ((e) & 1) * 8192)
#define RADDR(q) ((q) == 0 ? 49152 : (q) == 1 ? 65536 : (q) == 2 ? KVA : 0)
  auto put_partial = [&](int base) {
    const lptr red = lds + base + (rt * 32 + r) * 256;
#pragma unroll
    for (int i = 0; i < 4; ++i) {
      *(LAS f32x4*)(red + (((2 * i + h) ^ (r & 15)) * 16)) = V4(d0, i);
      *(LAS f32x4*)(red + (((8 + 2 * i + h) ^ (r & 15)) * 16)) = V4(d1, i);
    }
  };
  if (wid < 4) {
    d0 = d1 = (f32x16){};
#define E_DMA(st, i) issue_dp(st, wid * 2 + (i))
#define E_STAGE(k, X0, Y0, X1, Y1) do { D_STEPX(e0, e1, EP(2 * (k) + 1), ha[(2 * (k)) % 14], hb[(2 * (k)) % 14], true, X0, Y0); \
      D_STEPX(e1, e0, EP(2 * (k) + 2), ha[(2 * (k) + 1) % 14], hb[(2 * (k) + 1) % 14], (k) < 13, X1, Y1); } while (0)
#define HE(e) H_LOAD((e) % 14, h_so(e))
#define HP(e) "+v"(ha[(e) % 14]), "+v"(hb[(e) % 14])
#define W2(k, sa, sb, e) do { E_STAGE(k, E_DMA(sa, 0), E_DMA(sa, 1), E_DMA(sb, 0), E_DMA(sb, 1)); HE(e); } while (0)
#define W1(k, sa, e) do { E_STAGE(k, E_DMA(sa, 0), E_DMA(sa, 1), HE(e), HE((e) + 1)); HE((e) + 2); } while (0)
#define FB(...) PIN_AFTER("s_waitcnt vmcnt(10) lgkmcnt(0)\n\ts_barrier", __VA_ARGS__)
    D_READ_A(e0, EP(0)); D_READ_B(e0, EP(0));
    E_STAGE(0, HE(7), HE(8), HE(9), ); E_STAGE(1, HE(10), , HE(11), );
    BAR();
    ex[0] = V4(OA, 0); ex[64] = V4(OA, 1); ex[128] = V4(OB, 0); ex[192] = V4(OB, 1); el[0] = OA[8]; el[64] = OB[8];
    W2(2, 5, 6, 12);   FB(HP(7), HP(8), HP(9), HP(10), HP(11));
    W2(3, 7, 8, 13);   FB(HP(12));
    W1(4, 9, 14);      FB(HP(13));
    W2(5, 10, 14, 17); FB(HP(14), HP(15), HP(16));
    W2(6, 11, 15, 18); FB(HP(17));
    W1(7, 12, 19);     FB(HP(18));
    W1(8, 13, 22);
    PIN_AFTER("s_waitcnt vmcnt(6) lgkmcnt(0)\n\ts_barrier", HP(19), HP(20), HP(21));
    E_STAGE(9, HE(25), HE(26), HE(27), E_DMA(16, 0));
    E_STAGE(10, E_DMA(16, 1), E_DMA(17, 0), E_DMA(17, 1), BLOAD(xv, off, rx, 0u));
    PIN_AFTER("s_waitcnt vmcnt(9)", HP(22), HP(23), HP(24), HP(25), HP(26), HP(27));
    E_STAGE(11, , , , ); E_STAGE(12, , , , ); E_STAGE(13, , , , );
    if (b == 193 && wid == 0) {
#pragma unroll 1
      for (int i = 0; i < 4; ++i) wait_flags(ws + WS_FH + i * 512, 64, tag, lane);
      if (lane == 0) set_flag(ws + WS_EPOCH, tag);
    }
    PIN_AFTER("s_waitcnt vmcnt(0) lgkmcnt(0)\n\ts_barrier", "+v"(xv));
    put_partial(kq ? RADDR(1) : RADDR(0));
  } else {
    auto store_o = [&](const f32x16& O, int i) {
      float il = __builtin_amdgcn_rcpf(O[8]);
      asm volatile("s_nop 0" : "+v"(il));
      const u32x4 v = {pk2(mul1(O[0], il), mul1(O[1], il)), pk2(mul1(O[2], il), mul1(O[3], il)), pk2(mul1(O[4], il), mul1(O[5], il)), pk2(mul1(O[6], il), mul1(O[7], il))};
      BSTORE_DEV(v, ro, (unsigned)((qt0 + i) * 32 + head) * 1024 + voff);
    };
    auto check_o = [&](f32x16 O, const bf16x8& Q, int i) {
      float mag = __builtin_fabsf(O[0]);
#pragma unroll
      for (int j = 1; j < 8; ++j) mag += __builtin_fabsf(O[j]);
      const bool bad = (int)!(O[8] < 0x1p120f) | (int)!(mag < 0x1p126f) | (int)!(xhalf_max(mag) > 0x1p-100f);
      if (__builtin_expect(__any(bad), 0)) {
        const f32x16 z = {};
        float mt = -__builtin_inff();
#pragma unroll 1
        for (int tt = 0; tt < 64; ++tt) {
          const f32x16 s = MFMA(__builtin_bit_cast(bf16x8, __builtin_amdgcn_raw_buffer_load_b128(rkv, voff, tt * 2048, 16)), Q, z);
#pragma unroll
          for (int j = 0; j < 16; ++j) mt = __builtin_fmaxf(mt, s[j]);
        }
        mt = xhalf_max(mt);
        O = z;
#pragma unroll 1
        for (int tt = 0; tt < 64; ++tt) {
          f32x16 s = MFMA(__builtin_bit_cast(bf16x8, __builtin_amdgcn_raw_buffer_load_b128(rkv, voff, ((tt + 63) & 63) * 2048, 16)), Q, z);
          bf16x8 v0 = __builtin_bit_cast(bf16x8, __builtin_amdgcn_raw_buffer_load_b128(rkv, vvo, ((tt + 63) & 63) * 2048 + 1024, 16));
          bf16x8 v1 = __builtin_bit_cast(bf16x8, __builtin_amdgcn_raw_buffer_load_b128(rkv, vvo, tt * 2048 + 1536, 16));
          if (r >= 16) { v0 = PAD; v1 = PAD; }
#pragma unroll
          for (int j = 0; j < 16; ++j) s[j] = __builtin_amdgcn_exp2f(s[j] - mt);
          O = MFMA(v0, __builtin_bit_cast(bf16x8, pack8(s, 0)), O);
          O = MFMA(v1, __builtin_bit_cast(bf16x8, pack8(s, 8)), O);
        }
        asm volatile("s_nop 15" : "+v"(O));
        wait_vm<0>();
        store_o(O, i);
      }
    };
#pragma unroll
    for (int j = 0; j < 4; ++j) { OA[j] = add1(OA[j], p0[j]); OA[4 + j] = add1(OA[4 + j], p1[j]); }
    OA[8] += pl0;
    store_o(OA, 0);
    asm volatile("s_nop 3" : "+v"(OB) :: "memory");
#pragma unroll
    for (int j = 0; j < 4; ++j) { OB[j] = add1(OB[j], p2[j]); OB[4 + j] = add1(OB[4 + j], p3[j]); }
    OB[8] += pl1;
    store_o(OB, 1);
    check_o(OA, QA, 0); check_o(OB, QB, 1);
    wait_vm<0>();
    __builtin_amdgcn_raw_buffer_store_b64(__builtin_bit_cast(u32x2, tag), mk_rsrc(ws + WS_FO, 16384),
                                          lane < 2 ? (unsigned)((qt0 + lane) * 32 + head) * 8 : OOB, 0, 16);
    BLOAD(xv, off, rx, 0u);
    D_READ_A(e0, EP(28)); D_READ_B(e0, EP(28));
    d0 = d1 = (f32x16){};
    if (wid == 4) wait_flags(ws + WS_FO + rb * 512, 64, tag, lane);
    PIN_AFTER("s_waitcnt vmcnt(0) lgkmcnt(0)\n\ts_barrier", "+v"(xv));
    u32x4 oa[8], ob[8];
    const unsigned obase = (unsigned)((rb * 2 + rt) * 32 + (kq & 1) * 2) * 1024;
#pragma unroll
    for (int j = 0; j < 8; ++j) { BLOAD_DEV(oa[j], voff, ro, obase + j * 4096); BLOAD_DEV(ob[j], voff, ro, obase + j * 4096 + 1024); }
#define O_STEP(c, n, j) do { wait_vm2<14 - 2 * (j)>(oa[j], ob[j]); D_STEPX(c, n, EP(29 + (j)), oa[j], ob[j], (j) < 7, , ); } while (0)
    O_STEP(e0, e1, 0); O_STEP(e1, e0, 1); O_STEP(e0, e1, 2); O_STEP(e1, e0, 3); O_STEP(e0, e1, 4); O_STEP(e1, e0, 5); O_STEP(e0, e1, 6); O_STEP(e1, e0, 7);
    put_partial((kq & 1) ? RADDR(3) : RADDR(2));
  }
  BAR();
  f32x4 s0 = {bf_lo(xv[0]), bf_hi(xv[0]), bf_lo(xv[1]), bf_hi(xv[1])}, s1 = {bf_lo(xv[2]), bf_hi(xv[2]), bf_lo(xv[3]), bf_hi(xv[3])};
  {
    const int row = t >> 3, c = (t & 7) * 2;
    const lptr rd0 = lds + row * 256 + ((c ^ (row & 15)) * 16), rd1 = lds + row * 256 + (((c + 1) ^ (row & 15)) * 16);
#pragma unroll
    for (int p = 0; p < 4; ++p) {
      const int o = RADDR(p);
      s0 += *(const LAS f32x4*)(rd0 + o); s1 += *(const LAS f32x4*)(rd1 + o);
    }
  }
  __builtin_amdgcn_raw_buffer_store_b128((u32x4){pk2(s0[0], s0[1]), pk2(s0[2], s0[3]), pk2(s1[0], s1[1]), pk2(s1[2], s1[3])}, mk_rsrc(out, S_ * D_ * 2), off, 0, 0);
  asm volatile("Lk_tail:");
}

extern "C" void kernel_launch(void* const* d_in, const int* in_sizes, int n_in, void* d_out, int out_size, void* d_ws,
                              size_t ws_size, hipStream_t stream) {
  (void)hipFuncSetAttribute((const void*)k_all, hipFuncAttributeMaxDynamicSharedMemorySize, LDS_BYTES);
  k_all<<<256, 512, LDS_BYTES, stream>>>((const char*)d_in[0], (const char*)d_in[1], (const char*)d_in[2], (const char*)d_in[4],
                                         (const char*)d_in[5], (const char*)d_in[6], (char*)d_ws, (char*)d_out);
}
```

```cpp
#include <hip/hip_runtime.h>
#include <hip/hip_bf16.h>
#include <stdint.h>

typedef __attribute__((ext_vector_type(8))) short bf16x8;
typedef __attribute__((ext_vector_type(4))) short s16x4;
typedef __attribute__((ext_vector_type(16))) float f32x16;
typedef __attribute__((ext_vector_type(4))) float f32x4;
typedef __attribute__((ext_vector_type(2))) float f32x2;
typedef __attribute__((ext_vector_type(4))) unsigned u32x4;
typedef __attribute__((ext_vector_type(2))) unsigned u32x2;
typedef __attribute__((ext_vector_type(2))) __bf16 bf2;
typedef unsigned long long u64;
typedef __amdgpu_buffer_rsrc_t rsrc_t;
#define LAS __attribute__((address_space(3)))
#define GAS __attribute__((address_space(1)))
typedef LAS char* lptr;

#define MFMA(a, b, c) __builtin_amdgcn_mfma_f32_32x32x16_bf16((a), (b), (c), 0, 0, 0)
#define MM(c, a, b) do { __builtin_amdgcn_sched_barrier(0); c = MFMA(a, b, c); asm volatile("" : "+v"(c) :: "memory"); \
                         __builtin_amdgcn_sched_barrier(0); } while (0)
#define RFL(x) __builtin_amdgcn_readfirstlane(x)

constexpr int S_ = 2048, D_ = 512, F_ = 1792;
constexpr size_t WS_EPOCH = 0;
constexpr size_t WS_FH = 2048;
constexpr size_t WS_FQ = 16384;
constexpr size_t WS_FX = 49152;
constexpr size_t WS_FO = 32768;
constexpr size_t WS_Q    = 65536;
constexpr size_t WS_KV   = WS_Q + 2097152;
constexpr size_t WS_O    = WS_KV + 4194304;
constexpr size_t WS_XN   = WS_O + 2097152;
constexpr size_t WS_H    = WS_XN + 2097152;
constexpr float QSCALE = 0.25f * 1.4426950408889634f;
constexpr float MBIAS = 80.0f;
constexpr int LDS_BYTES = 163840;
constexpr unsigned SPIN_CAP = 1u << 22;
constexpr unsigned OOB = 0x7ff00000u;

__device__ __forceinline__ rsrc_t mk_rsrc(const void* p, unsigned bytes) {
  return __builtin_amdgcn_make_buffer_rsrc((void*)p, (short)0, (int)bytes, 0x00020000);
}
__device__ __forceinline__ void dma16(rsrc_t r, unsigned voff, unsigned soff, unsigned lds) {
  unsigned keep;
  asm volatile("s_mov_b32 %0, m0\n\ts_mov_b32 m0, %1\n\ts_nop 0\n\tbuffer_load_dwordx4 %2, %3, %4 offen lds\n\ts_mov_b32 m0, %0"
               : "=&s"(keep) : "s"(lds), "v"(voff), "s"(r), "s"(soff) : "memory");
}
__device__ __forceinline__ void dma16_dev(rsrc_t r, unsigned voff, unsigned soff, unsigned lds) {
  unsigned keep;
  asm volatile("s_mov_b32 %0, m0\n\ts_mov_b32 m0, %1\n\ts_nop 0\n\tbuffer_load_dwordx4 %2, %3, %4 offen sc1 lds\n\ts_mov_b32 m0, %0"
               : "=&s"(keep) : "s"(lds), "v"(voff), "s"(r), "s"(soff) : "memory");
}
__device__ __forceinline__ void dma16x3(rsrc_t r, unsigned voff, unsigned soff, unsigned lds) {
  unsigned keep;
  asm volatile("s_mov_b32 %0, m0\n\ts_mov_b32 m0, %1\n\ts_nop 0\n\tbuffer_load_dwordx4 %2, %3, %4 offen lds\n\t"
               "buffer_load_dwordx4 %2, %3, %4 offen offset:1024 lds\n\tbuffer_load_dwordx4 %2, %3, %4 offen offset:2048 lds\n\ts_mov_b32 m0, %0"
               : "=&s"(keep) : "s"(lds), "v"(voff), "s"(r), "s"(soff) : "memory");
}
__device__ __forceinline__ float fma1(float a, float b, float c) {
  float d; asm("v_fma_f32 %0, %1, %2, %3" : "=v"(d) : "v"(a), "v"(b), "v"(c)); return d;
}
__device__ __forceinline__ float add1(float a, float b) { float d; asm("v_add_f32 %0, %1, %2" : "=v"(d) : "v"(a), "v"(b)); return d; }
__device__ __forceinline__ float mul1(float a, float b) { float d; asm("v_mul_f32 %0, %1, %2" : "=v"(d) : "v"(a), "v"(b)); return d; }
#define BLOAD(dst, voff, r, soff) asm volatile("buffer_load_dwordx4 %0, %1, %2, %3 offen" : "=v"(dst) : "v"(voff), "s"(r), "s"(soff) : "memory")
#define BLOAD_DEV(dst, voff, r, soff) asm volatile("buffer_load_dwordx4 %0, %1, %2, %3 offen sc1" : "=v"(dst) : "v"(voff), "s"(r), "s"(soff) : "memory")
#define BSTORE_DEV(val, r, voff) __builtin_amdgcn_raw_buffer_store_b128((val), (r), (voff), 0, 16)
#define WAIT_BAR(N) asm volatile("s_waitcnt vmcnt(" #N ") lgkmcnt(0)\n\ts_barrier" ::: "memory")
#define BAR() asm volatile("s_waitcnt lgkmcnt(0)\n\ts_barrier" ::: "memory")
template <int N> __device__ __forceinline__ void wait_bar() { asm volatile("s_waitcnt vmcnt(%0) lgkmcnt(0)\n\ts_barrier" :: "n"(N) : "memory"); }
template <int N> __device__ __forceinline__ void wait_vm2(u32x4& a, u32x4& b) {
  asm volatile("s_waitcnt vmcnt(%0)" :: "n"(N) : "memory"); __builtin_amdgcn_sched_barrier(0); asm volatile("" : "+v"(a), "+v"(b) :: "memory");
}
template <int N> __device__ __forceinline__ void wait_vm1(u32x4& a) {
  asm volatile("s_waitcnt vmcnt(%0)" :: "n"(N) : "memory"); __builtin_amdgcn_sched_barrier(0); asm volatile("" : "+v"(a) :: "memory");
}

__device__ __forceinline__ unsigned pk2(float lo, float hi) {
  f32x2 v = {lo, hi};
  return __builtin_bit_cast(unsigned, __builtin_convertvector(v, bf2));
}
__device__ __forceinline__ float bf_lo(unsigned u) { return __uint_as_float(u << 16); }
__device__ __forceinline__ float bf_hi(unsigned u) { return __uint_as_float(u & 0xffff0000u); }
__device__ __forceinline__ float xhalf_max(float v) {
  auto r = __builtin_amdgcn_permlane32_swap(__float_as_uint(v), __float_as_uint(v), false, false);
  return __builtin_fmaxf(__uint_as_float(r[0]), __uint_as_float(r[1]));
}
__device__ __forceinline__ bf16x8 lds_frag(lptr p) { return *(const LAS bf16x8*)p; }
typedef short v4i16_t __attribute__((ext_vector_type(4)));
__device__ __forceinline__ bf16x8 lds_frag_tr(lptr p0, lptr p1) {
  s16x4 a = __builtin_bit_cast(s16x4, __builtin_amdgcn_ds_read_tr16_b64_v4i16((LAS v4i16_t*)p0));
  s16x4 b = __builtin_bit_cast(s16x4, __builtin_amdgcn_ds_read_tr16_b64_v4i16((LAS v4i16_t*)p1));
  return (bf16x8){a[0], a[1], a[2], a[3], b[0], b[1], b[2], b[3]};
}
__device__ __forceinline__ float dot2(unsigned a, unsigned b, float c) {
  return __builtin_amdgcn_fdot2_f32_bf16(__builtin_bit_cast(bf2, a), __builtin_bit_cast(bf2, b), c, false);
}
__device__ __forceinline__ u32x4 pack8(const f32x16& a, int o) {
  return (u32x4){pk2(a[o], a[o + 1]), pk2(a[o + 2], a[o + 3]), pk2(a[o + 4], a[o + 5]), pk2(a[o + 6], a[o + 7])};
}
template <int CTRL> __device__ __forceinline__ float dpp_add(float x) {
  return x + __builtin_bit_cast(float, __builtin_amdgcn_mov_dpp(__builtin_bit_cast(int, x), CTRL, 0xf, 0xf, true));
}
__device__ __forceinline__ float sum8(float x) { return dpp_add<0x141>(dpp_add<0x4E>(dpp_add<0xB1>(x))); }

__device__ __forceinline__ u64 read_tag(const char* ws) {
  u64 e;
  asm volatile("s_load_dwordx2 %0, %1, 0x0\n\ts_waitcnt lgkmcnt(0)" : "=s"(e) : "s"(ws + WS_EPOCH) : "memory");
  e = (e ^ (e >> 31)) * 0x9E3779B97F4A7C15ull + 0x632BE59BD9B4E019ull;
  return e ^ (e >> 29);
}
__device__ __forceinline__ void set_flag(char* p, u64 tag) { __hip_atomic_store((GAS u64*)p, tag, __ATOMIC_RELAXED, __HIP_MEMORY_SCOPE_AGENT); }
__device__ __forceinline__ void wait_flags(const char* p, int n, u64 tag, int lane) {
  GAS u64* f = (GAS u64*)p + (lane < n ? lane : 0);
#pragma unroll 1
  for (unsigned spins = 0; spins < SPIN_CAP; ++spins) {
    if (__all(__hip_atomic_load(f, __ATOMIC_RELAXED, __HIP_MEMORY_SCOPE_AGENT) == tag)) break;
    __builtin_amdgcn_s_sleep(4);
  }
  asm volatile("" ::: "memory");
}


__device__ __forceinline__ void dma16x2(rsrc_t r, unsigned voff, unsigned soff0, unsigned soff1, unsigned lds) {
  unsigned keep;
  asm volatile("s_mov_b32 %0, m0\n\ts_mov_b32 m0, %1\n\ts_nop 0\n\tbuffer_load_dwordx4 %2, %3, %4 offen lds\n\t"
               "buffer_load_dwordx4 %2, %3, %5 offen offset:1024 lds\n\ts_mov_b32 m0, %0"
               : "=&s"(keep) : "s"(lds), "v"(voff), "s"(r), "s"(soff0), "s"(soff1 - 1024u) : "memory");
}
template <int N> __device__ __forceinline__ void wait_vm() { asm volatile("s_waitcnt vmcnt(%0)" :: "n"(N) : "memory"); }
#define PIN_AFTER(W, ...) do { asm volatile(W ::: "memory"); __builtin_amdgcn_sched_barrier(0); asm volatile("" : __VA_ARGS__ :: "memory"); } while (0)
#define V4(c, g_) (f32x4){c[4 * (g_)], c[4 * (g_) + 1], c[4 * (g_) + 2], c[4 * (g_) + 3]}

constexpr int WR = 65536, QSLOT = 24576;
constexpr int GSLOT = 20480;
constexpr int REC = 2080, CHUNK = 4 * REC;
constexpr int KVA = WR + 3 * GSLOT, KVB = KVA - 4 * CHUNK;

struct F4 { bf16x8 x0, x1, w0, w1; };
struct QFrag { bf16x8 x, q, k, v; };

template <int G>
__device__ __forceinline__ void qkv_steps(const lptr (&pa)[4], lptr pw, lptr pw2, QFrag& c, QFrag& n, f32x16& aq, f32x16& ak, f32x16& av,
                                          char* fx, u64 tag, int lane, bool shares) {
  if constexpr (G < 32) {
    constexpr int s = G >> 2, N = G + 1, ns = N >> 2;
    if constexpr ((G & 3) == 3) {
      if constexpr (s == 0) { if (!shares) wait_vm<7>(); }
      if constexpr (s == 0) wait_bar<9>(); else if constexpr (s == 1) wait_bar<4>(); else if constexpr (s == 2) wait_bar<1>(); else BAR();
      if constexpr (s == 1) { if (lane == 0) set_flag(fx, tag); }
    }
    const lptr w = ((ns & 2) ? pw2 : pw) + (ns & 1) * QSLOT + (N & 3) * 6144;
    MM(aq, c.q, c.x);
    if constexpr (N < 32) { n.x = lds_frag(pa[N & 3] + ns * 4096); n.q = lds_frag_tr(w, w + 256); }
    MM(ak, c.k, c.x);
    if constexpr (N < 32) { n.k = lds_frag_tr(w + 1024, w + 1024 + 256); n.v = lds_frag_tr(w + 2048, w + 2048 + 256); }
    MM(av, c.x, c.v);
    qkv_steps<G + 1>(pa, pw, pw2, n, c, aq, ak, av, fx, tag, lane, shares);
  }
}

#define EXP(x) "v_exp_f32 v" #x ", v" #x "\n\t"
#define CVT(d, a, b) "v_cvt_pk_bf16_f32 v" #d ", v" #a ", v" #b "\n\t"
#define MF(d, a, b, c) "v_mfma_f32_32x32x16_bf16 " d ", " a ", " b ", " c "\n\t"
#define HALF(c0, c1, c2, c3, c4, c5, c6, c7, c8, c9, c10, c11, c12, c13, c14, c15, QK, PV1, PV0, X0, X1) \
  QK EXP(c0) EXP(c1) EXP(c2) EXP(c3) PV1 X0 \
  EXP(c4) EXP(c5) CVT(80, c0, c1) CVT(81, c2, c3) EXP(c6) EXP(c7) EXP(c8) EXP(c9) CVT(82, c4, c5) CVT(83, c6, c7) \
  EXP(c10) EXP(c11) PV0 X1 \
  EXP(c12) EXP(c13) CVT(84, c8, c9) CVT(85, c10, c11) EXP(c14) EXP(c15) CVT(86, c12, c13) CVT(87, c14, c15)
#define TILE(off, S, X0, X1, X2, X3) \
  S \
  HALF(16, 17, 18, 19, 20, 21, 22, 23, 24, 25, 26, 27, 28, 29, 30, 31, \
       MF("v[32:47]", "v[68:71]", "v[92:95]", "v[96:111]") "ds_read_b128 v[68:71], v88 offset:" off "\n\t", \
       MF("v[112:127]", "v[76:79]", "v[84:87]", "v[112:127]") "ds_read_b128 v[76:79], v90 offset:" off "\n\t", \
       "s_waitcnt lgkmcnt(2)\n\t" MF("v[48:63]", "v[72:75]", "v[80:83]", "v[48:63]"), X0, X1) \
  HALF(32, 33, 34, 35, 36, 37, 38, 39, 40, 41, 42, 43, 44, 45, 46, 47, \
       "s_waitcnt lgkmcnt(0)\n\t" MF("v[16:31]", "v[68:71]", "v[64:67]", "v[0:15]"), \
       MF("v[48:63]", "v[76:79]", "v[84:87]", "v[48:63]"), \
       MF("v[112:127]", "v[72:75]", "v[80:83]", "v[112:127]") "ds_read_b128 v[72:75], v89 offset:" off "\n\t", X2, X3)
#define T_NONE(off) TILE(off, "", "", "", "", "")
#define T_KV(off, m, so) TILE(off, "s_mov_b32 m0, %[" #m "]\n\t", "", "buffer_load_dwordx4 %[voff], %[rkv], %[" #so "] offen sc1 lds\n\t", \
    "buffer_load_dwordx4 %[voff], %[rkv], %[" #so "] offen offset:1024 sc1 lds\n\t", "")
#define T_WD(off, st) TILE(off, "s_add_u32 m0, %[dm0], 16384*" #st "\n\ts_add_u32 %[t0], %[ds0], 131072*" #st "\n\t", "", \
    "buffer_load_dwordx4 %[vw1], %[rwd], %[t0] offen lds\n\ts_add_u32 %[t0], %[t0], 7168\n\t", "buffer_load_dwordx4 %[vw1], %[rwd], %[t0] offen offset:1024 lds\n\t", "")
#define T_KVI(off, MO, SO) TILE(off, "s_add_u32 m0, %[kvm], " #MO "\n\ts_add_u32 %[t0], %[kvs], " #SO "\n\t", "", \
    "buffer_load_dwordx4 %[voff], %[rkv], %[t0] offen sc1 lds\n\t", "buffer_load_dwordx4 %[voff], %[rkv], %[t0] offen offset:1024 sc1 lds\n\t", "")
#define VB(n) "s_waitcnt vmcnt(" #n ")\n\ts_barrier\n\t"
#define SB_ "s_barrier\n\t"
#define HLA(d) "buffer_load_dwordx4 %[" #d "], %[voff], %[rh], %[hs] offen sc1\n\t"
#define HLB(d) "buffer_load_dwordx4 %[" #d "], %[voff], %[rh], %[hs] offen offset:1024 sc1\n\ts_add_u32 %[hs], %[hs], 4096\n\t"
#define T_H2(off, da, db, dc, dd) TILE(off, "", HLA(da), HLB(db), HLA(dc), HLB(dd))
#define ATT_BEGIN "s_mov_b32 %[keep], m0\n\ts_waitcnt lgkmcnt(0)\n\ts_nop 2\n\t"
#define ATT_END "s_mov_b32 m0, %[keep]\n\ts_waitcnt lgkmcnt(0)\n\ts_nop 3\n\t"
#define ATT_STATE "+{v[48:63]}"(OA), "+{v[112:127]}"(OB), "+{v[16:31]}"(SA), "+{v[68:71]}"(K), "+{v[72:75]}"(V0), "+{v[76:79]}"(V1), \
                  "+{v[84:87]}"(P1), [t0] "=&s"(t0), [keep] "=&s"(keep)
#define ATT_CONST "{v[0:15]}"(CA), "{v[96:111]}"(CB), "{v[64:67]}"(QA), "{v[92:95]}"(QB), [voff] "v"(voff)
#define ATT_IN ATT_CONST, "{v88}"(aK), "{v89}"(aV0), "{v90}"(aV1)
#define ATT_CLOB "v32", "v33", "v34", "v35", "v36", "v37", "v38", "v39", "v40", "v41", "v42", "v43", "v44", "v45", "v46", "v47", \
                 "v80", "v81", "v82", "v83", "scc", "memory"

struct GFrag { bf16x8 x[4], g, u; };
__host__ __device__ constexpr int gslot(int s) { return s < 8 ? (s & 3) : (s - 8) % 3; }
template <int S, int KG>
__device__ __forceinline__ void gu_read(const lptr (&pa)[4], const lptr (&pp)[4], lptr pg, lptr pg3, GFrag& n, int part) {
  const lptr x = pa[(S & 1) * 2 + KG] + (S >> 1) * 4096, y = pp[(S & 1) * 2 + KG] + gslot(S) * GSLOT, w = (gslot(S) == 3 ? pg3 : pg + gslot(S) * GSLOT) + KG * 4096;
  if (part == 0) { n.x[0] = lds_frag(x); n.g = lds_frag_tr(w, w + 256); }
  if (part == 1) { n.x[1] = lds_frag(x + 32768); n.u = lds_frag_tr(w + 8192, w + 8192 + 256); }
  if (part == 2) { n.x[2] = lds_frag(y); n.x[3] = lds_frag(y + 2048); }
}
template <int S, class FILL>
__device__ __forceinline__ void gu_stages(const lptr (&pa)[4], const lptr (&pp)[4], lptr pg, lptr pg3, GFrag& f0, GFrag& f1, f32x16 (&g)[4], f32x16 (&u)[4], FILL fill) {
  if constexpr (S < 16) {
#define GU8(c, R0, R1, R2, D) do { MM(g[0], c.g, c.x[0]); R0; MM(g[1], c.g, c.x[1]); R1; MM(u[0], c.u, c.x[0]); R2; MM(u[1], c.u, c.x[1]); \
      MM(g[2], c.g, c.x[2]); D; MM(u[2], c.u, c.x[2]); MM(g[3], c.g, c.x[3]); MM(u[3], c.u, c.x[3]); } while (0)
    GU8(f0, (gu_read<S, 1>(pa, pp, pg, pg3, f1, 0)), (gu_read<S, 1>(pa, pp, pg, pg3, f1, 1)), (gu_read<S, 1>(pa, pp, pg, pg3, f1, 2)), );
    if constexpr (S < 15) {
      if constexpr (S == 0) wait_bar<6>(); else if constexpr (S == 2 || S == 14) wait_bar<0>(); else wait_bar<5>();
      GU8(f1, (gu_read<S + 1, 0>(pa, pp, pg, pg3, f0, 0)), (gu_read<S + 1, 0>(pa, pp, pg, pg3, f0, 1)), (gu_read<S + 1, 0>(pa, pp, pg, pg3, f0, 2)),
          if (S >= 8 && S <= 12) fill(S + 3));
    } else {
      GU8(f1, , , , );
    }
    gu_stages<S + 1>(pa, pp, pg, pg3, f0, f1, g, u, fill);
  }
}

__global__ __launch_bounds__(512, 2) void k_all(const char* __restrict__ gx, const char* __restrict__ gwqkv, const char* __restrict__ gwo,
                                                const char* __restrict__ gwg, const char* __restrict__ gwu, const char* __restrict__ gwd,
                                                char* __restrict__ ws, char* __restrict__ out) {
  extern __shared__ __attribute__((aligned(16))) char smem[];
  const lptr lds = (lptr)smem;
  const unsigned lds0 = (unsigned)(uintptr_t)lds;
  const int b = blockIdx.x;
  unsigned cpf;
  {
    u64 pc; unsigned o = ((b >> 3) * 16 + (threadIdx.x & 15)) * 128;
    asm volatile("s_getpc_b64 %1\nLk_here:\n\tv_min_u32 %2, Lk_tail-Lk_here-4, %2\n\tglobal_load_dword %0, %2, %1" : "=v"(cpf), "=&s"(pc), "+v"(o) :: "memory");
  }
  __builtin_amdgcn_sched_barrier(0);
  const int lane = threadIdx.x & 63, wid = RFL(threadIdx.x >> 6);
  const int xcd = b & 7, bi = b >> 3;
  const int rb = (xcd >> 1) * 8 + (bi & 7);
  const int rot = (bi + 2 * (bi >> 3)) & 7;
  const rsrc_t rx = mk_rsrc(gx, S_ * D_ * 2);
  u32x4 xr[8];
  {
    const unsigned vx = (threadIdx.x >> 3) * 1024 + (threadIdx.x & 7) * 16, sx = (unsigned)rb * 65536 + rot * 128;
#pragma unroll
    for (int i = 0; i < 8; ++i) BLOAD(xr[i], vx, rx, sx ^ (i * 128));
  }
  __builtin_amdgcn_sched_barrier(0);
  const int cb = (xcd & 1) * 4 + (bi >> 3);
  const int cq = (cb + 4 - (int)((uintptr_t)gwqkv >> 7)) & 7, co = (cb + 4 - (int)((uintptr_t)gwd >> 7)) & 7;
  const int head = xcd * 4 + (bi >> 3);
  const int r = lane & 31, h = lane >> 5;
  const unsigned trn = ((lane & 15) >> 2) * 64 + ((lane >> 4) & 1) * 32 + (lane & 3) * 8;
  const unsigned voff = lane * 16;

  const rsrc_t rw = mk_rsrc(gwqkv, D_ * 3 * D_ * 2), rxn = mk_rsrc(ws + WS_XN, 2097152);
  BAR();
  const unsigned vw3 = ((lane >> 2) & 7) * 3072 + (lane >> 5) * 64 + (lane & 3) * 16;
  auto issue_wk = [&](int s, int kb) {
    const unsigned so = (unsigned)(kb * 8) * 3072 + (s ^ rot) * (64 * 3072);
    dma16x3(rw, vw3, so + cq * 128, lds0 + WR + (s & 3) * QSLOT + kb * 3072);
  };
  auto issue_w = [&](int s) { issue_wk(s, wid); };
  issue_w(0); issue_w(1);
  unsigned pf;
#define TOUCH() asm volatile("global_load_dword %0, %1, off" : "=v"(pf) : "v"((h ? gwu : gwg) + (size_t)((b * 8 + wid) * 7 + (r < 7 ? r : 0)) * 128) : "memory")
  const u64 tag = read_tag(ws);
  float rs, mr;
  {
    const unsigned ONE2 = 0x3f803f80u;
    float s0 = 0.f, s1 = 0.f, q0 = 0.f, q1 = 0.f;
#define STAT(i) do { wait_vm1<13 - (i)>(xr[i]); \
      s0 = dot2(xr[i][0], ONE2, s0); q0 = dot2(xr[i][0], xr[i][0], q0); s1 = dot2(xr[i][1], ONE2, s1); q1 = dot2(xr[i][1], xr[i][1], q1); \
      s0 = dot2(xr[i][2], ONE2, s0); q0 = dot2(xr[i][2], xr[i][2], q0); s1 = dot2(xr[i][3], ONE2, s1); q1 = dot2(xr[i][3], xr[i][3], q1); \
      asm volatile("" : "+v"(s0), "+v"(s1), "+v"(q0), "+v"(q1)); } while (0)
    STAT(0); STAT(1); STAT(2); STAT(3); STAT(4); STAT(5); STAT(6); STAT(7);
    const float mean = sum8(s0 + s1) * (1.0f / 512.0f);
    rs = __builtin_amdgcn_rsqf(__builtin_fmaxf(sum8(q0 + q1) * (1.0f / 512.0f) - mean * mean, 0.0f) + 1e-5f);
    mr = -mean * rs;
  }
  const unsigned xo = (lane & 7) * 512 + ((((wid & 3) * 8 + (lane >> 3)) ^ (lane & 7)) * 16);
  const lptr pxw = lds + (wid >> 2) * 32768 + xo;
#pragma unroll
  for (int j = 0; j < 8; ++j) {
    u32x4 n;
#pragma unroll
    for (int i = 0; i < 4; ++i) n[i] = pk2(__builtin_fmaf(bf_lo(xr[j][i]), rs, mr), __builtin_fmaf(bf_hi(xr[j][i]), rs, mr));
    if (j < 2) BSTORE_DEV(n, rxn, (cb < 4 ? 0u : OOB) + (unsigned)(rb * 8 + (rot ^ j)) * 8192 + (wid >> 2) * 4096 + xo);
    *(LAS u32x4*)(pxw + j * 4096) = n;
  }
  issue_w(2); issue_w(3);
  TOUCH();
  const bool has_ffn = cb < 7;
  const int fb = cb * 2 + (rb & 1), w4 = wid & 3;
  const rsrc_t rg = mk_rsrc(gwg - 1024, D_ * F_ * 2 + 1024), ru = mk_rsrc(gwu - 1024, D_ * F_ * 2 + 1024);
  const unsigned vwf = ((lane >> 2) & 7) * (F_ * 2) + (lane >> 5) * 64 + (lane & 3) * 16;
  auto gu_w = [&](int s) {
    const unsigned so = (has_ffn ? 0u : OOB) + (unsigned)(w4 * 8) * (F_ * 2) + fb * 256 + (unsigned)(s ^ (2 * rot)) * (32 * F_ * 2) + 1024;
    const unsigned d = lds0 + WR + gslot(s) * GSLOT + w4 * 2048;
    dma16x2(rg, vwf, so, so + 128, d); dma16x2(ru, vwf, so, so + 128, d + 8192);
  };
  auto gu_x = [&](int s) {
    dma16_dev(rxn, voff, (has_ffn ? 0u : OOB) + (unsigned)((rb ^ 1) * 8 + ((s >> 1) ^ rot)) * 8192 + (w4 >> 1) * 4096 + (s & 1) * 2048 + (w4 & 1) * 1024,
              lds0 + WR + gslot(s) * GSLOT + 16384 + w4 * 1024);
  };
  auto gu_fill = [&](int s) { gu_w(s); gu_x(s); };
  u64 fl;
  lptr pa[4];
#pragma unroll
  for (int g = 0; g < 4; ++g) pa[g] = lds + (2 * g + h) * 512 + ((r ^ (2 * g + h)) * 16);

  const rsrc_t rkv = mk_rsrc(ws + WS_KV + (size_t)head * 131072, 131072), rh = mk_rsrc(ws + WS_H, 7340032);
  const unsigned vvo = h * 256 + (r & 15) * 16;
  const bf16x8 ONES = {0x3f80, 0x3f80, 0x3f80, 0x3f80, 0x3f80, 0x3f80, 0x3f80, 0x3f80};
  const bf16x8 PAD = ((r & 27) == 16) ? ONES : (bf16x8){};
  const int qt0 = (bi & 7) * 8 + w4 * 2;
  const unsigned ring = lds0 + (wid < 4 ? KVA : KVB), kt0 = wid < 4 ? 0 : 32;
  const unsigned kvm = ring + w4 * REC, kvs = (kt0 + w4) * 2048;
  u32x4 QAu, QBu, Ku, V0u;
  auto att_loads = [&] {
    const rsrc_t rq = mk_rsrc(ws + WS_Q, 2097152);
    BLOAD_DEV(QAu, voff, rq, (unsigned)(head * 64 + qt0) * 1024);
    BLOAD_DEV(QBu, voff, rq, (unsigned)(head * 64 + qt0 + 1) * 1024);
    BLOAD_DEV(Ku, voff, rkv, ((kt0 + 63) & 63) * 2048);
    BLOAD_DEV(V0u, vvo, rkv, ((kt0 + 63) & 63) * 2048 + 1024);
  };
  auto att_ring = [&] {
    dma16_dev(rkv, voff, kvs, kvm); dma16_dev(rkv, voff, kvs + 1024, kvm + 1024);
    dma16_dev(rkv, voff, kvs + 8192, kvm + CHUNK); dma16_dev(rkv, voff, kvs + 8192 + 1024, kvm + CHUNK + 1024);
    *(LAS bf16x8*)(lds + (ring - lds0) + (w4 * 4 + (r & 3)) * REC + 2048 + h * 16) = h ? (bf16x8){} : ONES;
  };
  LAS float* const mx = (LAS float*)(lds + KVA + 4 * CHUNK + w4 * 512) + lane;
  bf16x8 QA, QB, K, V0, V1, P1;
  f32x16 SA, CA, CB, OA, OB;
#define MV2(d, s) "v_mov_b64 v[" #d ":" #d "+1], " s "\n\t"
#define MV16(d, s) MV2(d, s) MV2(d + 2, s) MV2(d + 4, s) MV2(d + 6, s) MV2(d + 8, s) MV2(d + 10, s) MV2(d + 12, s) MV2(d + 14, s)
  auto att_init = [&](float ma, float mb) {
    QA = __builtin_bit_cast(bf16x8, QAu); QB = __builtin_bit_cast(bf16x8, QBu); K = __builtin_bit_cast(bf16x8, Ku);
    V0 = (r >= 16) ? PAD : __builtin_bit_cast(bf16x8, V0u);
    asm volatile(MV16(0, "%[ma]") MV2(76, "0") MV2(78, "0") MF("v[16:31]", "v[68:71]", "v[64:67]", "v[0:15]")
                 MV16(96, "%[mb]") MV16(48, "0") MV16(112, "0") MV2(84, "0") MV2(86, "0")
                 : "=&{v[0:15]}"(CA), "=&{v[96:111]}"(CB), "=&{v[16:31]}"(SA), "=&{v[48:63]}"(OA), "=&{v[112:127]}"(OB), "=&{v[76:79]}"(V1), "=&{v[84:87]}"(P1)
                 : "{v[68:71]}"(K), "{v[64:67]}"(QA), [ma] "v"((f32x2){ma, ma}), [mb] "v"((f32x2){mb, mb}));
  };
  const unsigned apad = ((r & 27) == 16) ? 2048 : 2064;
  const unsigned aK = ring + voff, aV0 = ring + (r < 16 ? 1024 + vvo : apad), aV1 = ring + (r < 16 ? 1536 + vvo : apad);
  unsigned t0, keep;
  const int rt = wid & 1, kq = wid >> 1;
  const rsrc_t rwd = mk_rsrc(gwd, F_ * D_ * 2), rwo = mk_rsrc(gwo, D_ * D_ * 2), ro = mk_rsrc(ws + WS_O, 2097152);
  const unsigned vw1 = ((lane >> 2) & 7) * 1024 + (lane >> 5) * 64 + (lane & 3) * 16;
#define DADDR(st) ((st) < 5 ? (st) * 16384 : (st) == 5 || (st) == 11 ? KVA : (st) < 11 ? ((st) - 6) * 16384 : (st) < 14 ? ((st) - 12) * 16384 : \
                   (st) == 14 ? 81920 : (st) == 15 ? 147456 : (st) == 16 ? 98304 : 32768)
  auto d_so = [&](int st, int pc) { return (unsigned)((st < 14 ? st : st - 14) * 128 + pc * 16) * 1024 + co * 128; };
  auto d_m = [&](int st, int pc) { return lds0 + DADDR(st) + pc * 2048; };
  auto issue_dp = [&](int st, int pc) { dma16x2(st < 14 ? rwd : rwo, vw1, d_so(st, pc), d_so(st, pc) + 8192, d_m(st, pc)); };
  auto h_so = [&](int e) { return (unsigned)((rb * 2 + rt) * 112 + e * 4 + kq * 2) * 1024; };
  const unsigned kvd = kvs + (wid < 4 ? 4 * 8192 : 0);
  u32x4 ha[14], hb[14], xv;
  const unsigned kve = wid < 4 ? OOB : kvd;
  LAS f32x4* ex = (LAS f32x4*)(lds + 147456 + w4 * 4096) + lane;
  LAS float* el = (LAS float*)(lds + 145408 + w4 * 512) + lane;
  if (cb >= 4) wait_vm<10>();
  WAIT_BAR(12);

  if (wid < 4) {
    {
      const int ch = wid >> 1;
      lptr par[4];
#pragma unroll
      for (int g = 0; g < 4; ++g) par[g] = pa[g] + rt * 32768;
      lptr pw = lds + WR + h * 3072 + ch * 512 + trn, pw2 = pw + 2 * QSLOT;
      asm volatile("" : "+v"(pw2));
      const int pair = cq * 2 + ch, tile = rb * 2 + rt;
      f32x16 aq = {}, ak = {}, av = {};
      QFrag f0, f1;
      f0.x = lds_frag(par[0]); f0.q = lds_frag_tr(pw, pw + 256); f0.k = lds_frag_tr(pw + 1024, pw + 1024 + 256); f0.v = lds_frag_tr(pw + 2048, pw + 2048 + 256);
      qkv_steps<0>(par, pw, pw2, f0, f1, aq, ak, av, ws + WS_FX + (rb * 8 + cb) * 8, tag, threadIdx.x, cb < 4);
#pragma unroll
      for (int i = 0; i < 16; ++i) aq[i] *= QSCALE;
      u32x4 qs[6] = {pack8(ak, 0), pack8(ak, 8), pack8(av, 0), pack8(av, 8), pack8(aq, 0), pack8(aq, 8)};
      asm volatile("" : "+v"(qs[0]), "+v"(qs[1]), "+v"(qs[2]), "+v"(qs[3]), "+v"(qs[4]), "+v"(qs[5]));
      const rsrc_t rq = mk_rsrc(ws + WS_Q, 2097152 + 4194304);
      const unsigned ko = 2097152 + (unsigned)((2 * pair) * 64 + ((tile + 63) & 63)) * 2048 + voff;
      const unsigned vo = 2097152 + (unsigned)(2 * pair + (r >> 4)) * 131072 + vvo;
      const unsigned qo = (unsigned)((2 * pair) * 64 + tile) * 1024 + voff;
      auto qst = [&](int i) {
        BSTORE_DEV(qs[i], rq, i == 0 ? ko : i == 1 ? ko + 131072 : i == 2 ? vo + ((tile + 63) & 63) * 2048 + 1024 : i == 3 ? vo + tile * 2048 + 1536 : i == 4 ? qo : qo + 65536);
      };
      auto fq_set = [&] {
        __builtin_amdgcn_raw_buffer_store_b64(__builtin_bit_cast(u32x2, tag), mk_rsrc(ws + WS_FQ, 8192),
                                              lane == 0 ? (unsigned)((cq * 2 + (wid >> 1)) * 64 + rb * 2 + (wid & 1)) * 8 : OOB, 0, 16);
      };
      if (__builtin_expect(has_ffn, 1)) {
        BAR();
        qst(0); qst(1); qst(2);
        BAR(); qst(3); qst(4); qst(5); gu_fill(4);
        BAR(); gu_fill(5);
        BAR(); gu_fill(6);
        WAIT_BAR(10); fq_set(); gu_fill(7);
        WAIT_BAR(10); gu_fill(8);
        WAIT_BAR(10); gu_fill(9);
        WAIT_BAR(10); gu_fill(10);
      } else {
        qst(0); qst(1); qst(2); qst(3); qst(4); qst(5);
        wait_vm<0>();
        fq_set();
#pragma unroll 1
        for (int i = 0; i < 8; ++i) BAR();
      }
    }
    if (!has_ffn) __builtin_amdgcn_s_sleep(32);
    asm volatile("global_load_dwordx2 %0, %1, off sc1" : "=v"(fl) : "v"(ws + WS_FQ + ((head >> 1) * 64 + lane) * 8) : "memory");
    WAIT_BAR(11);
    PIN_AFTER("s_waitcnt vmcnt(0)", "+v"(fl));
    if (__builtin_expect(!__all(fl == tag), 0)) wait_flags(ws + WS_FQ + (head >> 1) * 512, 64, tag, lane);
    u32x4 K1u;
    att_loads();
    BLOAD_DEV(K1u, voff, rkv, 0u);
    BAR();
    att_ring();
    PIN_AFTER("s_waitcnt vmcnt(4)", "+v"(QAu), "+v"(QBu), "+v"(Ku), "+v"(V0u), "+v"(K1u));
    float ma, mb;
    {
      const f32x16 z = {};
      const bf16x8 k0 = __builtin_bit_cast(bf16x8, Ku), k1 = __builtin_bit_cast(bf16x8, K1u), qa = __builtin_bit_cast(bf16x8, QAu), qb = __builtin_bit_cast(bf16x8, QBu);
      const f32x16 a0 = MFMA(k0, qa, z), a1 = MFMA(k1, qa, z), b0 = MFMA(k0, qb, z), b1 = MFMA(k1, qb, z);
      ma = a0[0]; mb = b0[0];
#pragma unroll
      for (int i = 0; i < 16; ++i) { ma = __builtin_fmaxf(__builtin_fmaxf(ma, a0[i]), a1[i]); mb = __builtin_fmaxf(__builtin_fmaxf(mb, b0[i]), b1[i]); }
      asm volatile("" : "+v"(ma), "+v"(mb));
    }
    BAR();
    ma = -(xhalf_max(ma) + MBIAS); mb = -(xhalf_max(mb) + MBIAS);
    mx[0] = ma; mx[64] = mb;
    att_init(ma, mb);
    asm volatile(ATT_BEGIN "s_setprio 2\n\t"
                 VB(2) T_KVI("0", 16640, 16384) T_NONE("2080") SB_ T_NONE("4160") T_NONE("6240")
                 VB(2) T_KVI("8320", 24960, 24576) T_NONE("10400") SB_ T_NONE("12480") T_NONE("14560")
                 VB(2) T_KVI("16640", 0, 32768) T_NONE("18720") SB_ T_NONE("20800") T_NONE("22880")
                 VB(2) T_KVI("24960", 8320, 40960) T_NONE("27040") SB_ T_NONE("29120") T_NONE("31200")
                 ATT_END
                 : ATT_STATE : ATT_IN, [rkv] "s"(rkv), [kvm] "s"(kvm), [kvs] "s"(kvs) : ATT_CLOB);
    __builtin_amdgcn_s_setprio(0);
    wait_vm<2>();
  } else {
    auto w2 = [&](int s) { issue_wk(s, w4 * 2); issue_wk(s, w4 * 2 + 1); };
    if (cb >= 4) wait_vm<7>();
    WAIT_BAR(9);  w2(4);
    WAIT_BAR(10); w2(5);
    WAIT_BAR(13); w2(6);
    WAIT_BAR(12); w2(7);
    WAIT_BAR(12);
    asm volatile("global_load_dwordx2 %0, %1, off sc1" : "=v"(fl) : "v"(ws + WS_FX + ((rb ^ 1) * 8 + (lane & 3)) * 8) : "memory");
    gu_w(0);
    if (!has_ffn) wait_vm<7>();
    WAIT_BAR(11); gu_w(1);
    if (!has_ffn) wait_vm<0>();
    PIN_AFTER("s_waitcnt vmcnt(8) lgkmcnt(0)\n\ts_barrier", "+v"(fl));
    const bool xok = __all(fl == tag) || !has_ffn;
    if (xok) gu_x(0);
    gu_w(2);
    if (xok) { gu_x(1); gu_x(2); }
    BAR();
    if (__builtin_expect(xok, 1)) { gu_fill(3); wait_vm<11>(); }
    else { wait_flags(ws + WS_FX + (rb ^ 1) * 64, 4, tag, lane); gu_x(0); gu_x(1); gu_x(2); gu_fill(3); wait_vm<7>(); }
    BAR();
    f32x16 g[4], u[4];
    if (has_ffn) {
      lptr pp[4];
#pragma unroll
      for (int i = 0; i < 4; ++i) { pp[i] = pa[i] + (WR + 16384 - (i >> 1) * 2048); asm volatile("" : "+v"(pp[i])); g[i] = (f32x16){}; u[i] = (f32x16){}; }
      const lptr pg = lds + WR + h * 2048 + (w4 >> 1) * 1024 + (w4 & 1) * 512 + trn;
      lptr pg3 = pg + 3 * GSLOT;
      asm volatile("" : "+v"(pg3));
      GFrag f0, f1;
      gu_read<0, 0>(pa, pp, pg, pg3, f0, 0); gu_read<0, 0>(pa, pp, pg, pg3, f0, 1); gu_read<0, 0>(pa, pp, pg, pg3, f0, 2);
      gu_stages<0>(pa, pp, pg, pg3, f0, f1, g, u, gu_fill);
    } else {
      unsigned d[5];
      wait_vm<0>();
#pragma unroll 1
      for (int i = 0; i < 11; ++i) BAR();
#pragma unroll
      for (int i = 0; i < 5; ++i) {
        const int t = (rb * 4 + w4) * 144 + i * 32 + (r + i * 32 < 144 ? r : 0);
        const char* p = (t < 14336) ? gwd + (size_t)t * 128 : gwo + (size_t)(t - 14336) * 128;
        asm volatile("global_load_dword %0, %1, off" : "=v"(d[i]) : "v"(p) : "memory");
      }
#pragma unroll 1
      for (int i = 11; i < 15; ++i) BAR();
      PIN_AFTER("s_waitcnt vmcnt(0)", "+v"(d[0]), "+v"(d[1]), "+v"(d[2]), "+v"(d[3]), "+v"(d[4]));
    }
    BAR();
    __builtin_amdgcn_s_setprio(3);
    att_loads(); att_ring();
    auto swiglu = [&](int i) {
#pragma unroll
      for (int j = 0; j < 16; ++j) g[i][j] = g[i][j] * u[i][j] * __builtin_amdgcn_rcpf(1.0f + __builtin_amdgcn_exp2f(g[i][j] * -1.4426950408889634f));
      const unsigned ho = (unsigned)(((rb ^ (i >> 1)) * 2 + (i & 1)) * 112 + fb * 8 + w4 * 2) * 1024 + voff;
      BSTORE_DEV(pack8(g[i], 0), rh, ho);
      BSTORE_DEV(pack8(g[i], 8), rh, ho + 1024);
    };
    if (has_ffn) swiglu(0);
    BAR();
    if (has_ffn) { swiglu(1); swiglu(2); }
    BAR();
    const float ma = mx[0], mb = mx[64];
    if (has_ffn) swiglu(3); else wait_vm<0>();
    PIN_AFTER("s_waitcnt vmcnt(8)", "+v"(QAu), "+v"(QBu), "+v"(Ku), "+v"(V0u));
    att_init(ma, mb);
  }
  asm volatile("" :: "v"(pf), "v"(cpf));
  asm volatile(ATT_BEGIN "s_setprio 0\n\t" SB_
               T_KVI("0", 16640, 16384) T_WD("2080", 0) T_WD("4160", 1) T_WD("6240", 2)
               VB(8)
               "buffer_store_dwordx2 %[tag], %[fho], %[rfl], 0 offen sc1\n\t"
               T_KVI("8320", 24960, 24576) T_WD("10400", 3) T_WD("12480", 4) T_NONE("14560")
               VB(7)
               T_KV("16640", kvm, s5_) T_NONE("18720")
               TILE("20800", "", "buffer_load_dwordx2 %[fl], %[fhi], %[rfl], 0 offen sc1\n\t", "", "", "") T_NONE("22880")
               ATT_END
               : ATT_STATE, [fl] "=&v"(fl)
               : ATT_IN, [rkv] "s"(rkv), [rwd] "s"(rwd), [vw1] "v"(vw1), [kvm] "s"(kvm), [kvs] "s"(kvd),
                 [dm0] "s"(RFL(d_m(0, wid))), [ds0] "s"(RFL(d_so(0, wid))), [s5_] "s"(kve + 4 * 8192), [rfl] "s"(mk_rsrc(ws + WS_FH, 2048)), [tag] "v"(tag),
                 [fho] "v"(threadIdx.x == 0 ? (unsigned)(rb * 8 + cb) * 8 : OOB), [fhi] "v"((unsigned)((rb >> 1) * 16 + (lane & 15)) * 8) : ATT_CLOB);
  PIN_AFTER("s_waitcnt vmcnt(0)", "+v"(fl));
  if (__builtin_expect(!__all(fl == tag), 0)) wait_flags(ws + WS_FH + (rb >> 1) * 128, 16, tag, lane);
#define H_OUT(n, st) [a##n] "=&v"(ha[st]), [b##n] "=&v"(hb[st])
  unsigned hs = h_so(0) + (wid >> 2) * OOB;
  f32x4 p0, p1, p2, p3;
  float pl0, pl1;
  asm volatile(ATT_BEGIN SB_
               TILE("24960", "s_add_u32 m0, %[kvm], 8320\n\ts_add_u32 %[t0], %[kvs], 40960\n\t", "buffer_load_dwordx4 %[voff], %[rkv], %[t0] offen sc1 lds\n\t",
                    "buffer_load_dwordx4 %[voff], %[rkv], %[t0] offen offset:1024 sc1 lds\n\t", HLA(a0), HLB(b0))
               T_H2("27040", a1, b1, a2, b2) T_H2("29120", a3, b3, a4, b4) T_H2("31200", a5, b5, a6, b6)
               "s_cmp_lt_u32 %[wid], 4\n\ts_cbranch_scc1 L_skip_%=\n\t"
               "s_setprio 2\n\t"
               SB_ T_KVI("0", 16640, 49152) T_NONE("2080") SB_ T_NONE("4160") T_NONE("6240")
               VB(2) T_KVI("8320", 24960, 57344)
               TILE("10400", "", "", "", "ds_read_b128 %[p0], %[ex]\n\tds_read_b128 %[p1], %[ex] offset:1024\n\tds_read_b32 %[pl0], %[el]\n\t", "")
               SB_
               TILE("12480", "", "", "", "ds_read_b128 %[p2], %[ex] offset:2048\n\tds_read_b128 %[p3], %[ex] offset:3072\n\tds_read_b32 %[pl1], %[el] offset:256\n\t", "")
               T_NONE("14560")
               VB(2) T_NONE("16640") T_NONE("18720") SB_ T_NONE("20800") T_NONE("22880")
               VB(0) T_NONE("24960") T_NONE("27040") SB_ T_NONE("29120") T_NONE("31200")
               "L_skip_%=:\n\t"
               ATT_END
               : ATT_STATE, H_OUT(0, 0), H_OUT(1, 1), H_OUT(2, 2), H_OUT(3, 3), H_OUT(4, 4), H_OUT(5, 5), H_OUT(6, 6),
                 [p0] "=&v"(p0), [p1] "=&v"(p1), [p2] "=&v"(p2), [p3] "=&v"(p3), [pl0] "=&v"(pl0), [pl1] "=&v"(pl1), [hs] "+s"(hs)
               : ATT_IN, [rkv] "s"(rkv), [kvm] "s"(kvm), [kvs] "s"(kve), [wid] "s"(wid), [ex] "v"(ex), [el] "v"(el),
                 [rh] "s"(rh) : ATT_CLOB);

  const int t = threadIdx.x;
  const unsigned off = (unsigned)(rb * 64 + (t >> 3)) * 1024 + co * 128 + (t & 7) * 16;
  f32x16 d0, d1;
  F4 e0, e1;
#define D_PTR(pd, a) ((a) < 65536 ? pd[0] + (a) : (a) < KVA ? pd[1] + ((a) - 65536) : pd[2] + ((a) - KVA))
#define D_READ_A(n, w_) do { n.w0 = lds_frag_tr(w_, w_ + 1024); n.w1 = lds_frag_tr(w_ + 512, w_ + 1536); } while (0)
#define D_READ_B(n, w_) do { n.x0 = lds_frag_tr(w_ + 2048, w_ + 3072); n.x1 = lds_frag_tr(w_ + 2560, w_ + 3584); } while (0)
#define D_STEPX(c, n, nxt, A, B, RD, X, Y) do { \
    MM(d0, c.w0, __builtin_bit_cast(bf16x8, A)); if (RD) D_READ_A(n, (nxt)); MM(d1, c.w1, __builtin_bit_cast(bf16x8, A)); if (RD) D_READ_B(n, (nxt)); \
    MM(d0, c.x0, __builtin_bit_cast(bf16x8, B)); X; MM(d1, c.x1, __builtin_bit_cast(bf16x8, B)); Y; } while (0)
#define H_LOAD(e, so) do { BLOAD_DEV(ha[e], voff, rh, so); BLOAD_DEV(hb[e], voff, rh, (so) + 1024); } while (0)
  PIN_AFTER("s_waitcnt vmcnt(0)", "+v"(ha[0]), "+v"(ha[1]), "+v"(ha[2]), "+v"(ha[3]), "+v"(ha[4]), "+v"(ha[5]), "+v"(ha[6]),
            "+v"(hb[0]), "+v"(hb[1]), "+v"(hb[2]), "+v"(hb[3]), "+v"(hb[4]), "+v"(hb[5]), "+v"(hb[6]));
  OB = MFMA(V1, P1, OB);
  lptr pe[3];
  pe[0] = lds + (kq & 1) * 4096 + h * 256 + trn; pe[1] = pe[0] + 65536; pe[2] = pe[0] + KVA;
  asm volatile("" : "+v"(pe[0]), "+v"(pe[1]), "+v"(pe[2]));
#define EP(e) D_PTR(pe, DADDR((e) >> 1) + ((e) & 1) * 8192)
#define RADDR(q) ((q) == 0 ? 49152 : (q) == 1 ? 65536 : (q) == 2 ? KVA : 0)
  auto put_partial = [&](int base) {
    const lptr red = lds + base + (rt * 32 + r) * 256;
#pragma unroll
    for (int i = 0; i < 4; ++i) {
      *(LAS f32x4*)(red + (((2 * i + h) ^ (r & 15)) * 16)) = V4(d0, i);
      *(LAS f32x4*)(red + (((8 + 2 * i + h) ^ (r & 15)) * 16)) = V4(d1, i);
    }
  };
  if (wid < 4) {
    d0 = d1 = (f32x16){};
#define E_DMA(st, i) issue_dp(st, wid * 2 + (i))
#define E_STAGE(k, X0, Y0, X1, Y1) do { D_STEPX(e0, e1, EP(2 * (k) + 1), ha[(2 * (k)) % 14], hb[(2 * (k)) % 14], true, X0, Y0); \
      D_STEPX(e1, e0, EP(2 * (k) + 2), ha[(2 * (k) + 1) % 14], hb[(2 * (k) + 1) % 14], (k) < 13, X1, Y1); } while (0)
#define HE(e) H_LOAD((e) % 14, h_so(e))
#define HP(e) "+v"(ha[(e) % 14]), "+v"(hb[(e) % 14])
#define W2(k, sa, sb, e) do { E_STAGE(k, E_DMA(sa, 0), E_DMA(sa, 1), E_DMA(sb, 0), E_DMA(sb, 1)); HE(e); } while (0)
#define W1(k, sa, e) do { E_STAGE(k, E_DMA(sa, 0), E_DMA(sa, 1), HE(e), HE((e) + 1)); HE((e) + 2); } while (0)
#define FB(N, ...) PIN_AFTER("s_waitcnt vmcnt(" #N ") lgkmcnt(0)\n\ts_barrier", __VA_ARGS__)
    D_READ_A(e0, EP(0)); D_READ_B(e0, EP(0));
    E_STAGE(0, HE(7), HE(8), HE(9), ); E_STAGE(1, HE(10), , HE(11), );
    BAR();
    ex[0] = V4(OA, 0); ex[64] = V4(OA, 1); ex[128] = V4(OB, 0); ex[192] = V4(OB, 1); el[0] = OA[8]; el[64] = OB[8];
    W2(2, 5, 6, 12);   FB(18, HP(7));
    W2(3, 7, 8, 13);   FB(16, HP(8), HP(9), HP(10), HP(11));
    W1(4, 9, 14);      WAIT_BAR(22);
    W2(5, 10, 14, 17); FB(20, HP(12), HP(13));
    W2(6, 11, 15, 18); FB(22, HP(14), HP(15));
    W1(7, 12, 19);     FB(20, HP(16), HP(17));
    W1(8, 13, 22);     FB(6, HP(18), HP(19), HP(20), HP(21));
    E_STAGE(9, HE(25), HE(26), HE(27), E_DMA(16, 0));
    E_STAGE(10, E_DMA(16, 1), E_DMA(17, 0), E_DMA(17, 1), );
    PIN_AFTER("s_waitcnt vmcnt(8)", HP(22), HP(23), HP(24), HP(25), HP(26), HP(27));
    E_STAGE(11, , , , ); E_STAGE(12, , , , ); E_STAGE(13, , , , );
    if (b == 193 && wid == 0) {
#pragma unroll 1
      for (int i = 0; i < 4; ++i) wait_flags(ws + WS_FH + i * 512, 64, tag, lane);
      if (lane == 0) set_flag(ws + WS_EPOCH, tag);
    }
  } else {
    auto store_o = [&](const f32x16& O, int i) {
      float il = __builtin_amdgcn_rcpf(O[8]);
      asm volatile("s_nop 0" : "+v"(il));
      const u32x4 v = {pk2(mul1(O[0], il), mul1(O[1], il)), pk2(mul1(O[2], il), mul1(O[3], il)), pk2(mul1(O[4], il), mul1(O[5], il)), pk2(mul1(O[6], il), mul1(O[7], il))};
      BSTORE_DEV(v, ro, (unsigned)((qt0 + i) * 32 + head) * 1024 + voff);
    };
    auto check_o = [&](f32x16 O, const bf16x8& Q, int i) {
      float mag = __builtin_fabsf(O[0]);
#pragma unroll
      for (int j = 1; j < 8; ++j) mag += __builtin_fabsf(O[j]);
      const bool bad = (int)!(O[8] < 0x1p120f) | (int)!(mag < 0x1p126f) | (int)!(xhalf_max(mag) > 0x1p-100f);
      if (__builtin_expect(__any(bad), 0)) {
        const f32x16 z = {};
        float mt = -__builtin_inff();
#pragma unroll 1
        for (int tt = 0; tt < 64; ++tt) {
          const f32x16 s = MFMA(__builtin_bit_cast(bf16x8, __builtin_amdgcn_raw_buffer_load_b128(rkv, voff, tt * 2048, 16)), Q, z);
#pragma unroll
          for (int j = 0; j < 16; ++j) mt = __builtin_fmaxf(mt, s[j]);
        }
        mt = xhalf_max(mt);
        O = z;
#pragma unroll 1
        for (int tt = 0; tt < 64; ++tt) {
          f32x16 s = MFMA(__builtin_bit_cast(bf16x8, __builtin_amdgcn_raw_buffer_load_b128(rkv, voff, ((tt + 63) & 63) * 2048, 16)), Q, z);
          bf16x8 v0 = __builtin_bit_cast(bf16x8, __builtin_amdgcn_raw_buffer_load_b128(rkv, vvo, ((tt + 63) & 63) * 2048 + 1024, 16));
          bf16x8 v1 = __builtin_bit_cast(bf16x8, __builtin_amdgcn_raw_buffer_load_b128(rkv, vvo, tt * 2048 + 1536, 16));
          if (r >= 16) { v0 = PAD; v1 = PAD; }
#pragma unroll
          for (int j = 0; j < 16; ++j) s[j] = __builtin_amdgcn_exp2f(s[j] - mt);
          O = MFMA(v0, __builtin_bit_cast(bf16x8, pack8(s, 0)), O);
          O = MFMA(v1, __builtin_bit_cast(bf16x8, pack8(s, 8)), O);
        }
        asm volatile("s_nop 15" : "+v"(O));
        wait_vm<0>();
        store_o(O, i);
      }
    };
#pragma unroll
    for (int j = 0; j < 4; ++j) { OA[j] = add1(OA[j], p0[j]); OA[4 + j] = add1(OA[4 + j], p1[j]); }
    OA[8] += pl0;
    store_o(OA, 0);
    asm volatile("s_nop 3" : "+v"(OB) :: "memory");
#pragma unroll
    for (int j = 0; j < 4; ++j) { OB[j] = add1(OB[j], p2[j]); OB[4 + j] = add1(OB[4 + j], p3[j]); }
    OB[8] += pl1;
    store_o(OB, 1);
    check_o(OA, QA, 0); check_o(OB, QB, 1);
    wait_vm<0>();
    __builtin_amdgcn_raw_buffer_store_b64(__builtin_bit_cast(u32x2, tag), mk_rsrc(ws + WS_FO, 16384),
                                          lane < 2 ? (unsigned)((qt0 + lane) * 32 + head) * 8 : OOB, 0, 16);
    d0 = d1 = (f32x16){};
    asm volatile("" : "+v"(d0), "+v"(d1));
    __builtin_amdgcn_s_setprio(0);
  }
  if (wid < 4) wait_vm<0>();
  BAR();
  {
    BLOAD(xv, off, rx, 0u);
    const lptr pw = lds + (kq == 0 ? DADDR(14) : kq == 1 ? DADDR(15) : kq == 2 ? DADDR(16) : DADDR(17)) + h * 256 + trn;
    F4 wf[4];
#pragma unroll
    for (int j = 0; j < 4; ++j) { D_READ_A(wf[j], pw + j * 4096); D_READ_B(wf[j], pw + j * 4096); }
    wait_flags(ws + WS_FO + ((rb * 2 + rt) * 32 + kq * 8) * 8, 8, tag, lane);
    PIN_AFTER("s_waitcnt vmcnt(0)", "+v"(xv));
    u32x4 oa[4], ob[4];
    const unsigned obase = (unsigned)((rb * 2 + rt) * 32 + kq * 8) * 1024;
#pragma unroll
    for (int j = 0; j < 4; ++j) { BLOAD_DEV(oa[j], voff, ro, obase + j * 2048); BLOAD_DEV(ob[j], voff, ro, obase + j * 2048 + 1024); }
#define O_STEP(j) do { wait_vm2<6 - 2 * (j)>(oa[j], ob[j]); D_STEPX(wf[j], e0, pw, oa[j], ob[j], false, , ); } while (0)
    O_STEP(0); O_STEP(1); O_STEP(2); O_STEP(3);
    put_partial(kq == 0 ? RADDR(0) : kq == 1 ? RADDR(1) : kq == 2 ? RADDR(2) : RADDR(3));
  }
  BAR();
  f32x4 s0 = {bf_lo(xv[0]), bf_hi(xv[0]), bf_lo(xv[1]), bf_hi(xv[1])}, s1 = {bf_lo(xv[2]), bf_hi(xv[2]), bf_lo(xv[3]), bf_hi(xv[3])};
  {
    const int row = t >> 3, c = (t & 7) * 2;
    const lptr rd0 = lds + row * 256 + ((c ^ (row & 15)) * 16), rd1 = lds + row * 256 + (((c + 1) ^ (row & 15)) * 16);
#pragma unroll
    for (int p = 0; p < 4; ++p) {
      const int o = RADDR(p);
      s0 += *(const LAS f32x4*)(rd0 + o); s1 += *(const LAS f32x4*)(rd1 + o);
    }
  }
  __builtin_amdgcn_raw_buffer_store_b128((u32x4){pk2(s0[0], s0[1]), pk2(s0[2], s0[3]), pk2(s1[0], s1[1]), pk2(s1[2], s1[3])}, mk_rsrc(out, S_ * D_ * 2), off, 0, 0);
  asm volatile("Lk_tail:");
}

extern "C" void kernel_launch(void* const* d_in, const int* in_sizes, int n_in, void* d_out, int out_size, void* d_ws,
                              size_t ws_size, hipStream_t stream) {
  (void)hipFuncSetAttribute((const void*)k_all, hipFuncAttributeMaxDynamicSharedMemorySize, LDS_BYTES);
  k_all<<<256, 512, LDS_BYTES, stream>>>((const char*)d_in[0], (const char*)d_in[1], (const char*)d_in[2], (const char*)d_in[4],
                                         (const char*)d_in[5], (const char*)d_in[6], (char*)d_ws, (char*)d_out);
}
```

```cpp
#include <hip/hip_runtime.h>
#include <hip/hip_bf16.h>
#include <stdint.h>

typedef __attribute__((ext_vector_type(8))) short bf16x8;
typedef __attribute__((ext_vector_type(4))) short s16x4;
typedef __attribute__((ext_vector_type(16))) float f32x16;
typedef __attribute__((ext_vector_type(4))) float f32x4;
typedef __attribute__((ext_vector_type(2))) float f32x2;
typedef __attribute__((ext_vector_type(4))) unsigned u32x4;
typedef __attribute__((ext_vector_type(2))) unsigned u32x2;
typedef __attribute__((ext_vector_type(2))) __bf16 bf2;
typedef unsigned long long u64;
typedef __amdgpu_buffer_rsrc_t rsrc_t;
#define LAS __attribute__((address_space(3)))
#define GAS __attribute__((address_space(1)))
typedef LAS char* lptr;

#define MFMA(a, b, c) __builtin_amdgcn_mfma_f32_32x32x16_bf16((a), (b), (c), 0, 0, 0)
#define MM(c, a, b) do { __builtin_amdgcn_sched_barrier(0); c = MFMA(a, b, c); asm volatile("" : "+v"(c) :: "memory"); \
                         __builtin_amdgcn_sched_barrier(0); } while (0)
#define RFL(x) __builtin_amdgcn_readfirstlane(x)

constexpr int S_ = 2048, D_ = 512, F_ = 1792;
constexpr size_t WS_EPOCH = 0;
constexpr size_t WS_FH = 2048;
constexpr size_t WS_FQ = 16384;
constexpr size_t WS_FX = 49152;
constexpr size_t WS_FO = 32768;
constexpr size_t WS_Q    = 65536;
constexpr size_t WS_KV   = WS_Q + 2097152;
constexpr size_t WS_O    = WS_KV + 4194304;
constexpr size_t WS_XN   = WS_O + 2097152;
constexpr size_t WS_H    = WS_XN + 2097152;
constexpr float QSCALE = 0.25f * 1.4426950408889634f;
constexpr float MBIAS = 80.0f;
constexpr int LDS_BYTES = 163840;
constexpr unsigned SPIN_CAP = 1u << 22;
constexpr unsigned OOB = 0x7ff00000u;

__device__ __forceinline__ rsrc_t mk_rsrc(const void* p, unsigned bytes) {
  return __builtin_amdgcn_make_buffer_rsrc((void*)p, (short)0, (int)bytes, 0x00020000);
}
__device__ __forceinline__ void dma16_dev(rsrc_t r, unsigned voff, unsigned soff, unsigned lds) {
  unsigned keep;
  asm volatile("s_mov_b32 %0, m0\n\ts_mov_b32 m0, %1\n\ts_nop 0\n\tbuffer_load_dwordx4 %2, %3, %4 offen sc1 lds\n\ts_mov_b32 m0, %0"
               : "=&s"(keep) : "s"(lds), "v"(voff), "s"(r), "s"(soff) : "memory");
}
__device__ __forceinline__ void dma16x3(rsrc_t r, unsigned voff, unsigned soff, unsigned lds) {
  unsigned keep;
  asm volatile("s_mov_b32 %0, m0\n\ts_mov_b32 m0, %1\n\ts_nop 0\n\tbuffer_load_dwordx4 %2, %3, %4 offen lds\n\t"
               "buffer_load_dwordx4 %2, %3, %4 offen offset:1024 lds\n\tbuffer_load_dwordx4 %2, %3, %4 offen offset:2048 lds\n\ts_mov_b32 m0, %0"
               : "=&s"(keep) : "s"(lds), "v"(voff), "s"(r), "s"(soff) : "memory");
}
__device__ __forceinline__ float add1(float a, float b) { float d; asm("v_add_f32 %0, %1, %2" : "=v"(d) : "v"(a), "v"(b)); return d; }
__device__ __forceinline__ float mul1(float a, float b) { float d; asm("v_mul_f32 %0, %1, %2" : "=v"(d) : "v"(a), "v"(b)); return d; }
#define BLOAD(dst, voff, r, soff) asm volatile("buffer_load_dwordx4 %0, %1, %2, %3 offen" : "=v"(dst) : "v"(voff), "s"(r), "s"(soff) : "memory")
#define BLOAD_DEV(dst, voff, r, soff) asm volatile("buffer_load_dwordx4 %0, %1, %2, %3 offen sc1" : "=v"(dst) : "v"(voff), "s"(r), "s"(soff) : "memory")
#define BSTORE_DEV(val, r, voff) __builtin_amdgcn_raw_buffer_store_b128((val), (r), (voff), 0, 16)
#define WAIT_BAR(N) asm volatile("s_waitcnt vmcnt(" #N ") lgkmcnt(0)\n\ts_barrier" ::: "memory")
#define BAR() asm volatile("s_waitcnt lgkmcnt(0)\n\ts_barrier" ::: "memory")
template <int N> __device__ __forceinline__ void wait_bar() { asm volatile("s_waitcnt vmcnt(%0) lgkmcnt(0)\n\ts_barrier" :: "n"(N) : "memory"); }
template <int N> __device__ __forceinline__ void wait_vm2(u32x4& a, u32x4& b) {
  asm volatile("s_waitcnt vmcnt(%0)" :: "n"(N) : "memory"); __builtin_amdgcn_sched_barrier(0); asm volatile("" : "+v"(a), "+v"(b) :: "memory");
}
template <int N> __device__ __forceinline__ void wait_vm1(u32x4& a) {
  asm volatile("s_waitcnt vmcnt(%0)" :: "n"(N) : "memory"); __builtin_amdgcn_sched_barrier(0); asm volatile("" : "+v"(a) :: "memory");
}

__device__ __forceinline__ unsigned pk2(float lo, float hi) {
  f32x2 v = {lo, hi};
  return __builtin_bit_cast(unsigned, __builtin_convertvector(v, bf2));
}
__device__ __forceinline__ float bf_lo(unsigned u) { return __uint_as_float(u << 16); }
__device__ __forceinline__ float bf_hi(unsigned u) { return __uint_as_float(u & 0xffff0000u); }
__device__ __forceinline__ float xhalf_max(float v) {
  auto r = __builtin_amdgcn_permlane32_swap(__float_as_uint(v), __float_as_uint(v), false, false);
  return __builtin_fmaxf(__uint_as_float(r[0]), __uint_as_float(r[1]));
}
__device__ __forceinline__ bf16x8 lds_frag(lptr p) { return *(const LAS bf16x8*)p; }
typedef short v4i16_t __attribute__((ext_vector_type(4)));
__device__ __forceinline__ bf16x8 lds_frag_tr(lptr p0, lptr p1) {
  s16x4 a = __builtin_bit_cast(s16x4, __builtin_amdgcn_ds_read_tr16_b64_v4i16((LAS v4i16_t*)p0));
  s16x4 b = __builtin_bit_cast(s16x4, __builtin_amdgcn_ds_read_tr16_b64_v4i16((LAS v4i16_t*)p1));
  return (bf16x8){a[0], a[1], a[2], a[3], b[0], b[1], b[2], b[3]};
}
__device__ __forceinline__ float dot2(unsigned a, unsigned b, float c) {
  return __builtin_amdgcn_fdot2_f32_bf16(__builtin_bit_cast(bf2, a), __builtin_bit_cast(bf2, b), c, false);
}
__device__ __forceinline__ u32x4 pack8(const f32x16& a, int o) {
  return (u32x4){pk2(a[o], a[o + 1]), pk2(a[o + 2], a[o + 3]), pk2(a[o + 4], a[o + 5]), pk2(a[o + 6], a[o + 7])};
}
template <int CTRL> __device__ __forceinline__ float dpp_add(float x) {
  return x + __builtin_bit_cast(float, __builtin_amdgcn_mov_dpp(__builtin_bit_cast(int, x), CTRL, 0xf, 0xf, true));
}
__device__ __forceinline__ float sum8(float x) { return dpp_add<0x141>(dpp_add<0x4E>(dpp_add<0xB1>(x))); }

__device__ __forceinline__ u64 read_tag(const char* ws) {
  u64 e;
  asm volatile("s_load_dwordx2 %0, %1, 0x0\n\ts_waitcnt lgkmcnt(0)" : "=s"(e) : "s"(ws + WS_EPOCH) : "memory");
  e = (e ^ (e >> 31)) * 0x9E3779B97F4A7C15ull + 0x632BE59BD9B4E019ull;
  return e ^ (e >> 29);
}
__device__ __forceinline__ void set_flag(char* p, u64 tag) { __hip_atomic_store((GAS u64*)p, tag, __ATOMIC_RELAXED, __HIP_MEMORY_SCOPE_AGENT); }
__device__ __forceinline__ void wait_flags(const char* p, int n, u64 tag, int lane) {
  GAS u64* f = (GAS u64*)p + (lane < n ? lane : 0);
#pragma unroll 1
  for (unsigned spins = 0; spins < SPIN_CAP; ++spins) {
    if (__all(__hip_atomic_load(f, __ATOMIC_RELAXED, __HIP_MEMORY_SCOPE_AGENT) == tag)) break;
    __builtin_amdgcn_s_sleep(4);
  }
  asm volatile("" ::: "memory");
}


__device__ __forceinline__ void dma16x2(rsrc_t r, unsigned voff, unsigned soff0, unsigned soff1, unsigned lds) {
  unsigned keep;
  asm volatile("s_mov_b32 %0, m0\n\ts_mov_b32 m0, %1\n\ts_nop 0\n\tbuffer_load_dwordx4 %2, %3, %4 offen lds\n\t"
               "buffer_load_dwordx4 %2, %3, %5 offen offset:1024 lds\n\ts_mov_b32 m0, %0"
               : "=&s"(keep) : "s"(lds), "v"(voff), "s"(r), "s"(soff0), "s"(soff1 - 1024u) : "memory");
}
template <int N> __device__ __forceinline__ void wait_vm() { asm volatile("s_waitcnt vmcnt(%0)" :: "n"(N) : "memory"); }
#define PIN_AFTER(W, ...) do { asm volatile(W ::: "memory"); __builtin_amdgcn_sched_barrier(0); asm volatile("" : __VA_ARGS__ :: "memory"); } while (0)
#define V4(c, g_) (f32x4){c[4 * (g_)], c[4 * (g_) + 1], c[4 * (g_) + 2], c[4 * (g_) + 3]}

constexpr int WR = 65536, QSLOT = 24576;
constexpr int GSLOT = 20480;
constexpr int REC = 2080, CHUNK = 4 * REC;
constexpr int KVA = WR + 3 * GSLOT, KVB = KVA - 4 * CHUNK;

struct F4 { bf16x8 x0, x1, w0, w1; };
struct QFrag { bf16x8 x, q, k, v; };

template <int G>
__device__ __forceinline__ void qkv_steps(const lptr (&pa)[4], lptr pw, lptr pw2, QFrag& c, QFrag& n, f32x16& aq, f32x16& ak, f32x16& av,
                                          char* fx, u64 tag, int lane, bool shares) {
  if constexpr (G < 32) {
    constexpr int s = G >> 2, N = G + 1, ns = N >> 2;
    if constexpr ((G & 3) == 3) {
      if constexpr (s == 0) { if (!shares) wait_vm<7>(); }
      if constexpr (s == 0) wait_bar<9>(); else if constexpr (s == 1) wait_bar<4>(); else if constexpr (s == 2) wait_bar<1>(); else BAR();
      if constexpr (s == 1) { if (lane == 0) set_flag(fx, tag); }
    }
    const lptr w = ((ns & 2) ? pw2 : pw) + (ns & 1) * QSLOT + (N & 3) * 6144;
    MM(aq, c.q, c.x);
    if constexpr (N < 32) { n.x = lds_frag(pa[N & 3] + ns * 4096); n.q = lds_frag_tr(w, w + 256); }
    MM(ak, c.k, c.x);
    if constexpr (N < 32) { n.k = lds_frag_tr(w + 1024, w + 1024 + 256); n.v = lds_frag_tr(w + 2048, w + 2048 + 256); }
    MM(av, c.x, c.v);
    qkv_steps<G + 1>(pa, pw, pw2, n, c, aq, ak, av, fx, tag, lane, shares);
  }
}

#define EXP(x) "v_exp_f32 v" #x ", v" #x "\n\t"
#define CVT(d, a, b) "v_cvt_pk_bf16_f32 v" #d ", v" #a ", v" #b "\n\t"
#define MF(d, a, b, c) "v_mfma_f32_32x32x16_bf16 " d ", " a ", " b ", " c "\n\t"
#define HALF(c0, c1, c2, c3, c4, c5, c6, c7, c8, c9, c10, c11, c12, c13, c14, c15, QK, PV1, PV0, X0, X1) \
  QK EXP(c0) EXP(c1) EXP(c2) EXP(c3) PV1 X0 \
  EXP(c4) EXP(c5) CVT(80, c0, c1) CVT(81, c2, c3) EXP(c6) EXP(c7) EXP(c8) EXP(c9) CVT(82, c4, c5) CVT(83, c6, c7) \
  EXP(c10) EXP(c11) PV0 X1 \
  EXP(c12) EXP(c13) CVT(84, c8, c9) CVT(85, c10, c11) EXP(c14) EXP(c15) CVT(86, c12, c13) CVT(87, c14, c15)
#define TILE(off, S, X0, X1, X2, X3) \
  S \
  HALF(16, 17, 18, 19, 20, 21, 22, 23, 24, 25, 26, 27, 28, 29, 30, 31, \
       MF("v[32:47]", "v[68:71]", "v[92:95]", "v[96:111]") "ds_read_b128 v[68:71], v88 offset:" off "\n\t", \
       MF("v[112:127]", "v[76:79]", "v[84:87]", "v[112:127]") "ds_read_b128 v[76:79], v90 offset:" off "\n\t", \
       "s_waitcnt lgkmcnt(2)\n\t" MF("v[48:63]", "v[72:75]", "v[80:83]", "v[48:63]"), X0, X1) \
  HALF(32, 33, 34, 35, 36, 37, 38, 39, 40, 41, 42, 43, 44, 45, 46, 47, \
       "s_waitcnt lgkmcnt(0)\n\t" MF("v[16:31]", "v[68:71]", "v[64:67]", "v[0:15]"), \
       MF("v[48:63]", "v[76:79]", "v[84:87]", "v[48:63]"), \
       MF("v[112:127]", "v[72:75]", "v[80:83]", "v[112:127]") "ds_read_b128 v[72:75], v89 offset:" off "\n\t", X2, X3)
#define T_NONE(off) TILE(off, "", "", "", "", "")
#define T_KV(off, m, so) TILE(off, "s_mov_b32 m0, %[" #m "]\n\t", "", "buffer_load_dwordx4 %[voff], %[rkv], %[" #so "] offen sc1 lds\n\t", \
    "buffer_load_dwordx4 %[voff], %[rkv], %[" #so "] offen offset:1024 sc1 lds\n\t", "")
#define T_WD(off, st) TILE(off, "s_add_u32 m0, %[dm0], 16384*" #st "\n\ts_add_u32 %[t0], %[ds0], 131072*" #st "\n\t", "", \
    "buffer_load_dwordx4 %[vw1], %[rwd], %[t0] offen lds\n\ts_add_u32 %[t0], %[t0], 7168\n\t", "buffer_load_dwordx4 %[vw1], %[rwd], %[t0] offen offset:1024 lds\n\t", "")
#define T_KVI(off, MO, SO) TILE(off, "s_add_u32 m0, %[kvm], " #MO "\n\ts_add_u32 %[t0], %[kvs], " #SO "\n\t", "", \
    "buffer_load_dwordx4 %[voff], %[rkv], %[t0] offen sc1 lds\n\t", "buffer_load_dwordx4 %[voff], %[rkv], %[t0] offen offset:1024 sc1 lds\n\t", "")
#define VB(n) "s_waitcnt vmcnt(" #n ")\n\ts_barrier\n\t"
#define SB_ "s_barrier\n\t"
#define HLA(d) "buffer_load_dwordx4 %[" #d "], %[voff], %[rh], %[hs] offen sc1\n\t"
#define HLB(d) "buffer_load_dwordx4 %[" #d "], %[voff], %[rh], %[hs] offen offset:1024 sc1\n\ts_add_u32 %[hs], %[hs], 4096\n\t"
#define T_H2(off, da, db, dc, dd) TILE(off, "", HLA(da), HLB(db), HLA(dc), HLB(dd))
#define ATT_BEGIN "s_mov_b32 %[keep], m0\n\ts_waitcnt lgkmcnt(0)\n\ts_nop 2\n\t"
#define ATT_END "s_mov_b32 m0, %[keep]\n\ts_waitcnt lgkmcnt(0)\n\ts_nop 3\n\t"
#define ATT_STATE "+{v[48:63]}"(OA), "+{v[112:127]}"(OB), "+{v[16:31]}"(SA), "+{v[68:71]}"(K), "+{v[72:75]}"(V0), "+{v[76:79]}"(V1), \
                  "+{v[84:87]}"(P1), [t0] "=&s"(t0), [keep] "=&s"(keep)
#define ATT_CONST "{v[0:15]}"(CA), "{v[96:111]}"(CB), "{v[64:67]}"(QA), "{v[92:95]}"(QB), [voff] "v"(voff)
#define ATT_IN ATT_CONST, "{v88}"(aK), "{v89}"(aV0), "{v90}"(aV1)
#define ATT_CLOB "v32", "v33", "v34", "v35", "v36", "v37", "v38", "v39", "v40", "v41", "v42", "v43", "v44", "v45", "v46", "v47", \
                 "v80", "v81", "v82", "v83", "scc", "memory"

struct GFrag { bf16x8 x[4], g, u; };
__host__ __device__ constexpr int gslot(int s) { return s < 8 ? (s & 3) : (s - 8) % 3; }
template <int S, int KG>
__device__ __forceinline__ void gu_read(const lptr (&pa)[4], const lptr (&pp)[4], lptr pg, lptr pg3, GFrag& n, int part) {
  const lptr x = pa[(S & 1) * 2 + KG] + (S >> 1) * 4096, y = pp[(S & 1) * 2 + KG] + gslot(S) * GSLOT, w = (gslot(S) == 3 ? pg3 : pg + gslot(S) * GSLOT) + KG * 4096;
  if (part == 0) { n.x[0] = lds_frag(x); n.g = lds_frag_tr(w, w + 256); }
  if (part == 1) { n.x[1] = lds_frag(x + 32768); n.u = lds_frag_tr(w + 8192, w + 8192 + 256); }
  if (part == 2) { n.x[2] = lds_frag(y); n.x[3] = lds_frag(y + 2048); }
}
template <int S, class FILL>
__device__ __forceinline__ void gu_stages(const lptr (&pa)[4], const lptr (&pp)[4], lptr pg, lptr pg3, GFrag& f0, GFrag& f1, f32x16 (&g)[4], f32x16 (&u)[4], FILL fill) {
  if constexpr (S < 16) {
#define GU8(c, R0, R1, R2, D) do { MM(g[0], c.g, c.x[0]); R0; MM(g[1], c.g, c.x[1]); R1; MM(u[0], c.u, c.x[0]); R2; MM(u[1], c.u, c.x[1]); \
      MM(g[2], c.g, c.x[2]); D; MM(u[2], c.u, c.x[2]); MM(g[3], c.g, c.x[3]); MM(u[3], c.u, c.x[3]); } while (0)
    GU8(f0, (gu_read<S, 1>(pa, pp, pg, pg3, f1, 0)), (gu_read<S, 1>(pa, pp, pg, pg3, f1, 1)), (gu_read<S, 1>(pa, pp, pg, pg3, f1, 2)), );
    if constexpr (S < 15) {
      if constexpr (S == 0) wait_bar<6>(); else if constexpr (S == 2 || S == 14) wait_bar<0>(); else wait_bar<5>();
      GU8(f1, (gu_read<S + 1, 0>(pa, pp, pg, pg3, f0, 0)), (gu_read<S + 1, 0>(pa, pp, pg, pg3, f0, 1)), (gu_read<S + 1, 0>(pa, pp, pg, pg3, f0, 2)),
          if (S >= 8 && S <= 12) fill(S + 3));
    } else {
      GU8(f1, , , , );
    }
    gu_stages<S + 1>(pa, pp, pg, pg3, f0, f1, g, u, fill);
  }
}

__global__ __launch_bounds__(512, 2) void k_all(const char* __restrict__ gx, const char* __restrict__ gwqkv, const char* __restrict__ gwo,
                                                const char* __restrict__ gwg, const char* __restrict__ gwu, const char* __restrict__ gwd,
                                                char* __restrict__ ws, char* __restrict__ out) {
  extern __shared__ __attribute__((aligned(16))) char smem[];
  const lptr lds = (lptr)smem;
  const unsigned lds0 = (unsigned)(uintptr_t)lds;
  const int b = blockIdx.x;
  unsigned cpf;
  {
    u64 pc; unsigned o = ((b >> 3) * 16 + (threadIdx.x & 15)) * 128;
    asm volatile("s_getpc_b64 %1\nLk_here:\n\tv_min_u32 %2, Lk_tail-Lk_here-4, %2\n\tglobal_load_dword %0, %2, %1" : "=v"(cpf), "=&s"(pc), "+v"(o) :: "memory");
  }
  __builtin_amdgcn_sched_barrier(0);
  const int lane = threadIdx.x & 63, wid = RFL(threadIdx.x >> 6);
  const int xcd = b & 7, bi = b >> 3;
  const int rb = (xcd >> 1) * 8 + (bi & 7);
  const int rot = (bi + 2 * (bi >> 3)) & 7;
  const rsrc_t rx = mk_rsrc(gx, S_ * D_ * 2);
  u32x4 xr[8];
  {
    const unsigned vx = (threadIdx.x >> 3) * 1024 + (threadIdx.x & 7) * 16, sx = (unsigned)rb * 65536 + rot * 128;
#pragma unroll
    for (int i = 0; i < 8; ++i) BLOAD(xr[i], vx, rx, sx ^ (i * 128));
  }
  __builtin_amdgcn_sched_barrier(0);
  const int cb = (xcd & 1) * 4 + (bi >> 3);
  const int cq = (cb + 4 - (int)((uintptr_t)gwqkv >> 7)) & 7, co = (cb + 4 - (int)((uintptr_t)gwd >> 7)) & 7;
  const int head = xcd * 4 + (bi >> 3);
  const int r = lane & 31, h = lane >> 5;
  const unsigned trn = ((lane & 15) >> 2) * 64 + ((lane >> 4) & 1) * 32 + (lane & 3) * 8;
  const unsigned voff = lane * 16;

  const rsrc_t rw = mk_rsrc(gwqkv, D_ * 3 * D_ * 2), rxn = mk_rsrc(ws + WS_XN, 2097152);
  BAR();
  const unsigned vw3 = ((lane >> 2) & 7) * 3072 + (lane >> 5) * 64 + (lane & 3) * 16;
  auto issue_wk = [&](int s, int kb) {
    const unsigned so = (unsigned)(kb * 8) * 3072 + (s ^ rot) * (64 * 3072);
    dma16x3(rw, vw3, so + cq * 128, lds0 + WR + (s & 3) * QSLOT + kb * 3072);
  };
  auto issue_w = [&](int s) { issue_wk(s, wid); };
  issue_w(0); issue_w(1);
  unsigned pf;
#define TOUCH() asm volatile("global_load_dword %0, %1, off" : "=v"(pf) : "v"((h ? gwu : gwg) + (size_t)((b * 8 + wid) * 7 + (r < 7 ? r : 0)) * 128) : "memory")
  const u64 tag = read_tag(ws);
  float rs, mr;
  {
    const unsigned ONE2 = 0x3f803f80u;
    float s0 = 0.f, s1 = 0.f, q0 = 0.f, q1 = 0.f;
#define STAT(i) do { wait_vm1<13 - (i)>(xr[i]); \
      s0 = dot2(xr[i][0], ONE2, s0); q0 = dot2(xr[i][0], xr[i][0], q0); s1 = dot2(xr[i][1], ONE2, s1); q1 = dot2(xr[i][1], xr[i][1], q1); \
      s0 = dot2(xr[i][2], ONE2, s0); q0 = dot2(xr[i][2], xr[i][2], q0); s1 = dot2(xr[i][3], ONE2, s1); q1 = dot2(xr[i][3], xr[i][3], q1); \
      asm volatile("" : "+v"(s0), "+v"(s1), "+v"(q0), "+v"(q1)); } while (0)
    STAT(0); STAT(1); STAT(2); STAT(3); STAT(4); STAT(5); STAT(6); STAT(7);
    const float mean = sum8(s0 + s1) * (1.0f / 512.0f);
    rs = __builtin_amdgcn_rsqf(__builtin_fmaxf(sum8(q0 + q1) * (1.0f / 512.0f) - mean * mean, 0.0f) + 1e-5f);
    mr = -mean * rs;
  }
  const unsigned xo = (lane & 7) * 512 + ((((wid & 3) * 8 + (lane >> 3)) ^ (lane & 7)) * 16);
  const lptr pxw = lds + (wid >> 2) * 32768 + xo;
#pragma unroll
  for (int j = 0; j < 8; ++j) {
    u32x4 n;
#pragma unroll
    for (int i = 0; i < 4; ++i) n[i] = pk2(__builtin_fmaf(bf_lo(xr[j][i]), rs, mr), __builtin_fmaf(bf_hi(xr[j][i]), rs, mr));
    if (j < 2) BSTORE_DEV(n, rxn, (cb < 4 ? 0u : OOB) + (unsigned)(rb * 8 + (rot ^ j)) * 8192 + (wid >> 2) * 4096 + xo);
    *(LAS u32x4*)(pxw + j * 4096) = n;
  }
  issue_w(2); issue_w(3);
  TOUCH();
  const bool has_ffn = cb < 7;
  const int fb = cb * 2 + (rb & 1), w4 = wid & 3;
  const rsrc_t rg = mk_rsrc(gwg - 1024, D_ * F_ * 2 + 1024), ru = mk_rsrc(gwu - 1024, D_ * F_ * 2 + 1024);
  const unsigned vwf = ((lane >> 2) & 7) * (F_ * 2) + (lane >> 5) * 64 + (lane & 3) * 16;
  auto gu_w = [&](int s) {
    const unsigned so = (has_ffn ? 0u : OOB) + (unsigned)(w4 * 8) * (F_ * 2) + fb * 256 + (unsigned)(s ^ (2 * rot)) * (32 * F_ * 2) + 1024;
    const unsigned d = lds0 + WR + gslot(s) * GSLOT + w4 * 2048;
    dma16x2(rg, vwf, so, so + 128, d); dma16x2(ru, vwf, so, so + 128, d + 8192);
  };
  auto gu_x = [&](int s) {
    dma16_dev(rxn, voff, (has_ffn ? 0u : OOB) + (unsigned)((rb ^ 1) * 8 + ((s >> 1) ^ rot)) * 8192 + (w4 >> 1) * 4096 + (s & 1) * 2048 + (w4 & 1) * 1024,
              lds0 + WR + gslot(s) * GSLOT + 16384 + w4 * 1024);
  };
  auto gu_fill = [&](int s) { gu_w(s); gu_x(s); };
  u64 fl;
  lptr pa[4];
#pragma unroll
  for (int g = 0; g < 4; ++g) pa[g] = lds + (2 * g + h) * 512 + ((r ^ (2 * g + h)) * 16);

  const rsrc_t rkv = mk_rsrc(ws + WS_KV + (size_t)head * 131072, 131072), rh = mk_rsrc(ws + WS_H, 7340032);
  const unsigned vvo = h * 256 + (r & 15) * 16;
  const bf16x8 ONES = {0x3f80, 0x3f80, 0x3f80, 0x3f80, 0x3f80, 0x3f80, 0x3f80, 0x3f80};
  const bf16x8 PAD = ((r & 27) == 16) ? ONES : (bf16x8){};
  const int qt0 = (bi & 7) * 8 + w4 * 2;
  const unsigned ring = lds0 + (wid < 4 ? KVA : KVB), kt0 = wid < 4 ? 0 : 32;
  const unsigned kvm = ring + w4 * REC, kvs = (kt0 + w4) * 2048;
  u32x4 QAu, QBu, Ku, V0u;
  auto att_loads = [&] {
    const rsrc_t rq = mk_rsrc(ws + WS_Q, 2097152);
    BLOAD_DEV(QAu, voff, rq, (unsigned)(head * 64 + qt0) * 1024);
    BLOAD_DEV(QBu, voff, rq, (unsigned)(head * 64 + qt0 + 1) * 1024);
    BLOAD_DEV(Ku, voff, rkv, ((kt0 + 63) & 63) * 2048);
    BLOAD_DEV(V0u, vvo, rkv, ((kt0 + 63) & 63) * 2048 + 1024);
  };
  auto att_ring = [&] {
    dma16_dev(rkv, voff, kvs, kvm); dma16_dev(rkv, voff, kvs + 1024, kvm + 1024);
    dma16_dev(rkv, voff, kvs + 8192, kvm + CHUNK); dma16_dev(rkv, voff, kvs + 8192 + 1024, kvm + CHUNK + 1024);
    *(LAS bf16x8*)(lds + (ring - lds0) + (w4 * 4 + (r & 3)) * REC + 2048 + h * 16) = h ? (bf16x8){} : ONES;
  };
  LAS float* const mx = (LAS float*)(lds + KVA + 4 * CHUNK + w4 * 512) + lane;
  bf16x8 QA, QB, K, V0, V1, P1;
  f32x16 SA, CA, CB, OA, OB;
#define MV2(d, s) "v_mov_b64 v[" #d ":" #d "+1], " s "\n\t"
#define MV16(d, s) MV2(d, s) MV2(d + 2, s) MV2(d + 4, s) MV2(d + 6, s) MV2(d + 8, s) MV2(d + 10, s) MV2(d + 12, s) MV2(d + 14, s)
  auto att_init = [&](float ma, float mb) {
    QA = __builtin_bit_cast(bf16x8, QAu); QB = __builtin_bit_cast(bf16x8, QBu); K = __builtin_bit_cast(bf16x8, Ku);
    V0 = (r >= 16) ? PAD : __builtin_bit_cast(bf16x8, V0u);
    asm volatile(MV16(0, "%[ma]") MV2(76, "0") MV2(78, "0") MF("v[16:31]", "v[68:71]", "v[64:67]", "v[0:15]")
                 MV16(96, "%[mb]") MV16(48, "0") MV16(112, "0") MV2(84, "0") MV2(86, "0")
                 : "=&{v[0:15]}"(CA), "=&{v[96:111]}"(CB), "=&{v[16:31]}"(SA), "=&{v[48:63]}"(OA), "=&{v[112:127]}"(OB), "=&{v[76:79]}"(V1), "=&{v[84:87]}"(P1)
                 : "{v[68:71]}"(K), "{v[64:67]}"(QA), [ma] "v"((f32x2){ma, ma}), [mb] "v"((f32x2){mb, mb}));
  };
  const unsigned apad = ((r & 27) == 16) ? 2048 : 2064;
  const unsigned aK = ring + voff, aV0 = ring + (r < 16 ? 1024 + vvo : apad), aV1 = ring + (r < 16 ? 1536 + vvo : apad);
  unsigned t0, keep;
  const int rt = wid & 1, kq = wid >> 1;
  const rsrc_t rwd = mk_rsrc(gwd, F_ * D_ * 2), rwo = mk_rsrc(gwo, D_ * D_ * 2), ro = mk_rsrc(ws + WS_O, 2097152);
  const unsigned vw1 = ((lane >> 2) & 7) * 1024 + (lane >> 5) * 64 + (lane & 3) * 16;
#define DADDR(st) ((st) < 5 ? (st) * 16384 : (st) == 5 || (st) == 11 ? KVA : (st) < 11 ? ((st) - 6) * 16384 : (st) < 14 ? ((st) - 12) * 16384 : \
                   (st) == 14 ? 81920 : (st) == 15 ? 147456 : (st) == 16 ? 98304 : 32768)
  auto d_so = [&](int st, int pc) { return (unsigned)((st < 14 ? st : st - 14) * 128 + pc * 16) * 1024 + co * 128; };
  auto d_m = [&](int st, int pc) { return lds0 + DADDR(st) + pc * 2048; };
  auto issue_dp = [&](int st, int pc) { dma16x2(st < 14 ? rwd : rwo, vw1, d_so(st, pc), d_so(st, pc) + 8192, d_m(st, pc)); };
  auto h_so = [&](int e) { return (unsigned)((rb * 2 + rt) * 112 + e * 4 + kq * 2) * 1024; };
  const unsigned kvd = kvs + (wid < 4 ? 4 * 8192 : 0);
  u32x4 ha[14], hb[14], xv;
  const unsigned kve = wid < 4 ? OOB : kvd;
  LAS f32x4* ex = (LAS f32x4*)(lds + 147456 + w4 * 4096) + lane;
  LAS float* el = (LAS float*)(lds + 145408 + w4 * 512) + lane;
  if (cb >= 4) wait_vm<10>();
  WAIT_BAR(12);

  if (wid < 4) {
    {
      const int ch = wid >> 1;
      lptr par[4];
#pragma unroll
      for (int g = 0; g < 4; ++g) par[g] = pa[g] + rt * 32768;
      lptr pw = lds + WR + h * 3072 + ch * 512 + trn, pw2 = pw + 2 * QSLOT;
      asm volatile("" : "+v"(pw2));
      const int pair = cq * 2 + ch, tile = rb * 2 + rt;
      f32x16 aq = {}, ak = {}, av = {};
      QFrag f0, f1;
      f0.x = lds_frag(par[0]); f0.q = lds_frag_tr(pw, pw + 256); f0.k = lds_frag_tr(pw + 1024, pw + 1024 + 256); f0.v = lds_frag_tr(pw + 2048, pw + 2048 + 256);
      qkv_steps<0>(par, pw, pw2, f0, f1, aq, ak, av, ws + WS_FX + (rb * 8 + cb) * 8, tag, threadIdx.x, cb < 4);
#pragma unroll
      for (int i = 0; i < 16; ++i) aq[i] *= QSCALE;
      u32x4 qs[6] = {pack8(ak, 0), pack8(ak, 8), pack8(av, 0), pack8(av, 8), pack8(aq, 0), pack8(aq, 8)};
      asm volatile("" : "+v"(qs[0]), "+v"(qs[1]), "+v"(qs[2]), "+v"(qs[3]), "+v"(qs[4]), "+v"(qs[5]));
      const rsrc_t rq = mk_rsrc(ws + WS_Q, 2097152 + 4194304);
      const unsigned ko = 2097152 + (unsigned)((2 * pair) * 64 + ((tile + 63) & 63)) * 2048 + voff;
      const unsigned vo = 2097152 + (unsigned)(2 * pair + (r >> 4)) * 131072 + vvo;
      const unsigned qo = (unsigned)((2 * pair) * 64 + tile) * 1024 + voff;
      auto qst = [&](int i) {
        BSTORE_DEV(qs[i], rq, i == 0 ? ko : i == 1 ? ko + 131072 : i == 2 ? vo + ((tile + 63) & 63) * 2048 + 1024 : i == 3 ? vo + tile * 2048 + 1536 : i == 4 ? qo : qo + 65536);
      };
      auto fq_set = [&] {
        __builtin_amdgcn_raw_buffer_store_b64(__builtin_bit_cast(u32x2, tag), mk_rsrc(ws + WS_FQ, 8192),
                                              lane == 0 ? (unsigned)((cq * 2 + (wid >> 1)) * 64 + rb * 2 + (wid & 1)) * 8 : OOB, 0, 16);
      };
      if (__builtin_expect(has_ffn, 1)) {
        BAR();
        qst(0); qst(1); qst(2);
        BAR(); qst(3); qst(4); qst(5); gu_fill(4);
        BAR(); gu_fill(5);
        BAR(); gu_fill(6);
        WAIT_BAR(10); fq_set(); gu_fill(7);
        WAIT_BAR(10); gu_fill(8);
        WAIT_BAR(10); gu_fill(9);
        WAIT_BAR(10); gu_fill(10);
      } else {
        qst(0); qst(1); qst(2); qst(3); qst(4); qst(5);
        wait_vm<0>();
        fq_set();
#pragma unroll 1
        for (int i = 0; i < 8; ++i) BAR();
      }
    }
    if (!has_ffn) __builtin_amdgcn_s_sleep(32);
    asm volatile("global_load_dwordx2 %0, %1, off sc1" : "=v"(fl) : "v"(ws + WS_FQ + ((head >> 1) * 64 + lane) * 8) : "memory");
    WAIT_BAR(11);
    PIN_AFTER("s_waitcnt vmcnt(0)", "+v"(fl));
    if (__builtin_expect(!__all(fl == tag), 0)) wait_flags(ws + WS_FQ + (head >> 1) * 512, 64, tag, lane);
    u32x4 K1u;
    att_loads();
    BLOAD_DEV(K1u, voff, rkv, 0u);
    BAR();
    att_ring();
    PIN_AFTER("s_waitcnt vmcnt(4)", "+v"(QAu), "+v"(QBu), "+v"(Ku), "+v"(V0u), "+v"(K1u));
    float ma, mb;
    {
      const f32x16 z = {};
      const bf16x8 k0 = __builtin_bit_cast(bf16x8, Ku), k1 = __builtin_bit_cast(bf16x8, K1u), qa = __builtin_bit_cast(bf16x8, QAu), qb = __builtin_bit_cast(bf16x8, QBu);
      const f32x16 a0 = MFMA(k0, qa, z), a1 = MFMA(k1, qa, z), b0 = MFMA(k0, qb, z), b1 = MFMA(k1, qb, z);
      ma = a0[0]; mb = b0[0];
#pragma unroll
      for (int i = 0; i < 16; ++i) { ma = __builtin_fmaxf(__builtin_fmaxf(ma, a0[i]), a1[i]); mb = __builtin_fmaxf(__builtin_fmaxf(mb, b0[i]), b1[i]); }
      asm volatile("" : "+v"(ma), "+v"(mb));
    }
    BAR();
    ma = -(xhalf_max(ma) + MBIAS); mb = -(xhalf_max(mb) + MBIAS);
    mx[0] = ma; mx[64] = mb;
    att_init(ma, mb);
    asm volatile(ATT_BEGIN "s_setprio 2\n\t"
                 VB(2) T_KVI("0", 16640, 16384) T_NONE("2080") SB_ T_NONE("4160") T_NONE("6240")
                 VB(2) T_KVI("8320", 24960, 24576) T_NONE("10400") SB_ T_NONE("12480") T_NONE("14560")
                 VB(2) T_KVI("16640", 0, 32768) T_NONE("18720") SB_ T_NONE("20800") T_NONE("22880")
                 VB(2) T_KVI("24960", 8320, 40960) T_NONE("27040") SB_ T_NONE("29120") T_NONE("31200")
                 ATT_END
                 : ATT_STATE : ATT_IN, [rkv] "s"(rkv), [kvm] "s"(kvm), [kvs] "s"(kvs) : ATT_CLOB);
    __builtin_amdgcn_s_setprio(0);
    wait_vm<2>();
  } else {
    auto w2 = [&](int s) { issue_wk(s, w4 * 2); issue_wk(s, w4 * 2 + 1); };
    if (cb >= 4) wait_vm<7>();
    WAIT_BAR(9);  w2(4);
    WAIT_BAR(10); w2(5);
    WAIT_BAR(13); w2(6);
    WAIT_BAR(12); w2(7);
    WAIT_BAR(12);
    asm volatile("global_load_dwordx2 %0, %1, off sc1" : "=v"(fl) : "v"(ws + WS_FX + ((rb ^ 1) * 8 + (lane & 3)) * 8) : "memory");
    gu_w(0);
    if (!has_ffn) wait_vm<7>();
    WAIT_BAR(11); gu_w(1);
    if (!has_ffn) wait_vm<0>();
    PIN_AFTER("s_waitcnt vmcnt(8) lgkmcnt(0)\n\ts_barrier", "+v"(fl));
    const bool xok = __all(fl == tag) || !has_ffn;
    if (xok) gu_x(0);
    gu_w(2);
    if (xok) { gu_x(1); gu_x(2); }
    BAR();
    if (__builtin_expect(xok, 1)) { gu_fill(3); wait_vm<11>(); }
    else { wait_flags(ws + WS_FX + (rb ^ 1) * 64, 4, tag, lane); gu_x(0); gu_x(1); gu_x(2); gu_fill(3); wait_vm<7>(); }
    BAR();
    f32x16 g[4], u[4];
    if (has_ffn) {
      lptr pp[4];
#pragma unroll
      for (int i = 0; i < 4; ++i) { pp[i] = pa[i] + (WR + 16384 - (i >> 1) * 2048); asm volatile("" : "+v"(pp[i])); g[i] = (f32x16){}; u[i] = (f32x16){}; }
      const lptr pg = lds + WR + h * 2048 + (w4 >> 1) * 1024 + (w4 & 1) * 512 + trn;
      lptr pg3 = pg + 3 * GSLOT;
      asm volatile("" : "+v"(pg3));
      GFrag f0, f1;
      gu_read<0, 0>(pa, pp, pg, pg3, f0, 0); gu_read<0, 0>(pa, pp, pg, pg3, f0, 1); gu_read<0, 0>(pa, pp, pg, pg3, f0, 2);
      gu_stages<0>(pa, pp, pg, pg3, f0, f1, g, u, gu_fill);
    } else {
      unsigned d[5];
      wait_vm<0>();
#pragma unroll 1
      for (int i = 0; i < 11; ++i) BAR();
#pragma unroll
      for (int i = 0; i < 5; ++i) {
        const int t = (rb * 4 + w4) * 144 + i * 32 + (r + i * 32 < 144 ? r : 0);
        const char* p = (t < 14336) ? gwd + (size_t)t * 128 : gwo + (size_t)(t - 14336) * 128;
        asm volatile("global_load_dword %0, %1, off" : "=v"(d[i]) : "v"(p) : "memory");
      }
#pragma unroll 1
      for (int i = 11; i < 15; ++i) BAR();
      PIN_AFTER("s_waitcnt vmcnt(0)", "+v"(d[0]), "+v"(d[1]), "+v"(d[2]), "+v"(d[3]), "+v"(d[4]));
    }
    BAR();
    __builtin_amdgcn_s_setprio(3);
    att_loads(); att_ring();
    auto swiglu = [&](int i) {
#pragma unroll
      for (int j = 0; j < 16; ++j) g[i][j] = g[i][j] * u[i][j] * __builtin_amdgcn_rcpf(1.0f + __builtin_amdgcn_exp2f(g[i][j] * -1.4426950408889634f));
      const unsigned ho = (unsigned)(((rb ^ (i >> 1)) * 2 + (i & 1)) * 112 + fb * 8 + w4 * 2) * 1024 + voff;
      BSTORE_DEV(pack8(g[i], 0), rh, ho);
      BSTORE_DEV(pack8(g[i], 8), rh, ho + 1024);
    };
    if (has_ffn) swiglu(0);
    BAR();
    if (has_ffn) { swiglu(1); swiglu(2); }
    BAR();
    const float ma = mx[0], mb = mx[64];
    if (has_ffn) swiglu(3); else wait_vm<0>();
    PIN_AFTER("s_waitcnt vmcnt(8)", "+v"(QAu), "+v"(QBu), "+v"(Ku), "+v"(V0u));
    att_init(ma, mb);
  }
  asm volatile("" :: "v"(pf), "v"(cpf));
  asm volatile(ATT_BEGIN "s_setprio 0\n\t" SB_
               T_KVI("0", 16640, 16384) T_WD("2080", 0) T_WD("4160", 1) T_WD("6240", 2)
               VB(8)
               "buffer_store_dwordx2 %[tag], %[fho], %[rfl], 0 offen sc1\n\t"
               T_KVI("8320", 24960, 24576) T_WD("10400", 3) T_WD("12480", 4) T_NONE("14560")
               VB(7)
               T_KV("16640", kvm, s5_) T_NONE("18720")
               TILE("20800", "", "buffer_load_dwordx2 %[fl], %[fhi], %[rfl], 0 offen sc1\n\t", "", "", "") T_NONE("22880")
               ATT_END
               : ATT_STATE, [fl] "=&v"(fl)
               : ATT_IN, [rkv] "s"(rkv), [rwd] "s"(rwd), [vw1] "v"(vw1), [kvm] "s"(kvm), [kvs] "s"(kvd),
                 [dm0] "s"(RFL(d_m(0, wid))), [ds0] "s"(RFL(d_so(0, wid))), [s5_] "s"(kve + 4 * 8192), [rfl] "s"(mk_rsrc(ws + WS_FH, 2048)), [tag] "v"(tag),
                 [fho] "v"(threadIdx.x == 0 ? (unsigned)(rb * 8 + cb) * 8 : OOB), [fhi] "v"((unsigned)((rb >> 1) * 16 + (lane & 15)) * 8) : ATT_CLOB);
  PIN_AFTER("s_waitcnt vmcnt(0)", "+v"(fl));
  if (__builtin_expect(!__all(fl == tag), 0)) wait_flags(ws + WS_FH + (rb >> 1) * 128, 16, tag, lane);
#define H_OUT(n, st) [a##n] "=&v"(ha[st]), [b##n] "=&v"(hb[st])
  unsigned hs = h_so(0) + (wid >> 2) * OOB;
  f32x4 p0, p1, p2, p3;
  float pl0, pl1;
  asm volatile(ATT_BEGIN SB_
               TILE("24960", "s_add_u32 m0, %[kvm], 8320\n\ts_add_u32 %[t0], %[kvs], 40960\n\t", "buffer_load_dwordx4 %[voff], %[rkv], %[t0] offen sc1 lds\n\t",
                    "buffer_load_dwordx4 %[voff], %[rkv], %[t0] offen offset:1024 sc1 lds\n\t", HLA(a0), HLB(b0))
               T_H2("27040", a1, b1, a2, b2) T_H2("29120", a3, b3, a4, b4) T_H2("31200", a5, b5, a6, b6)
               "s_cmp_lt_u32 %[wid], 4\n\ts_cbranch_scc1 L_skip_%=\n\t"
               "s_setprio 2\n\t"
               SB_ T_KVI("0", 16640, 49152) T_NONE("2080") SB_ T_NONE("4160") T_NONE("6240")
               VB(2) T_KVI("8320", 24960, 57344)
               TILE("10400", "", "", "", "ds_read_b128 %[p0], %[ex]\n\tds_read_b128 %[p1], %[ex] offset:1024\n\tds_read_b32 %[pl0], %[el]\n\t", "")
               SB_
               TILE("12480", "", "", "", "ds_read_b128 %[p2], %[ex] offset:2048\n\tds_read_b128 %[p3], %[ex] offset:3072\n\tds_read_b32 %[pl1], %[el] offset:256\n\t", "")
               T_NONE("14560")
               VB(2) T_NONE("16640") T_NONE("18720") SB_ T_NONE("20800") T_NONE("22880")
               VB(0) T_NONE("24960") T_NONE("27040") SB_ T_NONE("29120") T_NONE("31200")
               "L_skip_%=:\n\t"
               ATT_END
               : ATT_STATE, H_OUT(0, 0), H_OUT(1, 1), H_OUT(2, 2), H_OUT(3, 3), H_OUT(4, 4), H_OUT(5, 5), H_OUT(6, 6),
                 [p0] "=&v"(p0), [p1] "=&v"(p1), [p2] "=&v"(p2), [p3] "=&v"(p3), [pl0] "=&v"(pl0), [pl1] "=&v"(pl1), [hs] "+s"(hs)
               : ATT_IN, [rkv] "s"(rkv), [kvm] "s"(kvm), [kvs] "s"(kve), [wid] "s"(wid), [ex] "v"(ex), [el] "v"(el),
                 [rh] "s"(rh) : ATT_CLOB);

  const int t = threadIdx.x;
  const unsigned off = (unsigned)(rb * 64 + (t >> 3)) * 1024 + co * 128 + (t & 7) * 16;
  f32x16 d0, d1;
  F4 e0, e1;
#define D_PTR(pd, a) ((a) < 65536 ? pd[0] + (a) : (a) < KVA ? pd[1] + ((a) - 65536) : pd[2] + ((a) - KVA))
#define D_READ_A(n, w_) do { n.w0 = lds_frag_tr(w_, w_ + 1024); n.w1 = lds_frag_tr(w_ + 512, w_ + 1536); } while (0)
#define D_READ_B(n, w_) do { n.x0 = lds_frag_tr(w_ + 2048, w_ + 3072); n.x1 = lds_frag_tr(w_ + 2560, w_ + 3584); } while (0)
#define D_STEPX(c, n, nxt, A, B, RD, X, Y) do { \
    MM(d0, c.w0, __builtin_bit_cast(bf16x8, A)); if (RD) D_READ_A(n, (nxt)); MM(d1, c.w1, __builtin_bit_cast(bf16x8, A)); if (RD) D_READ_B(n, (nxt)); \
    MM(d0, c.x0, __builtin_bit_cast(bf16x8, B)); X; MM(d1, c.x1, __builtin_bit_cast(bf16x8, B)); Y; } while (0)
#define H_LOAD(e, so) do { BLOAD_DEV(ha[e], voff, rh, so); BLOAD_DEV(hb[e], voff, rh, (so) + 1024); } while (0)
  PIN_AFTER("s_waitcnt vmcnt(0)", "+v"(ha[0]), "+v"(ha[1]), "+v"(ha[2]), "+v"(ha[3]), "+v"(ha[4]), "+v"(ha[5]), "+v"(ha[6]),
            "+v"(hb[0]), "+v"(hb[1]), "+v"(hb[2]), "+v"(hb[3]), "+v"(hb[4]), "+v"(hb[5]), "+v"(hb[6]));
  OB = MFMA(V1, P1, OB);
  lptr pe[3];
  pe[0] = lds + (kq & 1) * 4096 + h * 256 + trn; pe[1] = pe[0] + 65536; pe[2] = pe[0] + KVA;
  asm volatile("" : "+v"(pe[0]), "+v"(pe[1]), "+v"(pe[2]));
#define EP(e) D_PTR(pe, DADDR((e) >> 1) + ((e) & 1) * 8192)
#define RADDR(q) ((q) == 0 ? 49152 : (q) == 1 ? 65536 : (q) == 2 ? KVA : 0)
  auto put_partial = [&](int base) {
    const lptr red = lds + base + (rt * 32 + r) * 256;
#pragma unroll
    for (int i = 0; i < 4; ++i) {
      *(LAS f32x4*)(red + (((2 * i + h) ^ (r & 15)) * 16)) = V4(d0, i);
      *(LAS f32x4*)(red + (((8 + 2 * i + h) ^ (r & 15)) * 16)) = V4(d1, i);
    }
  };
  if (wid < 4) {
    d0 = d1 = (f32x16){};
#define E_DMA(st, i) issue_dp(st, wid * 2 + (i))
#define E_STAGE(k, X0, Y0, X1, Y1) do { D_STEPX(e0, e1, EP(2 * (k) + 1), ha[(2 * (k)) % 14], hb[(2 * (k)) % 14], true, X0, Y0); \
      D_STEPX(e1, e0, EP(2 * (k) + 2), ha[(2 * (k) + 1) % 14], hb[(2 * (k) + 1) % 14], (k) < 13, X1, Y1); } while (0)
#define HE(e) H_LOAD((e) % 14, h_so(e))
#define HP(e) "+v"(ha[(e) % 14]), "+v"(hb[(e) % 14])
#define W2(k, sa, sb, e) do { E_STAGE(k, E_DMA(sa, 0), E_DMA(sa, 1), E_DMA(sb, 0), E_DMA(sb, 1)); HE(e); } while (0)
#define W1(k, sa, e) do { E_STAGE(k, E_DMA(sa, 0), E_DMA(sa, 1), HE(e), HE((e) + 1)); HE((e) + 2); } while (0)
#define FB(N, ...) PIN_AFTER("s_waitcnt vmcnt(" #N ") lgkmcnt(0)\n\ts_barrier", __VA_ARGS__)
    D_READ_A(e0, EP(0)); D_READ_B(e0, EP(0));
    E_STAGE(0, HE(7), HE(8), HE(9), ); E_STAGE(1, HE(10), , HE(11), );
    BAR();
    ex[0] = V4(OA, 0); ex[64] = V4(OA, 1); ex[128] = V4(OB, 0); ex[192] = V4(OB, 1); el[0] = OA[8]; el[64] = OB[8];
    W2(2, 5, 6, 12);   FB(18, HP(7));
    W2(3, 7, 8, 13);   FB(16, HP(8), HP(9), HP(10), HP(11));
    W1(4, 9, 14);      WAIT_BAR(22);
    W2(5, 10, 14, 17); FB(20, HP(12), HP(13));
    W2(6, 11, 15, 18); FB(22, HP(14), HP(15));
    W1(7, 12, 19);     FB(20, HP(16), HP(17));
    W1(8, 13, 22);     FB(6, HP(18), HP(19), HP(20), HP(21));
    E_STAGE(9, HE(25), HE(26), HE(27), E_DMA(16, 0));
    E_STAGE(10, E_DMA(16, 1), E_DMA(17, 0), E_DMA(17, 1), );
    PIN_AFTER("s_waitcnt vmcnt(8)", HP(22), HP(23), HP(24), HP(25), HP(26), HP(27));
    E_STAGE(11, , , , ); E_STAGE(12, , , , ); E_STAGE(13, , , , );
    if (b == 193 && wid == 0) {
#pragma unroll 1
      for (int i = 0; i < 4; ++i) wait_flags(ws + WS_FH + i * 512, 64, tag, lane);
      if (lane == 0) set_flag(ws + WS_EPOCH, tag);
    }
  } else {
    auto store_o = [&](const f32x16& O, int i) {
      float il = __builtin_amdgcn_rcpf(O[8]);
      asm volatile("s_nop 0" : "+v"(il));
      const u32x4 v = {pk2(mul1(O[0], il), mul1(O[1], il)), pk2(mul1(O[2], il), mul1(O[3], il)), pk2(mul1(O[4], il), mul1(O[5], il)), pk2(mul1(O[6], il), mul1(O[7], il))};
      BSTORE_DEV(v, ro, (unsigned)((qt0 + i) * 32 + head) * 1024 + voff);
    };
    auto check_o = [&](f32x16 O, const bf16x8& Q, int i) {
      float mag = __builtin_fabsf(O[0]);
#pragma unroll
      for (int j = 1; j < 8; ++j) mag += __builtin_fabsf(O[j]);
      const bool bad = (int)!(O[8] < 0x1p120f) | (int)!(mag < 0x1p126f) | (int)!(xhalf_max(mag) > 0x1p-100f);
      if (__builtin_expect(__any(bad), 0)) {
        const f32x16 z = {};
        float mt = -__builtin_inff();
#pragma unroll 1
        for (int tt = 0; tt < 64; ++tt) {
          const f32x16 s = MFMA(__builtin_bit_cast(bf16x8, __builtin_amdgcn_raw_buffer_load_b128(rkv, voff, tt * 2048, 16)), Q, z);
#pragma unroll
          for (int j = 0; j < 16; ++j) mt = __builtin_fmaxf(mt, s[j]);
        }
        mt = xhalf_max(mt);
        O = z;
#pragma unroll 1
        for (int tt = 0; tt < 64; ++tt) {
          f32x16 s = MFMA(__builtin_bit_cast(bf16x8, __builtin_amdgcn_raw_buffer_load_b128(rkv, voff, ((tt + 63) & 63) * 2048, 16)), Q, z);
          bf16x8 v0 = __builtin_bit_cast(bf16x8, __builtin_amdgcn_raw_buffer_load_b128(rkv, vvo, ((tt + 63) & 63) * 2048 + 1024, 16));
          bf16x8 v1 = __builtin_bit_cast(bf16x8, __builtin_amdgcn_raw_buffer_load_b128(rkv, vvo, tt * 2048 + 1536, 16));
          if (r >= 16) { v0 = PAD; v1 = PAD; }
#pragma unroll
          for (int j = 0; j < 16; ++j) s[j] = __builtin_amdgcn_exp2f(s[j] - mt);
          O = MFMA(v0, __builtin_bit_cast(bf16x8, pack8(s, 0)), O);
          O = MFMA(v1, __builtin_bit_cast(bf16x8, pack8(s, 8)), O);
        }
        asm volatile("s_nop 15" : "+v"(O));
        wait_vm<0>();
        store_o(O, i);
      }
    };
#pragma unroll
    for (int j = 0; j < 4; ++j) { OA[j] = add1(OA[j], p0[j]); OA[4 + j] = add1(OA[4 + j], p1[j]); }
    OA[8] += pl0;
    store_o(OA, 0);
    asm volatile("s_nop 3" : "+v"(OB) :: "memory");
#pragma unroll
    for (int j = 0; j < 4; ++j) { OB[j] = add1(OB[j], p2[j]); OB[4 + j] = add1(OB[4 + j], p3[j]); }
    OB[8] += pl1;
    store_o(OB, 1);
    check_o(OA, QA, 0); check_o(OB, QB, 1);
    wait_vm<0>();
    __builtin_amdgcn_raw_buffer_store_b64(__builtin_bit_cast(u32x2, tag), mk_rsrc(ws + WS_FO, 16384),
                                          lane < 2 ? (unsigned)((qt0 + lane) * 32 + head) * 8 : OOB, 0, 16);
    d0 = d1 = (f32x16){};
    asm volatile("" : "+v"(d0), "+v"(d1));
    __builtin_amdgcn_s_setprio(0);
  }
  if (wid < 4) wait_vm<0>();
  BAR();
  {
    BLOAD(xv, off, rx, 0u);
    const lptr pw = lds + (kq == 0 ? DADDR(14) : kq == 1 ? DADDR(15) : kq == 2 ? DADDR(16) : DADDR(17)) + h * 256 + trn;
    F4 wf[4];
#pragma unroll
    for (int j = 0; j < 4; ++j) { D_READ_A(wf[j], pw + j * 4096); D_READ_B(wf[j], pw + j * 4096); }
    wait_flags(ws + WS_FO + ((rb * 2 + rt) * 32 + kq * 8) * 8, 8, tag, lane);
    PIN_AFTER("s_waitcnt vmcnt(0)", "+v"(xv));
    u32x4 oa[4], ob[4];
    const unsigned obase = (unsigned)((rb * 2 + rt) * 32 + kq * 8) * 1024;
#pragma unroll
    for (int j = 0; j < 4; ++j) { BLOAD_DEV(oa[j], voff, ro, obase + j * 2048); BLOAD_DEV(ob[j], voff, ro, obase + j * 2048 + 1024); }
#define O_STEP(j) do { wait_vm2<6 - 2 * (j)>(oa[j], ob[j]); D_STEPX(wf[j], e0, pw, oa[j], ob[j], false, , ); } while (0)
    O_STEP(0); O_STEP(1); O_STEP(2); O_STEP(3);
    put_partial(kq == 0 ? RADDR(0) : kq == 1 ? RADDR(1) : kq == 2 ? RADDR(2) : RADDR(3));
  }
  BAR();
  f32x4 s0 = {bf_lo(xv[0]), bf_hi(xv[0]), bf_lo(xv[1]), bf_hi(xv[1])}, s1 = {bf_lo(xv[2]), bf_hi(xv[2]), bf_lo(xv[3]), bf_hi(xv[3])};
  {
    const int row = t >> 3, c = (t & 7) * 2;
    const lptr rd0 = lds + row * 256 + ((c ^ (row & 15)) * 16), rd1 = lds + row * 256 + (((c + 1) ^ (row & 15)) * 16);
#pragma unroll
    for (int p = 0; p < 4; ++p) {
      const int o = RADDR(p);
      s0 += *(const LAS f32x4*)(rd0 + o); s1 += *(const LAS f32x4*)(rd1 + o);
    }
  }
  __builtin_amdgcn_raw_buffer_store_b128((u32x4){pk2(s0[0], s0[1]), pk2(s0[2], s0[3]), pk2(s1[0], s1[1]), pk2(s1[2], s1[3])}, mk_rsrc(out, S_ * D_ * 2), off, 0, 0);
}

__attribute__((used, noinline)) __device__ void k_all_end() { asm volatile("Lk_tail:"); }

extern "C" void kernel_launch(void* const* d_in, const int* in_sizes, int n_in, void* d_out, int out_size, void* d_ws,
                              size_t ws_size, hipStream_t stream) {
  (void)hipFuncSetAttribute((const void*)k_all, hipFuncAttributeMaxDynamicSharedMemorySize, LDS_BYTES);
  k_all<<<256, 512, LDS_BYTES, stream>>>((const char*)d_in[0], (const char*)d_in[1], (const char*)d_in[2], (const char*)d_in[4],
                                         (const char*)d_in[5], (const char*)d_in[6], (char*)d_ws, (char*)d_out);
}
```
